```python
import torch

def _softmax(x, axis=-1):
    return torch.softmax(x, dim=axis)
def _gelu(x):
    return torch.nn.functional.gelu(x, approximate="tanh")
def _silu(x):
    return torch.nn.functional.silu(x)
def _sigmoid(x):
    return torch.sigmoid(x)
def _layernorm(x, axis=-1, eps=1e-5):
    m = x.mean(dim=axis, keepdim=True)
    v = x.var(dim=axis, keepdim=True, unbiased=False)
    return (x - m) / torch.sqrt(v + eps)
def _scatter_add(tgt, idx, src, axis=0):
    return tgt.clone().index_add_(axis, idx, src)
def _segment_sum(src, seg, n):
    out = torch.zeros((n,) + src.shape[1:], dtype=src.dtype)
    return out.index_add_(0, seg, src)
def _pad(x, pw):
    flat = tuple(v for pair in reversed(pw) for v in pair)
    return torch.nn.functional.pad(x, flat)

kernel_name = 'sequential_preLN__grouped_kv_mqa__geglu_ffn__prompt'

def setup_inputs():

    _w = (lambda a: (lambda t: t.to(torch.bfloat16) if t.is_floating_point() else t)(torch.as_tensor(a)))
    def _A_setup():
        B, S, D, H = 1, 2048, 512, 32
        Dh = D // H
        return {
            "x": _w((lambda *s: torch.randn(*s, dtype=torch.bfloat16) * 0.05)(B, S, D)),
            "wq": _w((lambda *s: torch.randn(*s, dtype=torch.bfloat16) * 0.05)(D, H * Dh)),
            "wk": _w((lambda *s: torch.randn(*s, dtype=torch.bfloat16) * 0.05)(D, Dh)),
            "wv": _w((lambda *s: torch.randn(*s, dtype=torch.bfloat16) * 0.05)(D, Dh)),
            "wo": _w((lambda *s: torch.randn(*s, dtype=torch.bfloat16) * 0.05)(H * Dh, D)),
        }
    _A_in = _A_setup()

    def _B_setup():
        B, S, D, F = 1, 2048, 512, 1792
        return {
            "x": _w((lambda *s: torch.randn(*s, dtype=torch.bfloat16) * 0.05)(B, S, D)),
            "w_gate": _w((lambda *s: torch.randn(*s, dtype=torch.bfloat16) * 0.05)(D, F)),
            "w_up": _w((lambda *s: torch.randn(*s, dtype=torch.bfloat16) * 0.05)(D, F)),
            "w_down": _w((lambda *s: torch.randn(*s, dtype=torch.bfloat16) * 0.05)(F, D)),
        }
    _B_in = _B_setup()

    return {
        "A_x": _A_in["x"],
        "A_wq": _A_in["wq"],
        "A_wk": _A_in["wk"],
        "A_wv": _A_in["wv"],
        "A_wo": _A_in["wo"],
        "B_x": _B_in["x"],
        "B_w_gate": _B_in["w_gate"],
        "B_w_up": _B_in["w_up"],
        "B_w_down": _B_in["w_down"],
    }

def reference(A_x, A_wq, A_wk, A_wv, A_wo, B_x, B_w_gate, B_w_up, B_w_down):
    def _A_ref(x, wq, wk, wv, wo):
        B, S, D = x.shape
        H = 32
        Dh = D // H
        q = (lambda x, *d: x.permute(*d))((x @ wq).reshape(B, S, H, Dh), 0, 2, 1, 3)
        k = x @ wk
        v = x @ wv
        scores = torch.einsum("bhqd,bkd->bhqk", q, k) * (Dh ** -0.5)
        p = _softmax(scores, axis=-1)
        o = torch.einsum("bhqk,bkd->bhqd", p, v)
        return (lambda x, *d: x.permute(*d))(o, 0, 2, 1, 3).reshape(B, S, H * Dh) @ wo

    def _B_ref(x, w_gate, w_up, w_down):
        return (_gelu(x @ w_gate) * (x @ w_up)) @ w_down

    x = A_x
    h = x + _A_ref(_layernorm(x, axis=-1), A_wq, A_wk, A_wv, A_wo)
    h2 = h + _B_ref(_layernorm(h, axis=-1), B_w_gate, B_w_up, B_w_down)
    return h2
```

```cpp
#include <hip/hip_runtime.h>
#include <hip/hip_bf16.h>
typedef unsigned short u16;
__device__ __forceinline__ float b2f(u16 v){ return __uint_as_float(((unsigned)v)<<16); }
__device__ __forceinline__ u16 f2b(float f){ unsigned u=__float_as_uint(f); u += 0x7fff + ((u>>16)&1); return (u16)(u>>16); }
constexpr int S=2048, D=512, H=32, DH=16, F=1792;

__global__ void ln_k(const u16* x, u16* y){
  int r=blockIdx.x, l=threadIdx.x; float v[8]; float s=0;
  for(int i=0;i<8;i++){ v[i]=b2f(x[r*D+l*8+i]); s+=v[i]; }
  for(int o=32;o;o>>=1) s+=__shfl_xor(s,o);
  float m=s/D, q=0; for(int i=0;i<8;i++){ float d=v[i]-m; q+=d*d; }
  for(int o=32;o;o>>=1) q+=__shfl_xor(q,o);
  float rs=rsqrtf(q/D+1e-5f);
  for(int i=0;i<8;i++) y[r*D+l*8+i]=f2b((v[i]-m)*rs);
}
__global__ void gemm_k(const u16* A,const u16* B,u16* C,const u16* res,int M,int N,int K,float scale){
  __shared__ float As[16][17], Bs[16][17];
  int tx=threadIdx.x&15, ty=threadIdx.x>>4; int row=blockIdx.y*16+ty, col=blockIdx.x*16+tx; float acc=0;
  for(int k0=0;k0<K;k0+=16){ As[ty][tx]=b2f(A[row*K+k0+tx]); Bs[ty][tx]=(col<N)?b2f(B[(k0+ty)*N+col]):0.f; __syncthreads();
    for(int k=0;k<16;k++) acc+=As[ty][k]*Bs[k][tx]; __syncthreads(); }
  if(col<N){ float o=acc*scale; if(res) o=b2f(f2b(o))+b2f(res[row*N+col]); C[row*N+col]=f2b(o);} }
__global__ void attn_k(const u16* q,const u16* k,const u16* v,u16* o){
  int t=blockIdx.x, h=threadIdx.x; if(h>=H) return; float qv[16]; for(int d=0;d<16;d++) qv[d]=b2f(q[t*D+h*16+d]);
  float m=-1e30f; for(int j=0;j<S;j++){ float s=0; for(int d=0;d<16;d++) s+=qv[d]*b2f(k[j*16+d]); m=fmaxf(m,s*0.25f);}
  float l=0, acc[16]; for(int d=0;d<16;d++) acc[d]=0;
  for(int j=0;j<S;j++){ float s=0; for(int d=0;d<16;d++) s+=qv[d]*b2f(k[j*16+d]); float p=__expf(s*0.25f-m); l+=p; for(int d=0;d<16;d++) acc[d]+=p*b2f(v[j*16+d]); }
  for(int d=0;d<16;d++) o[t*D+h*16+d]=f2b(acc[d]/l);
}
__global__ void geglu_k(const u16* g,const u16* u,u16* h,int n){ int i=blockIdx.x*256+threadIdx.x; if(i<n){ float x=b2f(g[i]); float t=tanhf(0.7978845608f*(x+0.044715f*x*x*x)); h[i]=f2b(b2f(f2b(0.5f*x*(1+t)))*b2f(u[i])); } }

extern "C" void kernel_launch(void* const* d_in, const int* in_sizes, int n_in, void* d_out, int out_size, void* d_ws, size_t ws_size, hipStream_t stream){
  const u16 *x=(const u16*)d_in[0],*wq=(const u16*)d_in[1],*wk=(const u16*)d_in[2],*wv=(const u16*)d_in[3],*wo=(const u16*)d_in[4],*wg=(const u16*)d_in[6],*wu=(const u16*)d_in[7],*wd=(const u16*)d_in[8];
  u16* ws=(u16*)d_ws; u16 *ln=ws,*q=ln+S*D,*k=q+S*D,*v=k+S*16,*o=v+S*16,*h=o+S*D,*g=h+S*D,*u=g+S*F,*hh=u+S*F; u16* out=(u16*)d_out;
  ln_k<<<S,64,0,stream>>>(x,ln);
  gemm_k<<<dim3(D/16,S/16),256,0,stream>>>(ln,wq,q,nullptr,S,D,D,1.f);
  gemm_k<<<dim3(1,S/16),256,0,stream>>>(ln,wk,k,nullptr,S,16,D,1.f);
  gemm_k<<<dim3(1,S/16),256,0,stream>>>(ln,wv,v,nullptr,S,16,D,1.f);
  attn_k<<<S,64,0,stream>>>(q,k,v,o);
  gemm_k<<<dim3(D/16,S/16),256,0,stream>>>(o,wo,h,x,S,D,D,1.f);
  ln_k<<<S,64,0,stream>>>(h,ln);
  gemm_k<<<dim3(F/16,S/16),256,0,stream>>>(ln,wg,g,nullptr,S,F,D,1.f);
  gemm_k<<<dim3(F/16,S/16),256,0,stream>>>(ln,wu,u,nullptr,S,F,D,1.f);
  geglu_k<<<(S*F+255)/256,256,0,stream>>>(g,u,hh,S*F);
  gemm_k<<<dim3(D/16,S/16),256,0,stream>>>(hh,wd,out,h,S,D,F,1.f);
}
```

```cpp
#include <hip/hip_runtime.h>
#include <stdint.h>

typedef unsigned int u32;
typedef unsigned short u16;
typedef unsigned long long u64;
typedef __bf16 bf16x2 __attribute__((ext_vector_type(2)));
typedef __bf16 bf16x8 __attribute__((ext_vector_type(8)));
typedef __bf16 bf16x16 __attribute__((ext_vector_type(16)));
typedef float f32x2 __attribute__((ext_vector_type(2)));
typedef float f32x4 __attribute__((ext_vector_type(4)));
typedef float f32x8 __attribute__((ext_vector_type(8)));
typedef float f32x16 __attribute__((ext_vector_type(16)));
typedef u32 u32x2 __attribute__((ext_vector_type(2)));
typedef u32 u32x4 __attribute__((ext_vector_type(4)));
typedef short s16x4 __attribute__((ext_vector_type(4)));

#define AS3 __attribute__((address_space(3)))
#define DI __device__ __forceinline__

constexpr int S_ = 2048, D_ = 512, F_ = 1792;

constexpr size_t WS_Q = 0;
constexpr size_t WS_O = WS_Q + (2u << 20);
constexpr size_t WS_H = WS_O + (2u << 20);
constexpr size_t WS_K = WS_H + (2u << 20);
constexpr size_t WS_V = WS_K + 65536;
constexpr size_t WS_KM = WS_V + 69632;
constexpr size_t WS_ST = WS_KM + 1024;
constexpr size_t WS_CS = WS_ST + 131072;
constexpr size_t WS_WT = (WS_CS + 114688 + 4095) / 4096 * 4096;
constexpr size_t WS_F = WS_WT + 112 * 32768;
constexpr size_t WS_END = WS_F + (size_t)128 * 56 * 1024;

template <int N> DI void wait_vm() { asm volatile("s_waitcnt vmcnt(%0)" ::"i"(N) : "memory"); }
DI void bar() { asm volatile("s_barrier" ::: "memory"); }
DI void sched0() { __builtin_amdgcn_sched_barrier(0); }

template <int IMM> DI void dma16(u32 lds, const void* sbase, u32 voff) {
  asm volatile("s_mov_b32 m0, %0\n\ts_nop 0\n\tglobal_load_lds_dwordx4 %1, %2 offset:%3" ::"s"(lds), "v"(voff), "s"(sbase), "i"(IMM) : "memory");
}
DI void dma16x4(u32 lds, const void* sbase, u32 voff) {
  asm volatile("s_mov_b32 m0, %0\n\ts_nop 0\n\tglobal_load_lds_dwordx4 %1, %2\n\tglobal_load_lds_dwordx4 %1, %2 offset:1024\n\t"
               "global_load_lds_dwordx4 %1, %2 offset:2048\n\tglobal_load_lds_dwordx4 %1, %2 offset:3072" ::"s"(lds), "v"(voff), "s"(sbase) : "memory");
}
DI void touch_lines(u32 lds, const void* sbase, u32 voff) {
  asm volatile("s_mov_b32 m0, %0\n\ts_nop 0\n\tglobal_load_lds_dword %1, %2" ::"s"(lds), "v"(voff), "s"(sbase) : "memory");
}
template <int IMM> DI void gld16(u32x4& d, const void* sbase, u32 voff) {
  asm volatile("global_load_dwordx4 %0, %1, %2 offset:%3" : "=v"(d) : "v"(voff), "s"(sbase), "i"(IMM) : "memory");
}
template <int IMM> DI void gld8(u32x2& d, const void* sbase, u32 voff) {
  asm volatile("global_load_dwordx2 %0, %1, %2 offset:%3" : "=v"(d) : "v"(voff), "s"(sbase), "i"(IMM) : "memory");
}
DI void pin(u32x4& a) { asm volatile("" : "+v"(a)); }
DI void pin(u32x2& a) { asm volatile("" : "+v"(a)); }

DI u32x4 lds128(AS3 const char* p) { return *(AS3 const u32x4*)p; }
DI f32x4 ldsf4(AS3 const char* p) { return *(AS3 const f32x4*)p; }
DI u32x2 ldstr(AS3 const char* p) {
  return __builtin_bit_cast(u32x2, __builtin_amdgcn_ds_read_tr16_b64_v4i16((AS3 s16x4*)p));
}
DI u32x4 cat(u32x2 a, u32x2 b) { u32x4 r; r.x = a.x; r.y = a.y; r.z = b.x; r.w = b.y; return r; }
DI f32x16 mfma32(u32x4 a, u32x4 b, f32x16 c) {
  return __builtin_amdgcn_mfma_f32_32x32x16_bf16(__builtin_bit_cast(bf16x8, a), __builtin_bit_cast(bf16x8, b), c, 0, 0, 0);
}
DI f32x4 mfma16(u32x4 a, u32x4 b, f32x4 c) {
  return __builtin_amdgcn_mfma_f32_16x16x32_bf16(__builtin_bit_cast(bf16x8, a), __builtin_bit_cast(bf16x8, b), c, 0, 0, 0);
}
DI u32 pk(float a, float b) {
  f32x2 v = {a, b};
  return __builtin_bit_cast(u32, __builtin_convertvector(v, bf16x2));
}
DI float dot2(u32 a, u32 b, float c) {
  return __builtin_amdgcn_fdot2_f32_bf16(__builtin_bit_cast(bf16x2, a), __builtin_bit_cast(bf16x2, b), c, false);
}
DI float asf(u32 v) { return __builtin_bit_cast(float, v); }
DI float blo(u32 v) { return __builtin_bit_cast(float, v << 16); }
DI float bhi(u32 v) { return __builtin_bit_cast(float, v & 0xffff0000u); }
DI int wave_id() { return __builtin_amdgcn_readfirstlane(threadIdx.x >> 6); }
constexpr u32 ONES2 = 0x3F803F80u;

#define PROF_BEGIN
#define PROF_END(kid)

constexpr int K1_LDS = 65536 + 65536 + 16384 + 512;
__global__ __launch_bounds__(320) void k1_qkv(const u16* __restrict__ x, const u16* __restrict__ wq, const u16* __restrict__ wk,
                                              const u16* __restrict__ wv, char* __restrict__ ws) {
  extern __shared__ __attribute__((aligned(16))) char smem[];
  AS3 char* lds = (AS3 char*)smem;
  PROF_BEGIN
  const int l = threadIdx.x & 63, w = wave_id();
  const int b = blockIdx.x, xcd = b & 7, i = b >> 3, tt = xcd * 4 + (i & 3), cg = i >> 2;
  const u32 lb = (u32)(uintptr_t)lds;
  const int lr = l >> 3, lc = l & 7;
  const bool kvw = (w == 4) && (cg < 4);
  if (w < 4) {
    const u32 vx0 = lr * 1024 + ((lc ^ ((lr >> 1) & 7)) << 4);
    const u32 vx1 = lr * 1024 + ((lc ^ ((4 + (lr >> 1)) & 7)) << 4);
    const u32 vw = lr * 1024 + ((lc ^ (((lr >> 1) & 1) << 2)) << 4);
    const char* xb = (const char*)x + (size_t)(tt * 64 + 16 * w) * 1024;
    const char* wb = (const char*)wq + (size_t)(32 * w) * 1024 + cg * 128;
#pragma unroll
    for (int s = 0; s < 4; s++) {
#pragma unroll
      for (int j = 0; j < 4; j++) {
        const int kb = 2 * s + (j >> 1);
        dma16<0>(lb + kb * 8192 + (2 * w + (j & 1)) * 1024, xb + (j & 1) * 8192 + kb * 128, (j & 1) ? vx1 : vx0);
      }
#pragma unroll
      for (int j = 0; j < 4; j++)
        dma16<0>(lb + 65536 + (16 * s + 4 * w + j) * 1024, wb + (size_t)(128 * s + 8 * j) * 1024, vw);
    }
  } else if (kvw) {
    const char* src = (const char*)((cg & 1) ? wv : wk);
#pragma unroll
    for (int s = 0; s < 4; s++) dma16x4(lb + 131072 + s * 4096, src + s * 4096, l * 16);
  }
  const int r = l & 31, h = l >> 5;
  const int th = (w < 4) ? (w & 1) : ((cg >> 1) & 1);
  const int blk = (l >> 4) & 1, q = (l & 15) >> 2, p = l & 3;
  AS3 const char* xa[4];
#pragma unroll
  for (int k4 = 0; k4 < 4; k4++) xa[k4] = lds + (32 * th + r) * 128 + (((2 * k4 + h) ^ ((r >> 1) & 7)) << 4);
  f32x16 acc = {}, accs = {};
  float s1 = 0.f, s2 = 0.f;
  const u32x4 ones = {ONES2, ONES2, ONES2, ONES2};
  if (w < 4) {
    const int hp = w >> 1;
    AS3 const char* wa = lds + 65536 + (8 * h + q) * 128 + (((4 * hp + 2 * blk + (p >> 1)) ^ (((q >> 1) & 1) << 2)) << 4) + 8 * (p & 1);
#pragma unroll
    for (int s = 0; s < 4; s++) {
      sched0();
      if (s == 0) wait_vm<24>(); else if (s == 1) wait_vm<16>(); else if (s == 2) wait_vm<8>(); else wait_vm<0>();
      bar();
      sched0();
#pragma unroll
      for (int k8 = 0; k8 < 8; k8++) {
        const int ks = 8 * s + k8;
        const u32x4 bf = lds128(xa[ks & 3] + (ks >> 2) * 8192);
        const u32x4 af = cat(ldstr(wa + ks * 2048), ldstr(wa + ks * 2048 + 512));
        acc = mfma32(af, bf, acc);
        accs = mfma32(af, ones, accs);
        s1 = dot2(bf.x, ONES2, s1); s2 = dot2(bf.x, bf.x, s2);
        s1 = dot2(bf.y, ONES2, s1); s2 = dot2(bf.y, bf.y, s2);
        s1 = dot2(bf.z, ONES2, s1); s2 = dot2(bf.z, bf.z, s2);
        s1 = dot2(bf.w, ONES2, s1); s2 = dot2(bf.w, bf.w, s2);
      }
    }
    s1 += __shfl_xor(s1, 32); s2 += __shfl_xor(s2, 32);
    const float mu = s1 * (1.f / 512.f);
    const float var = fmaxf(s2 * (1.f / 512.f) - mu * mu, 0.f);
    const float sc = __builtin_amdgcn_rsqf(var + 1e-5f) * (0.25f * 1.4426950408889634f);
#pragma unroll
    for (int hh = 0; hh < 2; hh++) {
      u32x4 o;
      o.x = pk((acc[8 * hh + 0] - mu * accs[8 * hh + 0]) * sc, (acc[8 * hh + 1] - mu * accs[8 * hh + 1]) * sc);
      o.y = pk((acc[8 * hh + 2] - mu * accs[8 * hh + 2]) * sc, (acc[8 * hh + 3] - mu * accs[8 * hh + 3]) * sc);
      o.z = pk((acc[8 * hh + 4] - mu * accs[8 * hh + 4]) * sc, (acc[8 * hh + 5] - mu * accs[8 * hh + 5]) * sc);
      o.w = pk((acc[8 * hh + 6] - mu * accs[8 * hh + 6]) * sc, (acc[8 * hh + 7] - mu * accs[8 * hh + 7]) * sc);
      const int head = 4 * cg + 2 * hp + hh;
      *(u32x4*)(ws + WS_Q + ((size_t)((2 * tt + th) * 32 + head) * 64 + l) * 16) = o;
    }
  } else {
    const bool isV = cg & 1;
    AS3 const char* wa = lds + 131072 + (8 * h + q) * 32 + p * 8;
#pragma unroll
    for (int s = 0; s < 4; s++) {
      sched0();
      if (s == 0) wait_vm<12>(); else if (s == 1) wait_vm<8>(); else if (s == 2) wait_vm<4>(); else wait_vm<0>();
      bar();
      sched0();
      if (kvw) {
#pragma unroll
        for (int k8 = 0; k8 < 8; k8++) {
          const int ks = 8 * s + k8;
          const u32x4 bf = lds128(xa[ks & 3] + (ks >> 2) * 8192);
          const u32x4 af = cat(ldstr(wa + ks * 512), ldstr(wa + ks * 512 + 128));
          if (isV) { acc = mfma32(bf, af, acc); accs = mfma32(ones, af, accs); }
          else { acc = mfma32(af, bf, acc); accs = mfma32(af, ones, accs); }
          s1 = dot2(bf.x, ONES2, s1); s2 = dot2(bf.x, bf.x, s2);
          s1 = dot2(bf.y, ONES2, s1); s2 = dot2(bf.y, bf.y, s2);
          s1 = dot2(bf.z, ONES2, s1); s2 = dot2(bf.z, bf.z, s2);
          s1 = dot2(bf.w, ONES2, s1); s2 = dot2(bf.w, bf.w, s2);
        }
      }
    }
    if (kvw) {
      s1 += __shfl_xor(s1, 32); s2 += __shfl_xor(s2, 32);
      const float mu = s1 * (1.f / 512.f);
      const float var = fmaxf(s2 * (1.f / 512.f) - mu * mu, 0.f);
      const float rs = __builtin_amdgcn_rsqf(var + 1e-5f);
      const int T = 2 * tt + th;
      if (!isV) {
        float kv[8], n2 = 0.f;
#pragma unroll
        for (int j = 0; j < 8; j++) { kv[j] = (acc[j] - mu * accs[j]) * rs; n2 += kv[j] * kv[j]; }
        u32x4 o = {pk(kv[0], kv[1]), pk(kv[2], kv[3]), pk(kv[4], kv[5]), pk(kv[6], kv[7])};
        const int kap = (r & 0x13) | ((r & 4) << 1) | ((r & 8) >> 1);
        *(u32x4*)(ws + WS_K + ((size_t)T * 64 + h * 32 + kap) * 16) = o;
        n2 += __shfl_xor(n2, 32);
#pragma unroll
        for (int o2 = 16; o2; o2 >>= 1) n2 = fmaxf(n2, __shfl_xor(n2, o2));
        if (l == 0) ((float*)(ws + WS_KM))[T] = n2;
      } else {
        AS3 f32x2* st = (AS3 f32x2*)(lds + 147456);
        if (h == 0) { f32x2 v = {mu, rs}; st[r] = v; }
        float vv[16];
#pragma unroll
        for (int j = 0; j < 16; j++) {
          const f32x2 m = st[(j & 3) + 8 * (j >> 2) + 4 * h];
          vv[j] = (acc[j] - m.x * accs[0]) * m.y;
        }
        if (r <= 16) {
#pragma unroll
          for (int s = 0; s < 2; s++) {
            u32x4 o = {pk(vv[8 * s], vv[8 * s + 1]), pk(vv[8 * s + 2], vv[8 * s + 3]), pk(vv[8 * s + 4], vv[8 * s + 5]), pk(vv[8 * s + 6], vv[8 * s + 7])};
            if (r == 16) o = ones;
            *(u32x4*)(ws + WS_V + (size_t)T * 1088 + s * 544 + h * 272 + r * 16) = o;
          }
        }
      }
    }
  }
  PROF_END(0)
}

constexpr int K2_V = 65536, K2_STG = 135168, K2_JUNK = K2_STG + 16384, K2_LDS = K2_JUNK + 256;
constexpr float SAFE_LOG2 = 60.f;

template <bool SUBMAX>
DI void attn_tiles(AS3 const char* ka, AS3 const char* va, const u32x4 qf, const float negm, f32x16& o) {
#pragma unroll
  for (int u = 0; u < 4; u++) {
    const u32x4 kf = lds128(ka + u * 1024);
    const u32x4 v0 = lds128(va + u * 1088), v1 = lds128(va + u * 1088 + 544);
    f32x16 c;
#pragma unroll
    for (int j = 0; j < 16; j++) c[j] = SUBMAX ? negm : 0.f;
    f32x16 s = mfma32(kf, qf, c);
#pragma unroll
    for (int j = 0; j < 16; j++) s[j] = __builtin_amdgcn_exp2f(s[j]);
    const f32x8 p0 = __builtin_shufflevector(s, s, 0, 1, 2, 3, 4, 5, 6, 7);
    const f32x8 p1 = __builtin_shufflevector(s, s, 8, 9, 10, 11, 12, 13, 14, 15);
    o = __builtin_amdgcn_smfmac_f32_32x32x32_bf16(__builtin_bit_cast(bf16x8, v0), __builtin_bit_cast(bf16x16, p0), o, (int)0xDDDDDDDD, 0, 0);
    o = __builtin_amdgcn_smfmac_f32_32x32x32_bf16(__builtin_bit_cast(bf16x8, v1), __builtin_bit_cast(bf16x16, p1), o, (int)0xDDDDDDDD, 0, 0);
  }
}

__global__ __launch_bounds__(512) void k2_attn(const u16* __restrict__ wg, const u16* __restrict__ wu, const u16* __restrict__ wo,
                                               char* __restrict__ ws) {
  extern __shared__ __attribute__((aligned(16))) char smem[];
  AS3 char* lds = (AS3 char*)smem;
  PROF_BEGIN
  const int l = threadIdx.x & 63, w = wave_id();
  const int b = blockIdx.x, xcd = b & 7, i = b >> 3, tt = xcd * 4 + (i & 3), hg = i >> 2;
  const u32 lb = (u32)(uintptr_t)lds;
  const int th = w & 1, head = 4 * hg + (w >> 1);
  const int r = l & 31, h = l >> 5;
  u32x4 qf; u32x2 kmx;
  gld16<0>(qf, ws + WS_Q + (size_t)((2 * tt + th) * 32 + head) * 1024, l * 16);
  gld8<0>(kmx, ws + WS_KM, (l & 31) * 8);
  if (w < 4) dma16<0>(lb + K2_V + (17 * w + 16) * 1024, ws + WS_V + (17 * w + 16) * 1024, l * 16);
  const bool side = b < 224;
  const int cgp = b % 28, kc = b / 28;
#pragma unroll
  for (int c = 0; c < 4; c++) {
    dma16<0>(lb + (16 * c + 2 * w) * 1024, ws + WS_K + (16 * c + 2 * w) * 1024, l * 16);
    dma16<1024>(lb + (16 * c + 2 * w) * 1024, ws + WS_K + (16 * c + 2 * w) * 1024, l * 16);
    dma16<0>(lb + K2_V + (17 * c + 2 * w) * 1024, ws + WS_V + (17 * c + 2 * w) * 1024, l * 16);
    dma16<1024>(lb + K2_V + (17 * c + 2 * w) * 1024, ws + WS_V + (17 * c + 2 * w) * 1024, l * 16);
    if (c == 1) {
      const int kk = side ? kc : 0;
      const size_t ro = (size_t)(64 * kk + 8 * w) * 3584 + cgp * 128;
      const u32 vo = (l >> 3) * 3584 + (l & 7) * 16;
      dma16<0>(lb + K2_STG + w * 1024, (const char*)wg + ro, vo);
      dma16<0>(lb + K2_STG + 8192 + w * 1024, (const char*)wu + ro, vo);
    }
  }
  asm volatile("s_waitcnt vmcnt(18)" : "+v"(qf), "+v"(kmx)::"memory");
  float km = fmaxf(asf(kmx.x), asf(kmx.y));
#pragma unroll
  for (int o2 = 16; o2; o2 >>= 1) km = fmaxf(km, __shfl_xor(km, o2));
  float q2 = dot2(qf.x, qf.x, 0.f); q2 = dot2(qf.y, qf.y, q2); q2 = dot2(qf.z, qf.z, q2); q2 = dot2(qf.w, qf.w, q2);
  q2 += __shfl_xor(q2, 32);
  const bool unsafe = __any(!(q2 * km * 1.05f <= SAFE_LOG2 * SAFE_LOG2));

  AS3 const char* ka = lds + l * 16;
  AS3 const char* va = lds + K2_V + h * 272 + (r < 16 ? r * 16 : (r < 20 ? 256 : (r - 16) * 16));
  f32x16 o = {};
  auto side_job = [&]() {
    if (side && w < 4) {
      const int blk = (l >> 4) & 1, q = (l & 15) >> 2, p = l & 3;
      AS3 const char* sa = lds + K2_STG + blk * 8192 + (8 * h + q) * 128 + (2 * w + (p >> 1)) * 16 + 8 * (p & 1);
      float cs = 0.f;
      char* dst = ws + WS_WT + ((size_t)(4 * cgp + w) * 32 + 4 * kc) * 1024 + l * 16;
#pragma unroll
      for (int ks = 0; ks < 4; ks++) {
        const u32x4 f = cat(ldstr(sa + ks * 2048), ldstr(sa + ks * 2048 + 512));
        cs = dot2(f.x, ONES2, cs); cs = dot2(f.y, ONES2, cs); cs = dot2(f.z, ONES2, cs); cs = dot2(f.w, ONES2, cs);
        asm volatile("global_store_dwordx4 %0, %1, off sc1\n\ts_nop 1" ::"v"(dst + ks * 1024), "v"(f) : "memory");
      }
      cs += __shfl_xor(cs, 32);
      if (h == 0) ((float*)(ws + WS_CS))[((4 * cgp + w) * 32 + r) * 8 + kc] = cs;
    }
  };
  auto prefetch = [&]() {
    const char* src = (w < 7) ? (ws + WS_WT + ((size_t)(xcd & 1) * 224 + i * 7 + w) * 8192) : ((const char*)wo + (size_t)i * 16384);
    touch_lines(lb + K2_JUNK, src, l * 128);
    if (w == 7) touch_lines(lb + K2_JUNK, src + 8192, l * 128);
  };
  if (!unsafe) {
#pragma unroll 1
    for (int c = 0; c < 4; c++) {
      if (c == 0) wait_vm<14>(); else if (c == 1) wait_vm<10>(); else if (c == 2) wait_vm<4>(); else wait_vm<0>();
      bar();
      if (c == 2) side_job();
      if (c == 3) prefetch();
#pragma unroll 1
      for (int it = 0; it < 4; it++) {
        attn_tiles<false>(ka, va, qf, 0.f, o);
        ka += 4096; va += 4352;
      }
    }
  } else {
    float m = -3.0e38f;
#pragma unroll 1
    for (int c = 0; c < 4; c++) {
      if (c == 0) wait_vm<14>(); else if (c == 1) wait_vm<10>(); else if (c == 2) wait_vm<4>(); else wait_vm<0>();
      bar();
      if (c == 2) side_job();
      if (c == 3) prefetch();
#pragma unroll 1
      for (int t = 0; t < 16; t++) {
        const f32x16 z = {};
        const f32x16 s = mfma32(lds128(ka + (16 * c + t) * 1024), qf, z);
#pragma unroll
        for (int j = 0; j < 16; j++) m = fmaxf(m, s[j]);
      }
    }
    m = fmaxf(m, __shfl_xor(m, 32));
#pragma unroll 1
    for (int it = 0; it < 16; it++) {
      attn_tiles<true>(ka, va, qf, -m, o);
      ka += 4096; va += 4352;
    }
  }
  const float den = __shfl(o[8], r);
  const float inv = 1.f / den;
  const u32x4 res = {pk(o[0] * inv, o[1] * inv), pk(o[2] * inv, o[3] * inv), pk(o[4] * inv, o[5] * inv), pk(o[6] * inv, o[7] * inv)};
  const int t16 = 4 * tt + 2 * th + (r >> 4);
  *(u32x4*)(ws + WS_O + ((size_t)(t16 * 16 + (head >> 1)) * 64 + (2 * (head & 1) + h) * 16 + (r & 15)) * 16) = res;
  wait_vm<0>();
  PROF_END(1)
}

template <int NST, int RING, int AHEAD, bool IS_K3>
DI void gemm64(const char* __restrict__ act, const u16* __restrict__ wgt, const u16* __restrict__ xres, char* __restrict__ ws,
               u16* __restrict__ out, AS3 char* lds, const int tt, const int cg) {
  const int l = threadIdx.x & 63, w = wave_id();
  const u32 lb = (u32)(uintptr_t)lds;
  const int lr = l >> 3, lc = l & 7;
  const int g = l >> 4, q = (l & 15) >> 2, p = l & 3, r16 = l & 15;
  constexpr int NB = (AHEAD + 1 < NST ? AHEAD + 1 : NST);
  u32x4 bf[NB * 4];
  u32x2 res[4];
  const int tok = tt * 64 + 16 * w + r16;
  if (IS_K3) {
    const char* xb = (const char*)xres + (size_t)(tt * 64 + 16 * w) * 1024 + cg * 128;
    const u32 vo = r16 * 1024 + g * 8;
    gld8<0>(res[0], xb, vo); gld8<32>(res[1], xb, vo); gld8<64>(res[2], xb, vo); gld8<96>(res[3], xb, vo);
  } else {
    const char* hb = ws + WS_H + (size_t)((tt * 2 + (w >> 1)) * 32 + cg * 4) * 1024;
    const u32 vo = ((g >> 1) * 32 + (w & 1) * 16 + r16) * 16 + (g & 1) * 8;
    gld8<0>(res[0], hb, vo); gld8<1024>(res[1], hb, vo); gld8<2048>(res[2], hb, vo); gld8<3072>(res[3], hb, vo);
  }
  const char* ab = act + (size_t)(tt * 4 + w) * (NST * 4096);
  const char* wb = (const char*)wgt + (size_t)(32 * w) * 1024 + cg * 128;
  const u32 vw = lr * 1024 + ((lc ^ (((lr >> 1) & 3) << 1)) << 4);
  const u32 va = l * 16;
  auto issue = [&](const int s) {
    const u32 dst = lb + (s % RING) * 16384 + w * 4096;
    const char* src = wb + (size_t)s * 131072;
    dma16<0>(dst, src, vw); dma16<0>(dst + 1024, src + 8192, vw); dma16<0>(dst + 2048, src + 16384, vw); dma16<0>(dst + 3072, src + 24576, vw);
    const char* as = ab + s * 4096;
    const int o = (s % NB) * 4;
    gld16<0>(bf[o], as, va); gld16<1024>(bf[o + 1], as, va); gld16<2048>(bf[o + 2], as, va); gld16<3072>(bf[o + 3], as, va);
  };
#pragma unroll
  for (int s = 0; s < AHEAD && s < NST; s++) issue(s);
  const int swz = (2 * (g & 1) + (q >> 1)) << 1;
  AS3 const char* wa[4];
#pragma unroll
  for (int c = 0; c < 4; c++) wa[c] = lds + (16 * (g >> 1) + 4 * (g & 1) + q) * 128 + (((2 * c) ^ swz) + (p >> 1)) * 16 + 8 * (p & 1);
  f32x4 acc[4] = {};
#pragma unroll
  for (int s = 0; s < NST; s++) {
    constexpr int dummy = 0; (void)dummy;
    const int last = (s + AHEAD - 1 < NST - 1) ? (s + AHEAD - 1) : (NST - 1);
    const int o = (s % NB) * 4;
    sched0();
    switch (last - s) {
      case 0: asm volatile("s_waitcnt vmcnt(0)\n\ts_barrier" : "+v"(bf[o]), "+v"(bf[o + 1]), "+v"(bf[o + 2]), "+v"(bf[o + 3])::"memory"); break;
      case 1: asm volatile("s_waitcnt vmcnt(8)\n\ts_barrier" : "+v"(bf[o]), "+v"(bf[o + 1]), "+v"(bf[o + 2]), "+v"(bf[o + 3])::"memory"); break;
      case 2: asm volatile("s_waitcnt vmcnt(16)\n\ts_barrier" : "+v"(bf[o]), "+v"(bf[o + 1]), "+v"(bf[o + 2]), "+v"(bf[o + 3])::"memory"); break;
      case 3: asm volatile("s_waitcnt vmcnt(24)\n\ts_barrier" : "+v"(bf[o]), "+v"(bf[o + 1]), "+v"(bf[o + 2]), "+v"(bf[o + 3])::"memory"); break;
      case 4: asm volatile("s_waitcnt vmcnt(32)\n\ts_barrier" : "+v"(bf[o]), "+v"(bf[o + 1]), "+v"(bf[o + 2]), "+v"(bf[o + 3])::"memory"); break;
      case 5: asm volatile("s_waitcnt vmcnt(40)\n\ts_barrier" : "+v"(bf[o]), "+v"(bf[o + 1]), "+v"(bf[o + 2]), "+v"(bf[o + 3])::"memory"); break;
      default: asm volatile("s_waitcnt vmcnt(48)\n\ts_barrier" : "+v"(bf[o]), "+v"(bf[o + 1]), "+v"(bf[o + 2]), "+v"(bf[o + 3])::"memory"); break;
    }
    sched0();
    if (s + AHEAD < NST) issue(s + AHEAD);
    const int so = (s % RING) * 16384;
#pragma unroll
    for (int ks = 0; ks < 4; ks++) {
#pragma unroll
      for (int c = 0; c < 4; c++) {
        const u32x4 af = cat(ldstr(wa[c] + so + ks * 4096), ldstr(wa[c] + so + ks * 4096 + 1024));
        acc[c] = mfma16(af, bf[o + ks], acc[c]);
      }
    }
  }
  sched0();
  asm volatile("" : "+v"(res[0]), "+v"(res[1]), "+v"(res[2]), "+v"(res[3]));
  if (IS_K3) {
    float s1 = 0.f, s2 = 0.f;
    char* hb = ws + WS_H + (size_t)((tt * 2 + (w >> 1)) * 32 + cg * 4) * 1024 + ((g >> 1) * 32 + (w & 1) * 16 + r16) * 16 + (g & 1) * 8;
#pragma unroll
    for (int c = 0; c < 4; c++) {
      u32x2 hv;
      hv.x = pk(blo(res[c].x) + acc[c][0], bhi(res[c].x) + acc[c][1]);
      hv.y = pk(blo(res[c].y) + acc[c][2], bhi(res[c].y) + acc[c][3]);
      s1 = dot2(hv.x, ONES2, s1); s1 = dot2(hv.y, ONES2, s1);
      s2 = dot2(hv.x, hv.x, s2); s2 = dot2(hv.y, hv.y, s2);
      *(u32x2*)(hb + c * 1024) = hv;
    }
    s1 += __shfl_xor(s1, 16); s2 += __shfl_xor(s2, 16);
    s1 += __shfl_xor(s1, 32); s2 += __shfl_xor(s2, 32);
    if (g == 0) { f32x2 v = {s1, s2}; *(f32x2*)(ws + WS_ST + ((size_t)tok * 8 + cg) * 8) = v; }
  } else {
    u16* ob = out + (size_t)tok * 512 + cg * 64 + 4 * g;
#pragma unroll
    for (int c = 0; c < 4; c++) {
      u32x2 ov;
      ov.x = pk(blo(res[c].x) + acc[c][0], bhi(res[c].x) + acc[c][1]);
      ov.y = pk(blo(res[c].y) + acc[c][2], bhi(res[c].y) + acc[c][3]);
      *(u32x2*)(ob + 16 * c) = ov;
    }
  }
}

constexpr int K3_LDS = 4 * 16384;
__global__ __launch_bounds__(256) void k3_oproj(const u16* __restrict__ x, const u16* __restrict__ wo, char* __restrict__ ws) {
  extern __shared__ __attribute__((aligned(16))) char smem[];
  PROF_BEGIN
  const int b = blockIdx.x, xcd = b & 7, i = b >> 3;
  gemm64<4, 4, 4, true>(ws + WS_O, wo, x, ws, nullptr, (AS3 char*)smem, xcd * 4 + (i & 3), i >> 2);
  PROF_END(2)
}
constexpr int K5_RING = 9, K5_LDS = K5_RING * 16384;
__global__ __launch_bounds__(256) void k5_down(const u16* __restrict__ wd, char* __restrict__ ws, u16* __restrict__ out) {
  extern __shared__ __attribute__((aligned(16))) char smem[];
  PROF_BEGIN
  const int b = blockIdx.x, xcd = b & 7, i = b >> 3;
  gemm64<14, K5_RING, 7, false>(ws + WS_F, wd, nullptr, ws, out, (AS3 char*)smem, xcd * 4 + (i & 3), i >> 2);
  PROF_END(4)
}

constexpr int K4_RING = 9, K4_AHEAD = 6, K4_CS = K4_RING * 16384, K4_LDS = K4_CS + 1024;
DI u32x4 geglu(const f32x16& a, AS3 const char* cs, const float mu, const float rs) {
  const f32x4 cg0 = ldsf4(cs), cg1 = ldsf4(cs + 32), cu0 = ldsf4(cs + 64), cu1 = ldsf4(cs + 96);
  constexpr float C1 = -2.f * 0.7978845608028654f * 1.4426950408889634f, C2 = C1 * 0.044715f;
  float o[8];
#pragma unroll
  for (int j = 0; j < 8; j++) {
    const float cgv = j < 4 ? cg0[j & 3] : cg1[j & 3], cuv = j < 4 ? cu0[j & 3] : cu1[j & 3];
    const float gv = (a[j] - mu * cgv) * rs;
    const float uv = (a[8 + j] - mu * cuv) * rs;
    const float e = __builtin_amdgcn_exp2f(gv * (C1 + C2 * gv * gv));
    o[j] = gv * uv * __builtin_amdgcn_rcpf(1.f + e);
  }
  u32x4 r = {pk(o[0], o[1]), pk(o[2], o[3]), pk(o[4], o[5]), pk(o[6], o[7])};
  return r;
}
template <int N> DI void wait_bar4(u32x4& a, u32x4& b, u32x4& c, u32x4& d) {
  asm volatile("s_waitcnt vmcnt(%4)\n\ts_barrier" : "+v"(a), "+v"(b), "+v"(c), "+v"(d) : "i"(N) : "memory");
}
template <int N> DI void wait_bar() { asm volatile("s_waitcnt vmcnt(%0)\n\ts_barrier" ::"i"(N) : "memory"); }

__global__ __launch_bounds__(256) void k4_up(char* __restrict__ ws) {
  extern __shared__ __attribute__((aligned(16))) char smem[];
  AS3 char* lds = (AS3 char*)smem;
  PROF_BEGIN
  const int l = threadIdx.x & 63, w = wave_id();
  const int b = blockIdx.x, xcd = b & 7, i = b >> 3;
  const int tt = 4 * (xcd >> 1) + (i & 3), sl = 8 * (xcd & 1) + (i >> 2);
  const u32 lb = (u32)(uintptr_t)lds;
  const int r = l & 31, h = l >> 5;
  const int t32 = 4 * tt + w;
  u32x4 st[4], cp[2];
  {
    const char* sb = ws + WS_ST + (size_t)t32 * 32 * 64;
    gld16<0>(st[0], sb, r * 64); gld16<16>(st[1], sb, r * 64); gld16<32>(st[2], sb, r * 64); gld16<48>(st[3], sb, r * 64);
    const char* cb = ws + WS_CS + (size_t)(sl * 224 + 56 * w) * 32;
    const u32 co = (l < 56 ? l : 55) * 32;
    gld16<0>(cp[0], cb, co); gld16<16>(cp[1], cb, co);
  }
  u32x4 hf[32];
  const char* hb = ws + WS_H + (size_t)t32 * 32768;
  const char* wt = ws + WS_WT + (size_t)sl * 7 * 32768;
  const u32 va = l * 16;
  auto issue = [&](const int s) {
    const u32 dst = lb + (s % K4_RING) * 16384 + w * 4096;
    if (s < 8) {
      dma16x4(dst, wt + (size_t)w * 32768 + s * 4096, va);
      const char* hs = hb + s * 4096;
      gld16<0>(hf[4 * s], hs, va); gld16<1024>(hf[4 * s + 1], hs, va); gld16<2048>(hf[4 * s + 2], hs, va); gld16<3072>(hf[4 * s + 3], hs, va);
    } else {
      dma16x4(dst, wt + (size_t)(4 + ((s - 8) >> 1)) * 32768 + ((s - 8) & 1) * 16384 + w * 4096, va);
    }
  };
#pragma unroll
  for (int s = 0; s < K4_AHEAD; s++) issue(s);
  AS3 const char* wa = lds + l * 16;
  AS3 const char* csa = lds + K4_CS + h * 16;
  char* fo = ws + WS_F + ((size_t)(t32 * 2 + (r >> 4)) * 56 * 64 + h * 16 + (r & 15)) * 16;
  const int n0 = sl * 7;
  float mu = 0.f, rs = 0.f;
  f32x16 acc[4] = {};
  auto store_tile = [&](const int t, const f32x16& a) {
    const int n = n0 + t;
    const u32x4 o = geglu(a, csa + t * 128, mu, rs);
    *(u32x4*)(fo + ((n >> 1) * 64 + (n & 1) * 32) * 16) = o;
  };
#pragma unroll
  for (int s = 0; s < 8; s++) {
    sched0();
    if (s <= 2) wait_bar4<40>(hf[4 * s], hf[4 * s + 1], hf[4 * s + 2], hf[4 * s + 3]);
    else if (s == 3) wait_bar4<36>(hf[4 * s], hf[4 * s + 1], hf[4 * s + 2], hf[4 * s + 3]);
    else if (s == 4) wait_bar4<32>(hf[4 * s], hf[4 * s + 1], hf[4 * s + 2], hf[4 * s + 3]);
    else if (s == 5) wait_bar4<28>(hf[4 * s], hf[4 * s + 1], hf[4 * s + 2], hf[4 * s + 3]);
    else if (s == 6) wait_bar4<24>(hf[4 * s], hf[4 * s + 1], hf[4 * s + 2], hf[4 * s + 3]);
    else wait_bar4<20>(hf[4 * s], hf[4 * s + 1], hf[4 * s + 2], hf[4 * s + 3]);
    sched0();
    issue(s + K4_AHEAD);
    if (s == 0) {
      asm volatile("" : "+v"(st[0]), "+v"(st[1]), "+v"(st[2]), "+v"(st[3]), "+v"(cp[0]), "+v"(cp[1]));
      float s1 = 0.f, s2 = 0.f;
#pragma unroll
      for (int j = 0; j < 4; j++) {
        s1 += asf(st[j].x) + asf(st[j].z);
        s2 += asf(st[j].y) + asf(st[j].w);
      }
      mu = s1 * (1.f / 512.f);
      rs = __builtin_amdgcn_rsqf(fmaxf(s2 * (1.f / 512.f) - mu * mu, 0.f) + 1e-5f);
      float c = 0.f;
#pragma unroll
      for (int j = 0; j < 2; j++)
        c += (asf(cp[j].x) + asf(cp[j].y)) + (asf(cp[j].z) + asf(cp[j].w));
      if (l < 56) *(AS3 float*)(lds + K4_CS + (56 * w + l) * 4) = c;
      asm volatile("s_waitcnt lgkmcnt(0)" ::: "memory");
    }
    const int so = (s % K4_RING) * 16384;
#pragma unroll
    for (int ks = 0; ks < 4; ks++)
#pragma unroll
      for (int t = 0; t < 4; t++) acc[t] = mfma32(lds128(wa + so + t * 4096 + ks * 1024), hf[4 * s + ks], acc[t]);
  }
#pragma unroll
  for (int t = 4; t < 7; t++) {
    f32x16 a = {};
#pragma unroll
    for (int hh = 0; hh < 2; hh++) {
      const int s = 8 + 2 * (t - 4) + hh;
      sched0();
      if (s == 8) wait_bar<20>(); else if (s == 9) wait_bar<16>(); else if (s == 10) wait_bar<12>();
      else if (s == 11) wait_bar<8>(); else if (s == 12) wait_bar<4>(); else wait_bar<0>();
      sched0();
      const int so = (s % K4_RING) * 16384;
#pragma unroll
      for (int ks = 0; ks < 16; ks++) a = mfma32(lds128(wa + so + ks * 1024), hf[16 * hh + ks], a);
      if (t == 4 && hh == 0) { store_tile(0, acc[0]); store_tile(1, acc[1]); }
      if (t == 4 && hh == 1) { store_tile(2, acc[2]); store_tile(3, acc[3]); }
    }
    store_tile(t, a);
  }
  PROF_END(3)
}

extern "C" void kernel_launch(void* const* d_in, const int* in_sizes, int n_in, void* d_out, int out_size, void* d_ws, size_t ws_size,
                              hipStream_t stream) {
  const u16 *x = (const u16*)d_in[0], *wq = (const u16*)d_in[1], *wk = (const u16*)d_in[2], *wv = (const u16*)d_in[3],
            *wo = (const u16*)d_in[4], *wg = (const u16*)d_in[6], *wu = (const u16*)d_in[7], *wd = (const u16*)d_in[8];
  char* ws = (char*)d_ws;
  (void)hipFuncSetAttribute((const void*)k1_qkv, hipFuncAttributeMaxDynamicSharedMemorySize, K1_LDS);
  (void)hipFuncSetAttribute((const void*)k2_attn, hipFuncAttributeMaxDynamicSharedMemorySize, K2_LDS);
  (void)hipFuncSetAttribute((const void*)k3_oproj, hipFuncAttributeMaxDynamicSharedMemorySize, K3_LDS);
  (void)hipFuncSetAttribute((const void*)k4_up, hipFuncAttributeMaxDynamicSharedMemorySize, K4_LDS);
  (void)hipFuncSetAttribute((const void*)k5_down, hipFuncAttributeMaxDynamicSharedMemorySize, K5_LDS);
  k1_qkv<<<256, 320, K1_LDS, stream>>>(x, wq, wk, wv, ws);
  k2_attn<<<256, 512, K2_LDS, stream>>>(wg, wu, wo, ws);
  k3_oproj<<<256, 256, K3_LDS, stream>>>(x, wo, ws);
  k4_up<<<256, 256, K4_LDS, stream>>>(ws);
  k5_down<<<256, 256, K5_LDS, stream>>>(wd, ws, (u16*)d_out);
}
```

```cpp
#include <hip/hip_runtime.h>
#include <stdint.h>

typedef unsigned int u32;
typedef unsigned short u16;
typedef unsigned long long u64;
typedef __bf16 bf16x2 __attribute__((ext_vector_type(2)));
typedef __bf16 bf16x8 __attribute__((ext_vector_type(8)));
typedef __bf16 bf16x16 __attribute__((ext_vector_type(16)));
typedef float f32x2 __attribute__((ext_vector_type(2)));
typedef float f32x4 __attribute__((ext_vector_type(4)));
typedef float f32x8 __attribute__((ext_vector_type(8)));
typedef float f32x16 __attribute__((ext_vector_type(16)));
typedef u32 u32x2 __attribute__((ext_vector_type(2)));
typedef u32 u32x4 __attribute__((ext_vector_type(4)));
typedef short s16x4 __attribute__((ext_vector_type(4)));

#define AS3 __attribute__((address_space(3)))
#define DI __device__ __forceinline__

constexpr int S_ = 2048, D_ = 512, F_ = 1792;

constexpr size_t WS_Q = 0;
constexpr size_t WS_O = WS_Q + (2u << 20);
constexpr size_t WS_H = WS_O + (2u << 20);
constexpr size_t WS_K = WS_H + (2u << 20);
constexpr size_t WS_V = WS_K + 65536;
constexpr size_t WS_KM = WS_V + 69632;
constexpr size_t WS_ST = WS_KM + 1024;
constexpr size_t WS_CS = WS_ST + 131072;
constexpr size_t WS_WT = (WS_CS + 114688 + 4095) / 4096 * 4096;
constexpr size_t WS_F = WS_WT + 112 * 32768;
constexpr size_t WS_END = WS_F + (size_t)128 * 56 * 1024;

template <int N> DI void wait_vm() { asm volatile("s_waitcnt vmcnt(%0)" ::"i"(N) : "memory"); }
DI void bar() { asm volatile("s_barrier" ::: "memory"); }
template <int N> DI void wait_bar4(u32x4& a, u32x4& b, u32x4& c, u32x4& d) {
  asm volatile("s_waitcnt vmcnt(%4)\n\ts_barrier" : "+v"(a), "+v"(b), "+v"(c), "+v"(d) : "i"(N) : "memory");
}
template <int N> DI void wait_bar() { asm volatile("s_waitcnt vmcnt(%0)\n\ts_barrier" ::"i"(N) : "memory"); }

template <int V> struct IntC { static constexpr int value = V; };
template <int I, int N, class F> DI void sfor(F&& f) { if constexpr (I < N) { f(IntC<I>{}); sfor<I + 1, N>(f); } }
DI void sched0() { __builtin_amdgcn_sched_barrier(0); }

template <int IMM> DI void dma16(u32 lds, const void* sbase, u32 voff) {
  asm volatile("s_mov_b32 m0, %0\n\ts_nop 0\n\tglobal_load_lds_dwordx4 %1, %2 offset:%3" ::"s"(lds), "v"(voff), "s"(sbase), "i"(IMM) : "memory");
}
DI void dma16x4(u32 lds, const void* sbase, u32 voff) {
  asm volatile("s_mov_b32 m0, %0\n\ts_nop 0\n\tglobal_load_lds_dwordx4 %1, %2\n\tglobal_load_lds_dwordx4 %1, %2 offset:1024\n\t"
               "global_load_lds_dwordx4 %1, %2 offset:2048\n\tglobal_load_lds_dwordx4 %1, %2 offset:3072" ::"s"(lds), "v"(voff), "s"(sbase) : "memory");
}
DI void touch_lines(u32 lds, const void* sbase, u32 voff) {
  asm volatile("s_mov_b32 m0, %0\n\ts_nop 0\n\tglobal_load_lds_dword %1, %2" ::"s"(lds), "v"(voff), "s"(sbase) : "memory");
}
template <int IMM> DI void gld16(u32x4& d, const void* sbase, u32 voff) {
  asm volatile("global_load_dwordx4 %0, %1, %2 offset:%3" : "=v"(d) : "v"(voff), "s"(sbase), "i"(IMM) : "memory");
}
template <int IMM> DI void gld8(u32x2& d, const void* sbase, u32 voff) {
  asm volatile("global_load_dwordx2 %0, %1, %2 offset:%3" : "=v"(d) : "v"(voff), "s"(sbase), "i"(IMM) : "memory");
}
DI void pin(u32x4& a) { asm volatile("" : "+v"(a)); }
DI void pin(u32x2& a) { asm volatile("" : "+v"(a)); }

DI u32x4 lds128(AS3 const char* p) { return *(AS3 const u32x4*)p; }
DI f32x4 ldsf4(AS3 const char* p) { return *(AS3 const f32x4*)p; }
DI u32x2 ldstr(AS3 const char* p) {
  return __builtin_bit_cast(u32x2, __builtin_amdgcn_ds_read_tr16_b64_v4i16((AS3 s16x4*)p));
}
DI u32x4 cat(u32x2 a, u32x2 b) { u32x4 r; r.x = a.x; r.y = a.y; r.z = b.x; r.w = b.y; return r; }
DI f32x16 mfma32(u32x4 a, u32x4 b, f32x16 c) {
  return __builtin_amdgcn_mfma_f32_32x32x16_bf16(__builtin_bit_cast(bf16x8, a), __builtin_bit_cast(bf16x8, b), c, 0, 0, 0);
}
DI f32x4 mfma16(u32x4 a, u32x4 b, f32x4 c) {
  return __builtin_amdgcn_mfma_f32_16x16x32_bf16(__builtin_bit_cast(bf16x8, a), __builtin_bit_cast(bf16x8, b), c, 0, 0, 0);
}
DI u32 pk(float a, float b) {
  f32x2 v = {a, b};
  return __builtin_bit_cast(u32, __builtin_convertvector(v, bf16x2));
}
DI float dot2(u32 a, u32 b, float c) {
  return __builtin_amdgcn_fdot2_f32_bf16(__builtin_bit_cast(bf16x2, a), __builtin_bit_cast(bf16x2, b), c, false);
}
DI float asf(u32 v) { return __builtin_bit_cast(float, v); }
DI float blo(u32 v) { return __builtin_bit_cast(float, v << 16); }
DI float bhi(u32 v) { return __builtin_bit_cast(float, v & 0xffff0000u); }
template <int CTRL> DI float dpp_f(float v) { return asf((u32)__builtin_amdgcn_update_dpp(0, (int)__builtin_bit_cast(u32, v), CTRL, 0xf, 0xf, true)); }
DI float row16_max(float v) {
  v = fmaxf(v, dpp_f<0x128>(v)); v = fmaxf(v, dpp_f<0x124>(v)); v = fmaxf(v, dpp_f<0x122>(v)); return fmaxf(v, dpp_f<0x121>(v));
}
DI float xor32_add(float v) { const auto r = __builtin_amdgcn_permlane32_swap(__builtin_bit_cast(u32, v), __builtin_bit_cast(u32, v), false, false); return asf(r[0]) + asf(r[1]); }
DI float xor16_add(float v) { const auto r = __builtin_amdgcn_permlane16_swap(__builtin_bit_cast(u32, v), __builtin_bit_cast(u32, v), false, false); return asf(r[0]) + asf(r[1]); }
DI float xor32_max(float v) { const auto r = __builtin_amdgcn_permlane32_swap(__builtin_bit_cast(u32, v), __builtin_bit_cast(u32, v), false, false); return fmaxf(asf(r[0]), asf(r[1])); }
DI float xor16_max(float v) { const auto r = __builtin_amdgcn_permlane16_swap(__builtin_bit_cast(u32, v), __builtin_bit_cast(u32, v), false, false); return fmaxf(asf(r[0]), asf(r[1])); }
DI float lo32(float v) { const auto r = __builtin_amdgcn_permlane32_swap(__builtin_bit_cast(u32, v), __builtin_bit_cast(u32, v), false, false); return asf(r[0]); }
DI int wave_id() { return __builtin_amdgcn_readfirstlane(threadIdx.x >> 6); }
constexpr u32 ONES2 = 0x3F803F80u;

#define PROF_BEGIN
#define PROF_AT(j)
#define PROF_END(kid)

template <bool B> struct BoolC { static constexpr bool value = B; };
constexpr int K1_PF = 4;
constexpr int K1_LDS = 65536 + 65536 + 16384 + 512;
__global__ __launch_bounds__(320) void k1_qkv(const u16* __restrict__ x, const u16* __restrict__ wq, const u16* __restrict__ wk,
                                              const u16* __restrict__ wv, char* __restrict__ ws) {
  extern __shared__ __attribute__((aligned(16))) char smem[];
  AS3 char* lds = (AS3 char*)smem;
  PROF_BEGIN
  const int l = threadIdx.x & 63, w = wave_id();
  const int b = blockIdx.x, xcd = b & 7, i = b >> 3, tt = xcd * 4 + (i & 3), cg = i >> 2;
  const u32 lb = (u32)(uintptr_t)lds;
  const int lr = l >> 3, lc = l & 7;
  const bool kvw = (w == 4) && (cg < 4);
  if (w < 4) {
    const u32 vx0 = lr * 1024 + ((lc ^ ((lr >> 1) & 7)) << 4);
    const u32 vx1 = lr * 1024 + ((lc ^ ((4 + (lr >> 1)) & 7)) << 4);
    const u32 vw = lr * 1024 + ((lc ^ (((lr >> 1) & 1) << 2)) << 4);
    const char* xb = (const char*)x + (size_t)(tt * 64 + 16 * w) * 1024;
    const char* wb = (const char*)wq + (size_t)(32 * w) * 1024 + cg * 128;
#pragma unroll
    for (int s = 0; s < 4; s++) {
#pragma unroll
      for (int j = 0; j < 4; j++) {
        const int kb = 2 * s + (j >> 1);
        dma16<0>(lb + kb * 8192 + (2 * w + (j & 1)) * 1024, xb + (j & 1) * 8192 + kb * 128, (j & 1) ? vx1 : vx0);
      }
#pragma unroll
      for (int j = 0; j < 4; j++)
        dma16<0>(lb + 65536 + (16 * s + 4 * w + j) * 1024, wb + (size_t)(128 * s + 8 * j) * 1024, vw);
    }
  } else if (kvw) {
    const char* src = (const char*)((cg & 1) ? wv : wk);
#pragma unroll
    for (int s = 0; s < 4; s++) dma16x4(lb + 131072 + s * 4096, src + s * 4096, l * 16);
  }
  const int r = l & 31, h = l >> 5;
  const int th = (w < 4) ? (w & 1) : ((cg >> 1) & 1);
  const int blk = (l >> 4) & 1, q = (l & 15) >> 2, p = l & 3;
  AS3 const char* xa[4];
#pragma unroll
  for (int k4 = 0; k4 < 4; k4++) xa[k4] = lds + (32 * th + r) * 128 + (((2 * k4 + h) ^ ((r >> 1) & 7)) << 4);
  f32x16 acc = {}, accs = {};
  float s1 = 0.f, s2 = 0.f;
  const u32x4 ones = {ONES2, ONES2, ONES2, ONES2};
  if (w < 4) {
    const int hp = w >> 1;
    AS3 const char* wa = lds + 65536 + (8 * h + q) * 128 + (((4 * hp + 2 * blk + (p >> 1)) ^ (((q >> 1) & 1) << 2)) << 4) + 8 * (p & 1);
    u32x4 bfr[K1_PF], afr[K1_PF];
    auto rd = [&](const int ks) {
      bfr[ks % K1_PF] = lds128(xa[ks & 3] + (ks >> 2) * 8192);
      afr[ks % K1_PF] = cat(ldstr(wa + ks * 2048), ldstr(wa + ks * 2048 + 512));
    };
    wait_bar<24>();
    sched0();
#pragma unroll
    for (int ks = 0; ks < K1_PF; ks++) rd(ks);
#pragma unroll
    for (int ks = 0; ks < 32; ks++) {
      sched0();
      const u32x4 bf = bfr[ks % K1_PF], af = afr[ks % K1_PF];
      acc = mfma32(af, bf, acc);
      accs = mfma32(af, ones, accs);
      s1 = dot2(bf.x, ONES2, s1); s2 = dot2(bf.x, bf.x, s2);
      s1 = dot2(bf.y, ONES2, s1); s2 = dot2(bf.y, bf.y, s2);
      s1 = dot2(bf.z, ONES2, s1); s2 = dot2(bf.z, bf.z, s2);
      s1 = dot2(bf.w, ONES2, s1); s2 = dot2(bf.w, bf.w, s2);
      sched0();
      if (ks + K1_PF == 8) wait_bar<16>(); else if (ks + K1_PF == 16) wait_bar<8>(); else if (ks + K1_PF == 24) wait_bar<0>();
      if (ks + K1_PF < 32) rd(ks + K1_PF);
    }
    sched0();
    PROF_AT(2)
    s1 = xor32_add(s1); s2 = xor32_add(s2);
    const float mu = s1 * (1.f / 512.f);
    const float var = fmaxf(s2 * (1.f / 512.f) - mu * mu, 0.f);
    const float sc = __builtin_amdgcn_rsqf(var + 1e-5f) * (0.25f * 1.4426950408889634f);
#pragma unroll
    for (int hh = 0; hh < 2; hh++) {
      u32x4 o;
      o.x = pk((acc[8 * hh + 0] - mu * accs[8 * hh + 0]) * sc, (acc[8 * hh + 1] - mu * accs[8 * hh + 1]) * sc);
      o.y = pk((acc[8 * hh + 2] - mu * accs[8 * hh + 2]) * sc, (acc[8 * hh + 3] - mu * accs[8 * hh + 3]) * sc);
      o.z = pk((acc[8 * hh + 4] - mu * accs[8 * hh + 4]) * sc, (acc[8 * hh + 5] - mu * accs[8 * hh + 5]) * sc);
      o.w = pk((acc[8 * hh + 6] - mu * accs[8 * hh + 6]) * sc, (acc[8 * hh + 7] - mu * accs[8 * hh + 7]) * sc);
      const int head = 4 * cg + 2 * hp + hh;
      *(u32x4*)(ws + WS_Q + ((size_t)((2 * tt + th) * 32 + head) * 64 + l) * 16) = o;
    }
  } else {
    const bool isV = cg & 1;
    AS3 const char* wa = lds + 131072 + (8 * h + q) * 32 + p * 8;
    auto chain = [&](auto isv) {
      u32x4 bfr[K1_PF], afr[K1_PF];
      auto rd = [&](const int ks) {
        bfr[ks % K1_PF] = lds128(xa[ks & 3] + (ks >> 2) * 8192);
        afr[ks % K1_PF] = cat(ldstr(wa + ks * 512), ldstr(wa + ks * 512 + 128));
      };
      wait_bar<12>();
      sched0();
#pragma unroll
      for (int ks = 0; ks < K1_PF; ks++) rd(ks);
#pragma unroll
      for (int ks = 0; ks < 32; ks++) {
        sched0();
        const u32x4 bf = bfr[ks % K1_PF], af = afr[ks % K1_PF];
        if (decltype(isv)::value) { acc = mfma32(bf, af, acc); accs = mfma32(ones, af, accs); }
        else { acc = mfma32(af, bf, acc); accs = mfma32(af, ones, accs); }
        s1 = dot2(bf.x, ONES2, s1); s2 = dot2(bf.x, bf.x, s2);
        s1 = dot2(bf.y, ONES2, s1); s2 = dot2(bf.y, bf.y, s2);
        s1 = dot2(bf.z, ONES2, s1); s2 = dot2(bf.z, bf.z, s2);
        s1 = dot2(bf.w, ONES2, s1); s2 = dot2(bf.w, bf.w, s2);
        sched0();
        if (ks + K1_PF == 8) wait_bar<8>(); else if (ks + K1_PF == 16) wait_bar<4>(); else if (ks + K1_PF == 24) wait_bar<0>();
        if (ks + K1_PF < 32) rd(ks + K1_PF);
      }
      sched0();
    };
    if (!kvw) { bar(); bar(); bar(); bar(); }
    else if (isV) chain(BoolC<true>{});
    else chain(BoolC<false>{});
    if (kvw) {
      s1 = xor32_add(s1); s2 = xor32_add(s2);
      const float mu = s1 * (1.f / 512.f);
      const float var = fmaxf(s2 * (1.f / 512.f) - mu * mu, 0.f);
      const float rs = __builtin_amdgcn_rsqf(var + 1e-5f);
      const int T = 2 * tt + th;
      if (!isV) {
        float kv[8], n2 = 0.f;
#pragma unroll
        for (int j = 0; j < 8; j++) { kv[j] = (acc[j] - mu * accs[j]) * rs; n2 += kv[j] * kv[j]; }
        u32x4 o = {pk(kv[0], kv[1]), pk(kv[2], kv[3]), pk(kv[4], kv[5]), pk(kv[6], kv[7])};
        const int kap = (r & 0x13) | ((r & 4) << 1) | ((r & 8) >> 1);
        *(u32x4*)(ws + WS_K + ((size_t)T * 64 + h * 32 + kap) * 16) = o;
        n2 = xor16_max(row16_max(xor32_add(n2)));
        if (l == 0) ((float*)(ws + WS_KM))[T] = n2;
      } else {
        AS3 f32x2* st = (AS3 f32x2*)(lds + 147456);
        if (h == 0) { f32x2 v = {mu, rs}; st[r] = v; }
        float vv[16];
#pragma unroll
        for (int j = 0; j < 16; j++) {
          const f32x2 m = st[(j & 3) + 8 * (j >> 2) + 4 * h];
          vv[j] = (acc[j] - m.x * accs[0]) * m.y;
        }
        if (r <= 16) {
#pragma unroll
          for (int s = 0; s < 2; s++) {
            u32x4 o = {pk(vv[8 * s], vv[8 * s + 1]), pk(vv[8 * s + 2], vv[8 * s + 3]), pk(vv[8 * s + 4], vv[8 * s + 5]), pk(vv[8 * s + 6], vv[8 * s + 7])};
            if (r == 16) o = ones;
            *(u32x4*)(ws + WS_V + (size_t)T * 1088 + s * 544 + h * 272 + r * 16) = o;
          }
        }
      }
    }
  }
  PROF_END(0)
}

constexpr int K2_V = 65536, K2_STG = 135168, K2_JUNK = K2_STG + 16384, K2_LDS = K2_JUNK + 256;
constexpr float SAFE_LOG2 = 60.f;

template <bool SUBMAX>
DI void attn_tiles(AS3 const char* ka, AS3 const char* va, const u32x4 qf, const float negm, f32x16& o) {
#pragma unroll
  for (int u = 0; u < 4; u++) {
    const u32x4 kf = lds128(ka + u * 1024);
    const u32x4 v0 = lds128(va + u * 1088), v1 = lds128(va + u * 1088 + 544);
    f32x16 c;
#pragma unroll
    for (int j = 0; j < 16; j++) c[j] = SUBMAX ? negm : 0.f;
    f32x16 s = mfma32(kf, qf, c);
#pragma unroll
    for (int j = 0; j < 16; j++) s[j] = __builtin_amdgcn_exp2f(s[j]);
    const f32x8 p0 = __builtin_shufflevector(s, s, 0, 1, 2, 3, 4, 5, 6, 7);
    const f32x8 p1 = __builtin_shufflevector(s, s, 8, 9, 10, 11, 12, 13, 14, 15);
    o = __builtin_amdgcn_smfmac_f32_32x32x32_bf16(__builtin_bit_cast(bf16x8, v0), __builtin_bit_cast(bf16x16, p0), o, (int)0xDDDDDDDD, 0, 0);
    o = __builtin_amdgcn_smfmac_f32_32x32x32_bf16(__builtin_bit_cast(bf16x8, v1), __builtin_bit_cast(bf16x16, p1), o, (int)0xDDDDDDDD, 0, 0);
  }
}

#define AE(X, j) "v_exp_f32 v[" #X "+" #j "], v[" #X "+" #j "]\n\t"
#define ATILE(X, Y, KO, V0O, V1O)                                                               \
  AE(X, 0) AE(X, 1)                                                                              \
  "s_waitcnt lgkmcnt(2)\n\t"                                                                     \
  "v_smfmac_f32_32x32x32_bf16 v[0:15], v[60:63], v[" #Y "+8:" #Y "+15], v64\n\t"                 \
  "ds_read_b128 v[60:63], v66 offset:" #V1O "\n\t"                                               \
  AE(X, 2) AE(X, 3) AE(X, 4) AE(X, 5)                                                            \
  "s_waitcnt lgkmcnt(2)\n\t"                                                                     \
  "v_mfma_f32_32x32x16_bf16 v[" #Y ":" #Y "+15], v[52:55], v[48:51], 0\n\t"                      \
  "ds_read_b128 v[52:55], v65 offset:" #KO "\n\t"                                                \
  AE(X, 6) AE(X, 7) AE(X, 8) AE(X, 9)                                                            \
  "s_waitcnt lgkmcnt(2)\n\t"                                                                     \
  "v_smfmac_f32_32x32x32_bf16 v[0:15], v[56:59], v[" #X ":" #X "+7], v64\n\t"                    \
  "ds_read_b128 v[56:59], v66 offset:" #V0O "\n\t"                                               \
  AE(X, 10) AE(X, 11) AE(X, 12) AE(X, 13) AE(X, 14) AE(X, 15)
#define ALOOP                                                                                    \
  "1:\n\t" ATILE(16, 32, 2048, 1088, 544) ATILE(32, 16, 3072, 2176, 1632)                        \
  ATILE(16, 32, 4096, 3264, 2720) ATILE(32, 16, 5120, 4352, 3808)                                \
  "v_add_u32 v65, 0x1000, v65\n\tv_add_u32 v66, 0x1100, v66\n\t"                                 \
  "s_sub_u32 %[n], %[n], 1\n\ts_cmp_lg_u32 %[n], 0\n\ts_cbranch_scc1 1b\n\t"
#define AREGS "+{v[0:15]}"(o), "+{v[16:31]}"(sa), "+{v[32:47]}"(sb), "+{v[52:55]}"(kf), "+{v[56:59]}"(v0), "+{v[60:63]}"(v1), "+{v65}"(ka), "+{v66}"(va), [n] "+s"(n)
#define AINS "{v[48:51]}"(qf), "{v64}"(0xDDDDDDDDu)

__global__ __launch_bounds__(512) void k2_attn(const u16* __restrict__ wg, const u16* __restrict__ wu, const u16* __restrict__ wo,
                                               char* __restrict__ ws) {
  extern __shared__ __attribute__((aligned(16))) char smem[];
  AS3 char* lds = (AS3 char*)smem;
  PROF_BEGIN
  const int l = threadIdx.x & 63, w = wave_id();
  const int b = blockIdx.x, xcd = b & 7, i = b >> 3, tt = xcd * 4 + (i & 3), hg = i >> 2;
  const u32 lb = (u32)(uintptr_t)lds;
  const int th = w & 1, head = 4 * hg + (w >> 1);
  const int r = l & 31, h = l >> 5;
  u32x4 qf; u32x2 kmx;
  gld16<0>(qf, ws + WS_Q + (size_t)((2 * tt + th) * 32 + head) * 1024, l * 16);
  gld8<0>(kmx, ws + WS_KM, (l & 31) * 8);
  const bool side = b < 224;
  const int cgp = b % 28, kc = b / 28;
  {
    const u32 vo = l * 16;
    const char *kb = ws + WS_K, *vb = ws + WS_V;
    dma16<0>(lb + w * 1024, kb + w * 1024, vo);
    dma16<0>(lb + K2_V + w * 1024, vb + w * 1024, vo);
    const int p1 = (8 + 3 * w) * 1024;
    dma16<0>(lb + p1, kb + p1, vo); dma16<1024>(lb + p1, kb + p1, vo); dma16<2048>(lb + p1, kb + p1, vo);
    dma16<0>(lb + K2_V + p1, vb + p1, vo); dma16<1024>(lb + K2_V + p1, vb + p1, vo); dma16<2048>(lb + K2_V + p1, vb + p1, vo);
    {
      const int kk = side ? kc : 0;
      const size_t ro = (size_t)(64 * kk + 8 * w) * 3584 + cgp * 128;
      const u32 vs = (l >> 3) * 3584 + (l & 7) * 16;
      dma16<0>(lb + K2_STG + w * 1024, (const char*)wg + ro, vs);
      dma16<0>(lb + K2_STG + 8192 + w * 1024, (const char*)wu + ro, vs);
    }
    const int p2 = (32 + 4 * w) * 1024;
    dma16x4(lb + p2, kb + p2, vo);
    dma16x4(lb + K2_V + p2, vb + p2, vo);
    if (w < 4) dma16<0>(lb + K2_V + (64 + w) * 1024, vb + (64 + w) * 1024, vo);
  }
  asm volatile("s_waitcnt vmcnt(18)" : "+v"(qf), "+v"(kmx)::"memory");
  const float km = xor16_max(row16_max(fmaxf(asf(kmx.x), asf(kmx.y))));
  float q2 = dot2(qf.x, qf.x, 0.f); q2 = dot2(qf.y, qf.y, q2); q2 = dot2(qf.z, qf.z, q2); q2 = dot2(qf.w, qf.w, q2);
  q2 = xor32_add(q2);
  const bool unsafe = __any(!(q2 * km * 1.05f <= SAFE_LOG2 * SAFE_LOG2));

  u32 ka = lb + l * 16;
  u32 va = lb + K2_V + h * 272 + (r < 16 ? r * 16 : (r < 20 ? 256 : (r - 16) * 16));
  f32x16 o = {};
  auto side_job = [&]() {
    if (side && w < 4) {
      const int blk = (l >> 4) & 1, q = (l & 15) >> 2, p = l & 3;
      AS3 const char* sp = lds + K2_STG + blk * 8192 + (8 * h + q) * 128 + (2 * w + (p >> 1)) * 16 + 8 * (p & 1);
      float cs = 0.f;
      char* dst = ws + WS_WT + ((size_t)(4 * cgp + w) * 32 + 4 * kc) * 1024 + l * 16;
#pragma unroll
      for (int ks = 0; ks < 4; ks++) {
        const u32x4 f = cat(ldstr(sp + ks * 2048), ldstr(sp + ks * 2048 + 512));
        cs = dot2(f.x, ONES2, cs); cs = dot2(f.y, ONES2, cs); cs = dot2(f.z, ONES2, cs); cs = dot2(f.w, ONES2, cs);
        asm volatile("global_store_dwordx4 %0, %1, off sc1\n\ts_nop 1" ::"v"(dst + ks * 1024), "v"(f) : "memory");
      }
      cs = xor32_add(cs);
      if (h == 0) ((float*)(ws + WS_CS))[((4 * cgp + w) * 32 + r) * 8 + kc] = cs;
    }
  };
  auto prefetch = [&]() {
    const char* src = (w < 7) ? (ws + WS_WT + ((size_t)(xcd & 1) * 224 + i * 7 + w) * 8192) : ((const char*)wo + (size_t)i * 16384);
    touch_lines(lb + K2_JUNK, src, l * 128);
    if (w == 7) touch_lines(lb + K2_JUNK, src + 8192, l * 128);
  };
  if (!unsafe) {
    f32x16 sa = {}, sb = {};
    u32x4 kf = {}, v0 = {}, v1 = {};
    u32 n = 1;
    asm volatile("s_waitcnt vmcnt(16)\n\ts_barrier\n\t"
                 "ds_read_b128 v[52:55], v65\n\ts_waitcnt lgkmcnt(0)\n\t"
                 "v_mfma_f32_32x32x16_bf16 v[16:31], v[52:55], v[48:51], 0\n\t"
                 "ds_read_b128 v[52:55], v65 offset:1024\n\tds_read_b128 v[56:59], v66\n\ts_nop 7\n\ts_nop 2\n\t"
                 ALOOP "s_waitcnt vmcnt(8) lgkmcnt(0)\n\ts_nop 7\n\ts_barrier"
                 : AREGS : AINS : "memory", "scc");
    PROF_AT(1)
    side_job();
    n = 6;
    asm volatile(ALOOP "s_waitcnt vmcnt(0) lgkmcnt(0)\n\ts_nop 7\n\ts_barrier" : AREGS : AINS : "memory", "scc");
    PROF_AT(2)
    prefetch();
    n = 9;
    asm volatile(ALOOP "s_waitcnt lgkmcnt(0)\n\t"
                 "v_smfmac_f32_32x32x32_bf16 v[0:15], v[60:63], v[40:47], v64\n\ts_nop 15" : AREGS : AINS : "memory", "scc");
    PROF_AT(3)
  } else {
    wait_bar<16>();
    wait_bar<8>();
    side_job();
    wait_bar<0>();
    prefetch();
    AS3 const char* kp = lds + l * 16;
    AS3 const char* vp = lds + (va - lb);
    float m = -3.0e38f;
#pragma unroll 1
    for (int t = 0; t < 64; t++) {
      const f32x16 z = {};
      const f32x16 s = mfma32(lds128(kp + t * 1024), qf, z);
#pragma unroll
      for (int j = 0; j < 16; j++) m = fmaxf(m, s[j]);
    }
    m = xor32_max(m);
#pragma unroll 1
    for (int it = 0; it < 16; it++) {
      attn_tiles<true>(kp, vp, qf, -m, o);
      kp += 4096; vp += 4352;
    }
  }
  const float inv = __builtin_amdgcn_rcpf(lo32(o[8]));
  const u32x4 res = {pk(o[0] * inv, o[1] * inv), pk(o[2] * inv, o[3] * inv), pk(o[4] * inv, o[5] * inv), pk(o[6] * inv, o[7] * inv)};
  const int t16 = 4 * tt + 2 * th + (r >> 4);
  *(u32x4*)(ws + WS_O + ((size_t)(t16 * 16 + (head >> 1)) * 64 + (2 * (head & 1) + h) * 16 + (r & 15)) * 16) = res;
  wait_vm<0>();
  PROF_END(1)
}

template <int NST, int RING, int AHEAD, bool IS_K3>
DI void gemm64(const char* __restrict__ act, const u16* __restrict__ wgt, const u16* __restrict__ xres, char* __restrict__ ws,
               u16* __restrict__ out, AS3 char* lds, const int tt, const int cg) {
  PROF_BEGIN
  const int l = threadIdx.x & 63, w8 = wave_id(), w = w8 & 3, kg = w8 >> 2;
  const u32 lb = (u32)(uintptr_t)lds + kg * (RING * 16384);
  const int lr = l >> 3, lc = l & 7;
  const int g = l >> 4, q = (l & 15) >> 2, p = l & 3, r16 = l & 15;
  constexpr int NB = (AHEAD + 1 < NST ? AHEAD + 1 : NST);
  u32x4 bf[NB * 4];
  u32x2 res[4] = {};
  const int tok = tt * 64 + 16 * w + r16;
  if (kg == 0) {
    if (IS_K3) {
      const char* xb = (const char*)xres + (size_t)(tt * 64 + 16 * w) * 1024 + cg * 128;
      const u32 vo = r16 * 1024 + g * 8;
      gld8<0>(res[0], xb, vo); gld8<32>(res[1], xb, vo); gld8<64>(res[2], xb, vo); gld8<96>(res[3], xb, vo);
    } else {
      const char* hb = ws + WS_H + (size_t)((tt * 2 + (w >> 1)) * 32 + cg * 4) * 1024;
      const u32 vo = ((g >> 1) * 32 + (w & 1) * 16 + r16) * 16 + (g & 1) * 8;
      gld8<0>(res[0], hb, vo); gld8<1024>(res[1], hb, vo); gld8<2048>(res[2], hb, vo); gld8<3072>(res[3], hb, vo);
    }
  }
  const char* ab = act + (size_t)(tt * 4 + w) * (2 * NST * 4096) + kg * (NST * 4096);
  const char* wb = (const char*)wgt + (size_t)(kg * NST * 128 + 32 * w) * 1024 + cg * 128;
  const u32 vw = lr * 1024 + ((lc ^ (((lr >> 1) & 3) << 1)) << 4);
  const u32 va = l * 16;
  auto issue = [&](const int s) {
    const u32 dst = lb + (s % RING) * 16384 + w * 4096;
    const char* src = wb + (size_t)s * 131072;
    dma16<0>(dst, src, vw); dma16<0>(dst + 1024, src + 8192, vw); dma16<0>(dst + 2048, src + 16384, vw); dma16<0>(dst + 3072, src + 24576, vw);
    const char* as = ab + s * 4096;
    const int o = (s % NB) * 4;
    gld16<0>(bf[o], as, va); gld16<1024>(bf[o + 1], as, va); gld16<2048>(bf[o + 2], as, va); gld16<3072>(bf[o + 3], as, va);
  };
#pragma unroll
  for (int s = 0; s < AHEAD && s < NST; s++) issue(s);
  const int swz = (2 * (g & 1) + (q >> 1)) << 1;
  AS3 const char* wa[4];
#pragma unroll
  for (int c = 0; c < 4; c++)
    wa[c] = lds + kg * (RING * 16384) + (16 * (g >> 1) + 4 * (g & 1) + q) * 128 + (((2 * c) ^ swz) + (p >> 1)) * 16 + 8 * (p & 1);
  f32x4 acc[4] = {};
#pragma unroll
  for (int s = 0; s < NST; s++) {
    const int last = (s + AHEAD - 1 < NST - 1) ? (s + AHEAD - 1) : (NST - 1);
    const int o = (s % NB) * 4;
    sched0();
    if (last - s == 0) wait_bar4<0>(bf[o], bf[o + 1], bf[o + 2], bf[o + 3]);
    else if (last - s == 1) wait_bar4<8>(bf[o], bf[o + 1], bf[o + 2], bf[o + 3]);
    else if (last - s == 2) wait_bar4<16>(bf[o], bf[o + 1], bf[o + 2], bf[o + 3]);
    else wait_bar4<24>(bf[o], bf[o + 1], bf[o + 2], bf[o + 3]);
    sched0();
    if (s == 0) { PROF_AT(1) }
    const int so = (s % RING) * 16384;
    u32x4 af[4][2];
    auto rd = [&](const int u) {
      const int ks = u >> 1, c = (u & 1) * 2;
      af[u & 3][0] = cat(ldstr(wa[c] + so + ks * 4096), ldstr(wa[c] + so + ks * 4096 + 1024));
      af[u & 3][1] = cat(ldstr(wa[c + 1] + so + ks * 4096), ldstr(wa[c + 1] + so + ks * 4096 + 1024));
    };
    rd(0); rd(1); rd(2);
    sched0();
    if (s + AHEAD < NST) issue(s + AHEAD);
#pragma unroll
    for (int u = 0; u < 8; u++) {
      sched0();
      const int ks = u >> 1, c = (u & 1) * 2;
      acc[c] = mfma16(af[u & 3][0], bf[o + ks], acc[c]);
      acc[c + 1] = mfma16(af[u & 3][1], bf[o + ks], acc[c + 1]);
      sched0();
      if (u + 3 < 8) rd(u + 3);
    }
  }
  sched0();
  PROF_AT(2)
  AS3 char* red = lds + (w * 4) * 1024 + l * 16;
  asm volatile("s_waitcnt lgkmcnt(0)\n\ts_barrier" ::: "memory");
  if (kg == 1) {
#pragma unroll
    for (int c = 0; c < 4; c++) *(AS3 f32x4*)(red + c * 1024) = acc[c];
    asm volatile("s_waitcnt lgkmcnt(0)\n\ts_barrier" ::: "memory");
  } else {
    asm volatile("s_barrier" : "+v"(res[0]), "+v"(res[1]), "+v"(res[2]), "+v"(res[3])::"memory");
#pragma unroll
    for (int c = 0; c < 4; c++) acc[c] += ldsf4(red + c * 1024);
    if (IS_K3) {
      float s1 = 0.f, s2 = 0.f;
      char* hb = ws + WS_H + (size_t)((tt * 2 + (w >> 1)) * 32 + cg * 4) * 1024 + ((g >> 1) * 32 + (w & 1) * 16 + r16) * 16 + (g & 1) * 8;
#pragma unroll
      for (int c = 0; c < 4; c++) {
        u32x2 hv;
        hv.x = pk(blo(res[c].x) + acc[c][0], bhi(res[c].x) + acc[c][1]);
        hv.y = pk(blo(res[c].y) + acc[c][2], bhi(res[c].y) + acc[c][3]);
        s1 = dot2(hv.x, ONES2, s1); s1 = dot2(hv.y, ONES2, s1);
        s2 = dot2(hv.x, hv.x, s2); s2 = dot2(hv.y, hv.y, s2);
        *(u32x2*)(hb + c * 1024) = hv;
      }
      s1 = xor32_add(xor16_add(s1)); s2 = xor32_add(xor16_add(s2));
      if (g == 0) { f32x2 v = {s1, s2}; *(f32x2*)(ws + WS_ST + ((size_t)tok * 8 + cg) * 8) = v; }
    } else {
      u16* ob = out + (size_t)tok * 512 + cg * 64 + 4 * g;
#pragma unroll
      for (int c = 0; c < 4; c++) {
        u32x2 ov;
        ov.x = pk(blo(res[c].x) + acc[c][0], bhi(res[c].x) + acc[c][1]);
        ov.y = pk(blo(res[c].y) + acc[c][2], bhi(res[c].y) + acc[c][3]);
        *(u32x2*)(ob + 16 * c) = ov;
      }
    }
  }
  PROF_END(IS_K3 ? 2 : 4)
}

constexpr int K3_LDS = 2 * 2 * 16384;
__global__ __launch_bounds__(512) void k3_oproj(const u16* __restrict__ x, const u16* __restrict__ wo, char* __restrict__ ws) {
  extern __shared__ __attribute__((aligned(16))) char smem[];
  const int b = blockIdx.x, xcd = b & 7, i = b >> 3;
  gemm64<2, 2, 2, true>(ws + WS_O, wo, x, ws, nullptr, (AS3 char*)smem, xcd * 4 + (i & 3), i >> 2);
}
constexpr int K5_RING = 4, K5_LDS = 2 * K5_RING * 16384;
__global__ __launch_bounds__(512) void k5_down(const u16* __restrict__ wd, char* __restrict__ ws, u16* __restrict__ out) {
  extern __shared__ __attribute__((aligned(16))) char smem[];
  const int b = blockIdx.x, xcd = b & 7, i = b >> 3;
  gemm64<7, K5_RING, 3, false>(ws + WS_F, wd, nullptr, ws, out, (AS3 char*)smem, xcd * 4 + (i & 3), i >> 2);
}

constexpr int K4_RING = 9, K4_AHEAD = 6, K4_CS = K4_RING * 16384, K4_LDS = K4_CS + 1024, K4_PF = 8;
DI u32 geglu2(const f32x16& a, const int p, AS3 const char* cs, const float mu, const float rs) {
  const f32x2 cg = *(AS3 const f32x2*)(cs + (p >> 1) * 32 + (p & 1) * 8), cu = *(AS3 const f32x2*)(cs + 64 + (p >> 1) * 32 + (p & 1) * 8);
  constexpr float C1 = -2.f * 0.7978845608028654f * 1.4426950408889634f, C2 = C1 * 0.044715f;
  const f32x2 ag = {a[2 * p], a[2 * p + 1]}, au = {a[8 + 2 * p], a[9 + 2 * p]};
  const f32x2 gv = (ag - mu * cg) * rs, uv = (au - mu * cu) * rs;
  const f32x2 z = gv * (C2 * (gv * gv) + C1);
  const f32x2 e = {__builtin_amdgcn_exp2f(z.x), __builtin_amdgcn_exp2f(z.y)};
  const f32x2 d = e + 1.f;
  const f32x2 rc = {__builtin_amdgcn_rcpf(d.x), __builtin_amdgcn_rcpf(d.y)};
  const f32x2 o = gv * uv * rc;
  return pk(o.x, o.y);
}

__global__ __launch_bounds__(256) void k4_up(char* __restrict__ ws) {
  extern __shared__ __attribute__((aligned(16))) char smem[];
  AS3 char* lds = (AS3 char*)smem;
  PROF_BEGIN
  const int l = threadIdx.x & 63, w = wave_id();
  const int b = blockIdx.x, xcd = b & 7, i = b >> 3;
  const int tt = 4 * (xcd >> 1) + (i & 3), sl = 8 * (xcd & 1) + (i >> 2);
  const u32 lb = (u32)(uintptr_t)lds;
  const int r = l & 31, h = l >> 5;
  const int t32 = 4 * tt + w;
  u32x4 st[4], cp[2];
  {
    const char* sb = ws + WS_ST + (size_t)t32 * 32 * 64;
    gld16<0>(st[0], sb, r * 64); gld16<16>(st[1], sb, r * 64); gld16<32>(st[2], sb, r * 64); gld16<48>(st[3], sb, r * 64);
    const char* cb = ws + WS_CS + (size_t)(sl * 224 + 56 * w) * 32;
    const u32 co = (l < 56 ? l : 55) * 32;
    gld16<0>(cp[0], cb, co); gld16<16>(cp[1], cb, co);
  }
  u32x4 hf[32];
  const char* hb = ws + WS_H + (size_t)t32 * 32768;
  const char* wt = ws + WS_WT + (size_t)sl * 7 * 32768;
  const u32 va = l * 16;
  auto issue = [&](auto sc) {
    constexpr int s = decltype(sc)::value;
    const u32 dst = lb + (s % K4_RING) * 16384 + w * 4096;
    if constexpr (s < 8) {
      dma16x4(dst, wt + (size_t)w * 32768 + s * 4096, va);
      const char* hs = hb + s * 4096;
      gld16<0>(hf[4 * s], hs, va); gld16<1024>(hf[4 * s + 1], hs, va); gld16<2048>(hf[4 * s + 2], hs, va); gld16<3072>(hf[4 * s + 3], hs, va);
    } else {
      dma16x4(dst, wt + (size_t)(4 + ((s - 8) >> 1)) * 32768 + ((s - 8) & 1) * 16384 + w * 4096, va);
    }
  };
  auto arrive = [&](auto sc) {
    constexpr int s = decltype(sc)::value;
    constexpr int young = s <= 2 ? 40 : (s <= 7 ? 48 - 4 * s : 4 * (13 - s));
    if constexpr (s < 8) wait_bar4<young>(hf[4 * s], hf[4 * s + 1], hf[4 * s + 2], hf[4 * s + 3]);
    else wait_bar<young>();
    sched0();
    if constexpr (s + K4_AHEAD < 14) issue(IntC<s + K4_AHEAD>{});
  };
  sfor<0, K4_AHEAD>(issue);
  AS3 const char* wa = lds + l * 16;
  AS3 const char* csa = lds + K4_CS + h * 16;
  char* fo = ws + WS_F + ((size_t)(t32 * 2 + (r >> 4)) * 56 * 64 + h * 16 + (r & 15)) * 16;
  const int n0 = sl * 7;
  float mu, rs;
  f32x16 acc[7] = {};
  u32x4 af[K4_PF];
  auto rd = [&](auto Uc) {
    constexpr int U = decltype(Uc)::value, s = U >> 4, u = U & 15;
    af[U % K4_PF] = lds128(wa + (s % K4_RING) * 16384 + (s < 8 ? (u & 3) * 4096 + (u >> 2) * 1024 : u * 1024));
  };
  sched0();
  arrive(IntC<0>{});
  PROF_AT(1)
  sfor<0, K4_PF>(rd);
  {
    asm volatile("" : "+v"(st[0]), "+v"(st[1]), "+v"(st[2]), "+v"(st[3]), "+v"(cp[0]), "+v"(cp[1]));
    float s1 = 0.f, s2 = 0.f;
#pragma unroll
    for (int j = 0; j < 4; j++) {
      s1 += asf(st[j].x) + asf(st[j].z);
      s2 += asf(st[j].y) + asf(st[j].w);
    }
    mu = s1 * (1.f / 512.f);
    rs = __builtin_amdgcn_rsqf(fmaxf(s2 * (1.f / 512.f) - mu * mu, 0.f) + 1e-5f);
    float c = 0.f;
#pragma unroll
    for (int j = 0; j < 2; j++) c += (asf(cp[j].x) + asf(cp[j].y)) + (asf(cp[j].z) + asf(cp[j].w));
    if (l < 56) *(AS3 float*)(lds + K4_CS + (56 * w + l) * 4) = c;
  }
  u32x4 ob;
  sfor<0, 56>([&](auto Gc) {
    constexpr int G = decltype(Gc)::value;
    sched0();
    sfor<0, 4>([&](auto kc) {
      constexpr int U = 4 * G + decltype(kc)::value, s = U >> 4, u = U & 15;
      constexpr int t = s < 8 ? (u & 3) : 4 + ((s - 8) >> 1);
      constexpr int f = s < 8 ? 4 * s + (u >> 2) : 16 * ((s - 8) & 1) + u;
      acc[t] = mfma32(af[U % K4_PF], hf[f], acc[t]);
      constexpr int Un = U + K4_PF;
      if constexpr (Un < 224) {
        if constexpr ((Un & 15) == 0) { sched0(); arrive(IntC<(Un >> 4)>{}); }
        rd(IntC<Un>{});
      }
    });
    if constexpr (G >= 32) {
      constexpr int e = G - 32, t = e >> 2, p = e & 3;
      const u32 v = geglu2(acc[t], p, csa + t * 128, mu, rs);
      if (p == 0) ob.x = v; else if (p == 1) ob.y = v; else if (p == 2) ob.z = v; else ob.w = v;
      if (p == 3) { const int n = n0 + t; *(u32x4*)(fo + ((n >> 1) * 64 + (n & 1) * 32) * 16) = ob; }
#pragma unroll
      for (int k = 0; k < 4; k++) {
        __builtin_amdgcn_sched_group_barrier(0x008, 1, 0);
        __builtin_amdgcn_sched_group_barrier(0x100, 1, 0);
        __builtin_amdgcn_sched_group_barrier(0x002, 5, 0);
      }
    }
    if constexpr (G == 31) { PROF_AT(2) }
  });
  sched0();
  PROF_AT(3)
  {
    ob.x = geglu2(acc[6], 0, csa + 6 * 128, mu, rs); ob.y = geglu2(acc[6], 1, csa + 6 * 128, mu, rs);
    ob.z = geglu2(acc[6], 2, csa + 6 * 128, mu, rs); ob.w = geglu2(acc[6], 3, csa + 6 * 128, mu, rs);
    const int n = n0 + 6;
    *(u32x4*)(fo + ((n >> 1) * 64 + (n & 1) * 32) * 16) = ob;
  }
  PROF_END(3)
}

extern "C" void kernel_launch(void* const* d_in, const int* in_sizes, int n_in, void* d_out, int out_size, void* d_ws, size_t ws_size,
                              hipStream_t stream) {
  const u16 *x = (const u16*)d_in[0], *wq = (const u16*)d_in[1], *wk = (const u16*)d_in[2], *wv = (const u16*)d_in[3],
            *wo = (const u16*)d_in[4], *wg = (const u16*)d_in[6], *wu = (const u16*)d_in[7], *wd = (const u16*)d_in[8];
  char* ws = (char*)d_ws;
  (void)hipFuncSetAttribute((const void*)k1_qkv, hipFuncAttributeMaxDynamicSharedMemorySize, K1_LDS);
  (void)hipFuncSetAttribute((const void*)k2_attn, hipFuncAttributeMaxDynamicSharedMemorySize, K2_LDS);
  (void)hipFuncSetAttribute((const void*)k3_oproj, hipFuncAttributeMaxDynamicSharedMemorySize, K3_LDS);
  (void)hipFuncSetAttribute((const void*)k4_up, hipFuncAttributeMaxDynamicSharedMemorySize, K4_LDS);
  (void)hipFuncSetAttribute((const void*)k5_down, hipFuncAttributeMaxDynamicSharedMemorySize, K5_LDS);
  k1_qkv<<<256, 320, K1_LDS, stream>>>(x, wq, wk, wv, ws);
  k2_attn<<<256, 512, K2_LDS, stream>>>(wg, wu, wo, ws);
  k3_oproj<<<256, 512, K3_LDS, stream>>>(x, wo, ws);
  k4_up<<<256, 256, K4_LDS, stream>>>(ws);
  k5_down<<<256, 512, K5_LDS, stream>>>(wd, ws, (u16*)d_out);
}
```

```cpp
#include <hip/hip_runtime.h>
#include <stdint.h>

typedef unsigned int u32;
typedef unsigned short u16;
typedef unsigned long long u64;
typedef __bf16 bf16x2 __attribute__((ext_vector_type(2)));
typedef __bf16 bf16x8 __attribute__((ext_vector_type(8)));
typedef __bf16 bf16x16 __attribute__((ext_vector_type(16)));
typedef float f32x2 __attribute__((ext_vector_type(2)));
typedef float f32x4 __attribute__((ext_vector_type(4)));
typedef float f32x8 __attribute__((ext_vector_type(8)));
typedef float f32x16 __attribute__((ext_vector_type(16)));
typedef u32 u32x2 __attribute__((ext_vector_type(2)));
typedef u32 u32x4 __attribute__((ext_vector_type(4)));
typedef short s16x4 __attribute__((ext_vector_type(4)));

#define AS3 __attribute__((address_space(3)))
#define DI __device__ __forceinline__

constexpr int S_ = 2048, D_ = 512, F_ = 1792;

constexpr size_t WS_Q = 0;
constexpr size_t WS_O = WS_Q + (2u << 20);
constexpr size_t WS_H = WS_O + (2u << 20);
constexpr size_t WS_K = WS_H + (2u << 20);
constexpr size_t WS_V = WS_K + 65536;
constexpr size_t WS_KM = WS_V + 69632;
constexpr size_t WS_ST = WS_KM + 1024;
constexpr size_t WS_CS = WS_ST + 131072;
constexpr size_t WS_WT = (WS_CS + 114688 + 4095) / 4096 * 4096;
constexpr size_t WS_WD = WS_WT + 112 * 32768;
constexpr size_t WS_F = WS_WD + 16 * 112 * 1024;
constexpr size_t WS_END = WS_F + (size_t)64 * 112 * 1024;

template <int N> DI void wait_vm() { asm volatile("s_waitcnt vmcnt(%0)" ::"i"(N) : "memory"); }
DI void bar() { asm volatile("s_barrier" ::: "memory"); }
template <int N> DI void wait_bar4(u32x4& a, u32x4& b, u32x4& c, u32x4& d) {
  asm volatile("s_waitcnt vmcnt(%4)\n\ts_barrier" : "+v"(a), "+v"(b), "+v"(c), "+v"(d) : "i"(N) : "memory");
}
template <int N> DI void wait_bar() { asm volatile("s_waitcnt vmcnt(%0)\n\ts_barrier" ::"i"(N) : "memory"); }

template <int V> struct IntC { static constexpr int value = V; };
template <int I, int N, class F> DI void sfor(F&& f) { if constexpr (I < N) { f(IntC<I>{}); sfor<I + 1, N>(f); } }
DI void sched0() { __builtin_amdgcn_sched_barrier(0); }

template <int IMM> DI void dma16(u32 lds, const void* sbase, u32 voff) {
  asm volatile("s_mov_b32 m0, %0\n\ts_nop 0\n\tglobal_load_lds_dwordx4 %1, %2 offset:%3" ::"s"(lds), "v"(voff), "s"(sbase), "i"(IMM) : "memory");
}
DI void dma16x4(u32 lds, const void* sbase, u32 voff) {
  asm volatile("s_mov_b32 m0, %0\n\ts_nop 0\n\tglobal_load_lds_dwordx4 %1, %2\n\tglobal_load_lds_dwordx4 %1, %2 offset:1024\n\t"
               "global_load_lds_dwordx4 %1, %2 offset:2048\n\tglobal_load_lds_dwordx4 %1, %2 offset:3072" ::"s"(lds), "v"(voff), "s"(sbase) : "memory");
}
DI void touch_lines(u32 lds, const void* sbase, u32 voff) {
  asm volatile("s_mov_b32 m0, %0\n\ts_nop 0\n\tglobal_load_lds_dword %1, %2" ::"s"(lds), "v"(voff), "s"(sbase) : "memory");
}
template <int IMM> DI void gld16(u32x4& d, const void* sbase, u32 voff) {
  asm volatile("global_load_dwordx4 %0, %1, %2 offset:%3" : "=v"(d) : "v"(voff), "s"(sbase), "i"(IMM) : "memory");
}
template <int IMM> DI void gld8(u32x2& d, const void* sbase, u32 voff) {
  asm volatile("global_load_dwordx2 %0, %1, %2 offset:%3" : "=v"(d) : "v"(voff), "s"(sbase), "i"(IMM) : "memory");
}
DI void pin(u32x4& a) { asm volatile("" : "+v"(a)); }
DI void pin(u32x2& a) { asm volatile("" : "+v"(a)); }

DI u32x4 lds128(AS3 const char* p) { return *(AS3 const u32x4*)p; }
DI f32x4 ldsf4(AS3 const char* p) { return *(AS3 const f32x4*)p; }
DI u32x2 ldstr(AS3 const char* p) {
  return __builtin_bit_cast(u32x2, __builtin_amdgcn_ds_read_tr16_b64_v4i16((AS3 s16x4*)p));
}
DI u32x4 cat(u32x2 a, u32x2 b) { u32x4 r; r.x = a.x; r.y = a.y; r.z = b.x; r.w = b.y; return r; }
DI f32x16 mfma32(u32x4 a, u32x4 b, f32x16 c) {
  return __builtin_amdgcn_mfma_f32_32x32x16_bf16(__builtin_bit_cast(bf16x8, a), __builtin_bit_cast(bf16x8, b), c, 0, 0, 0);
}
DI f32x4 mfma16(u32x4 a, u32x4 b, f32x4 c) {
  return __builtin_amdgcn_mfma_f32_16x16x32_bf16(__builtin_bit_cast(bf16x8, a), __builtin_bit_cast(bf16x8, b), c, 0, 0, 0);
}
DI u32 pk(float a, float b) {
  f32x2 v = {a, b};
  return __builtin_bit_cast(u32, __builtin_convertvector(v, bf16x2));
}
DI float dot2(u32 a, u32 b, float c) {
  return __builtin_amdgcn_fdot2_f32_bf16(__builtin_bit_cast(bf16x2, a), __builtin_bit_cast(bf16x2, b), c, false);
}
DI float asf(u32 v) { return __builtin_bit_cast(float, v); }
DI float blo(u32 v) { return __builtin_bit_cast(float, v << 16); }
DI float bhi(u32 v) { return __builtin_bit_cast(float, v & 0xffff0000u); }
template <int CTRL> DI float dpp_f(float v) { return asf((u32)__builtin_amdgcn_update_dpp(0, (int)__builtin_bit_cast(u32, v), CTRL, 0xf, 0xf, true)); }
DI float row16_max(float v) {
  v = fmaxf(v, dpp_f<0x128>(v)); v = fmaxf(v, dpp_f<0x124>(v)); v = fmaxf(v, dpp_f<0x122>(v)); return fmaxf(v, dpp_f<0x121>(v));
}
DI float xor32_add(float v) { const auto r = __builtin_amdgcn_permlane32_swap(__builtin_bit_cast(u32, v), __builtin_bit_cast(u32, v), false, false); return asf(r[0]) + asf(r[1]); }
DI float xor16_add(float v) { const auto r = __builtin_amdgcn_permlane16_swap(__builtin_bit_cast(u32, v), __builtin_bit_cast(u32, v), false, false); return asf(r[0]) + asf(r[1]); }
DI float xor32_max(float v) { const auto r = __builtin_amdgcn_permlane32_swap(__builtin_bit_cast(u32, v), __builtin_bit_cast(u32, v), false, false); return fmaxf(asf(r[0]), asf(r[1])); }
DI float xor16_max(float v) { const auto r = __builtin_amdgcn_permlane16_swap(__builtin_bit_cast(u32, v), __builtin_bit_cast(u32, v), false, false); return fmaxf(asf(r[0]), asf(r[1])); }
DI float lo32(float v) { const auto r = __builtin_amdgcn_permlane32_swap(__builtin_bit_cast(u32, v), __builtin_bit_cast(u32, v), false, false); return asf(r[0]); }
DI int wave_id() { return __builtin_amdgcn_readfirstlane(threadIdx.x >> 6); }
constexpr u32 ONES2 = 0x3F803F80u;

#define PROF_BEGIN
#define PROF_AT(j)
#define PROF_END(kid)

template <bool B> struct BoolC { static constexpr bool value = B; };
constexpr int K1_PF = 4;
constexpr int K1_LDS = 65536 + 65536 + 16384 + 512;
__global__ __launch_bounds__(320) void k1_qkv(const u16* __restrict__ x, const u16* __restrict__ wq, const u16* __restrict__ wk,
                                              const u16* __restrict__ wv, char* __restrict__ ws) {
  extern __shared__ __attribute__((aligned(16))) char smem[];
  AS3 char* lds = (AS3 char*)smem;
  PROF_BEGIN
  const int l = threadIdx.x & 63, w = wave_id();
  const int b = blockIdx.x, xcd = b & 7, i = b >> 3, tt = xcd * 4 + (i & 3), cg = i >> 2;
  const u32 lb = (u32)(uintptr_t)lds;
  const int lr = l >> 3, lc = l & 7;
  const bool kvw = (w == 4) && (cg < 4);
  if (w < 4) {
    const u32 vx0 = lr * 1024 + ((lc ^ ((lr >> 1) & 7)) << 4);
    const u32 vx1 = lr * 1024 + ((lc ^ ((4 + (lr >> 1)) & 7)) << 4);
    const u32 vw = lr * 1024 + ((lc ^ (((lr >> 1) & 1) << 2)) << 4);
    const char* xb = (const char*)x + (size_t)(tt * 64 + 16 * w) * 1024;
    const char* wb = (const char*)wq + (size_t)(32 * w) * 1024 + cg * 128;
#pragma unroll
    for (int s = 0; s < 4; s++) {
#pragma unroll
      for (int j = 0; j < 4; j++) {
        const int kb = 2 * s + (j >> 1);
        dma16<0>(lb + kb * 8192 + (2 * w + (j & 1)) * 1024, xb + (j & 1) * 8192 + kb * 128, (j & 1) ? vx1 : vx0);
      }
#pragma unroll
      for (int j = 0; j < 4; j++)
        dma16<0>(lb + 65536 + (16 * s + 4 * w + j) * 1024, wb + (size_t)(128 * s + 8 * j) * 1024, vw);
    }
  } else if (kvw) {
    const char* src = (const char*)((cg & 1) ? wv : wk);
#pragma unroll
    for (int s = 0; s < 4; s++) dma16x4(lb + 131072 + s * 4096, src + s * 4096, l * 16);
  }
  const int r = l & 31, h = l >> 5;
  const int th = (w < 4) ? (w & 1) : ((cg >> 1) & 1);
  const int blk = (l >> 4) & 1, q = (l & 15) >> 2, p = l & 3;
  AS3 const char* xa[4];
#pragma unroll
  for (int k4 = 0; k4 < 4; k4++) xa[k4] = lds + (32 * th + r) * 128 + (((2 * k4 + h) ^ ((r >> 1) & 7)) << 4);
  f32x16 acc = {}, accs = {};
  float s1 = 0.f, s2 = 0.f;
  const u32x4 ones = {ONES2, ONES2, ONES2, ONES2};
  if (w < 4) {
    const int hp = w >> 1;
    AS3 const char* wa = lds + 65536 + (8 * h + q) * 128 + (((4 * hp + 2 * blk + (p >> 1)) ^ (((q >> 1) & 1) << 2)) << 4) + 8 * (p & 1);
    u32x4 bfr[K1_PF], afr[K1_PF];
    auto rd = [&](const int ks) {
      bfr[ks % K1_PF] = lds128(xa[ks & 3] + (ks >> 2) * 8192);
      afr[ks % K1_PF] = cat(ldstr(wa + ks * 2048), ldstr(wa + ks * 2048 + 512));
    };
    PROF_AT(5)
    wait_bar<24>();
    PROF_AT(1)
    sched0();
#pragma unroll
    for (int ks = 0; ks < K1_PF; ks++) rd(ks);
#pragma unroll
    for (int ks = 0; ks < 32; ks++) {
      sched0();
      const u32x4 bf = bfr[ks % K1_PF], af = afr[ks % K1_PF];
      acc = mfma32(af, bf, acc);
      accs = mfma32(af, ones, accs);
      s1 = dot2(bf.x, ONES2, s1); s2 = dot2(bf.x, bf.x, s2);
      s1 = dot2(bf.y, ONES2, s1); s2 = dot2(bf.y, bf.y, s2);
      s1 = dot2(bf.z, ONES2, s1); s2 = dot2(bf.z, bf.z, s2);
      s1 = dot2(bf.w, ONES2, s1); s2 = dot2(bf.w, bf.w, s2);
      sched0();
      if (ks + K1_PF == 8) { wait_bar<16>(); PROF_AT(3) } else if (ks + K1_PF == 16) wait_bar<8>(); else if (ks + K1_PF == 24) { wait_bar<0>(); PROF_AT(4) }
      if (ks + K1_PF < 32) rd(ks + K1_PF);
    }
    sched0();
    PROF_AT(2)
    s1 = xor32_add(s1); s2 = xor32_add(s2);
    const float mu = s1 * (1.f / 512.f);
    const float var = fmaxf(s2 * (1.f / 512.f) - mu * mu, 0.f);
    const float sc = __builtin_amdgcn_rsqf(var + 1e-5f) * (0.25f * 1.4426950408889634f);
#pragma unroll
    for (int hh = 0; hh < 2; hh++) {
      u32x4 o;
      o.x = pk((acc[8 * hh + 0] - mu * accs[8 * hh + 0]) * sc, (acc[8 * hh + 1] - mu * accs[8 * hh + 1]) * sc);
      o.y = pk((acc[8 * hh + 2] - mu * accs[8 * hh + 2]) * sc, (acc[8 * hh + 3] - mu * accs[8 * hh + 3]) * sc);
      o.z = pk((acc[8 * hh + 4] - mu * accs[8 * hh + 4]) * sc, (acc[8 * hh + 5] - mu * accs[8 * hh + 5]) * sc);
      o.w = pk((acc[8 * hh + 6] - mu * accs[8 * hh + 6]) * sc, (acc[8 * hh + 7] - mu * accs[8 * hh + 7]) * sc);
      const int head = 4 * cg + 2 * hp + hh;
      *(u32x4*)(ws + WS_Q + ((size_t)((2 * tt + th) * 32 + head) * 64 + l) * 16) = o;
    }
  } else {
    const bool isV = cg & 1;
    AS3 const char* wa = lds + 131072 + (8 * h + q) * 32 + p * 8;
    auto chain = [&](auto isv) {
      u32x4 bfr[K1_PF], afr[K1_PF];
      auto rd = [&](const int ks) {
        bfr[ks % K1_PF] = lds128(xa[ks & 3] + (ks >> 2) * 8192);
        afr[ks % K1_PF] = cat(ldstr(wa + ks * 512), ldstr(wa + ks * 512 + 128));
      };
      wait_bar<12>();
      sched0();
#pragma unroll
      for (int ks = 0; ks < K1_PF; ks++) rd(ks);
#pragma unroll
      for (int ks = 0; ks < 32; ks++) {
        sched0();
        const u32x4 bf = bfr[ks % K1_PF], af = afr[ks % K1_PF];
        if (decltype(isv)::value) { acc = mfma32(bf, af, acc); accs = mfma32(ones, af, accs); }
        else { acc = mfma32(af, bf, acc); accs = mfma32(af, ones, accs); }
        s1 = dot2(bf.x, ONES2, s1); s2 = dot2(bf.x, bf.x, s2);
        s1 = dot2(bf.y, ONES2, s1); s2 = dot2(bf.y, bf.y, s2);
        s1 = dot2(bf.z, ONES2, s1); s2 = dot2(bf.z, bf.z, s2);
        s1 = dot2(bf.w, ONES2, s1); s2 = dot2(bf.w, bf.w, s2);
        sched0();
        if (ks + K1_PF == 8) wait_bar<8>(); else if (ks + K1_PF == 16) wait_bar<4>(); else if (ks + K1_PF == 24) wait_bar<0>();
        if (ks + K1_PF < 32) rd(ks + K1_PF);
      }
      sched0();
    };
    if (!kvw) { bar(); bar(); bar(); bar(); }
    else if (isV) chain(BoolC<true>{});
    else chain(BoolC<false>{});
    if (kvw) {
      s1 = xor32_add(s1); s2 = xor32_add(s2);
      const float mu = s1 * (1.f / 512.f);
      const float var = fmaxf(s2 * (1.f / 512.f) - mu * mu, 0.f);
      const float rs = __builtin_amdgcn_rsqf(var + 1e-5f);
      const int T = 2 * tt + th;
      if (!isV) {
        float kv[8], n2 = 0.f;
#pragma unroll
        for (int j = 0; j < 8; j++) { kv[j] = (acc[j] - mu * accs[j]) * rs; n2 += kv[j] * kv[j]; }
        u32x4 o = {pk(kv[0], kv[1]), pk(kv[2], kv[3]), pk(kv[4], kv[5]), pk(kv[6], kv[7])};
        const int kap = (r & 0x13) | ((r & 4) << 1) | ((r & 8) >> 1);
        *(u32x4*)(ws + WS_K + ((size_t)T * 64 + h * 32 + kap) * 16) = o;
        n2 = xor16_max(row16_max(xor32_add(n2)));
        if (l == 0) ((float*)(ws + WS_KM))[T] = n2;
      } else {
        AS3 f32x2* st = (AS3 f32x2*)(lds + 147456);
        if (h == 0) { f32x2 v = {mu, rs}; st[r] = v; }
        float vv[16];
#pragma unroll
        for (int j = 0; j < 16; j++) {
          const f32x2 m = st[(j & 3) + 8 * (j >> 2) + 4 * h];
          vv[j] = (acc[j] - m.x * accs[0]) * m.y;
        }
        if (r <= 16) {
#pragma unroll
          for (int s = 0; s < 2; s++) {
            u32x4 o = {pk(vv[8 * s], vv[8 * s + 1]), pk(vv[8 * s + 2], vv[8 * s + 3]), pk(vv[8 * s + 4], vv[8 * s + 5]), pk(vv[8 * s + 6], vv[8 * s + 7])};
            if (r == 16) o = ones;
            *(u32x4*)(ws + WS_V + (size_t)T * 1088 + s * 544 + h * 272 + r * 16) = o;
          }
        }
      }
    }
  }
  PROF_END(0)
}

constexpr int K2_V = 65536, K2_STG = 135168, K2_JUNK = K2_STG + 24576, K2_LDS = K2_JUNK + 256;
constexpr float SAFE_LOG2 = 60.f;

template <bool SUBMAX>
DI void attn_tiles(AS3 const char* ka, AS3 const char* va, const u32x4 qf, const float negm, f32x16& o) {
#pragma unroll
  for (int u = 0; u < 4; u++) {
    const u32x4 kf = lds128(ka + u * 1024);
    const u32x4 v0 = lds128(va + u * 1088), v1 = lds128(va + u * 1088 + 544);
    f32x16 c;
#pragma unroll
    for (int j = 0; j < 16; j++) c[j] = SUBMAX ? negm : 0.f;
    f32x16 s = mfma32(kf, qf, c);
#pragma unroll
    for (int j = 0; j < 16; j++) s[j] = __builtin_amdgcn_exp2f(s[j]);
    const f32x8 p0 = __builtin_shufflevector(s, s, 0, 1, 2, 3, 4, 5, 6, 7);
    const f32x8 p1 = __builtin_shufflevector(s, s, 8, 9, 10, 11, 12, 13, 14, 15);
    o = __builtin_amdgcn_smfmac_f32_32x32x32_bf16(__builtin_bit_cast(bf16x8, v0), __builtin_bit_cast(bf16x16, p0), o, (int)0xDDDDDDDD, 0, 0);
    o = __builtin_amdgcn_smfmac_f32_32x32x32_bf16(__builtin_bit_cast(bf16x8, v1), __builtin_bit_cast(bf16x16, p1), o, (int)0xDDDDDDDD, 0, 0);
  }
}

#define AE(X, j) "v_exp_f32 v[" #X "+" #j "], v[" #X "+" #j "]\n\t"
#define ATILE(X, Y, KO, V0O, V1O)                                                               \
  AE(X, 0) AE(X, 1)                                                                              \
  "s_waitcnt lgkmcnt(2)\n\t"                                                                     \
  "v_smfmac_f32_32x32x32_bf16 v[0:15], v[60:63], v[" #Y "+8:" #Y "+15], v64\n\t"                 \
  "ds_read_b128 v[60:63], v66 offset:" #V1O "\n\t"                                               \
  AE(X, 2) AE(X, 3) AE(X, 4) AE(X, 5)                                                            \
  "s_waitcnt lgkmcnt(2)\n\t"                                                                     \
  "v_mfma_f32_32x32x16_bf16 v[" #Y ":" #Y "+15], v[52:55], v[48:51], 0\n\t"                      \
  "ds_read_b128 v[52:55], v65 offset:" #KO "\n\t"                                                \
  AE(X, 6) AE(X, 7) AE(X, 8) AE(X, 9)                                                            \
  "s_waitcnt lgkmcnt(2)\n\t"                                                                     \
  "v_smfmac_f32_32x32x32_bf16 v[0:15], v[56:59], v[" #X ":" #X "+7], v64\n\t"                    \
  "ds_read_b128 v[56:59], v66 offset:" #V0O "\n\t"                                               \
  AE(X, 10) AE(X, 11) AE(X, 12) AE(X, 13) AE(X, 14) AE(X, 15)
#define ALOOP                                                                                    \
  "1:\n\t" ATILE(16, 32, 2048, 1088, 544) ATILE(32, 16, 3072, 2176, 1632)                        \
  ATILE(16, 32, 4096, 3264, 2720) ATILE(32, 16, 5120, 4352, 3808)                                \
  "v_add_u32 v65, 0x1000, v65\n\tv_add_u32 v66, 0x1100, v66\n\t"                                 \
  "s_sub_u32 %[n], %[n], 1\n\ts_cmp_lg_u32 %[n], 0\n\ts_cbranch_scc1 1b\n\t"
#define AREGS "+{v[0:15]}"(o), "+{v[16:31]}"(sa), "+{v[32:47]}"(sb), "+{v[52:55]}"(kf), "+{v[56:59]}"(v0), "+{v[60:63]}"(v1), "+{v65}"(ka), "+{v66}"(va), [n] "+s"(n)
#define AINS "{v[48:51]}"(qf), "{v64}"(0xDDDDDDDDu)

__global__ __launch_bounds__(512) void k2_attn(const u16* __restrict__ wg, const u16* __restrict__ wu, const u16* __restrict__ wo,
                                               const u16* __restrict__ wd, char* __restrict__ ws) {
  extern __shared__ __attribute__((aligned(16))) char smem[];
  AS3 char* lds = (AS3 char*)smem;
  PROF_BEGIN
  const int l = threadIdx.x & 63, w = wave_id();
  const int b = blockIdx.x, xcd = b & 7, i = b >> 3, tt = xcd * 4 + (i & 3), hg = i >> 2;
  const u32 lb = (u32)(uintptr_t)lds;
  const int th = w & 1, head = 4 * hg + (w >> 1);
  const int r = l & 31, h = l >> 5;
  u32x4 qf; u32x2 kmx;
  gld16<0>(qf, ws + WS_Q + (size_t)((2 * tt + th) * 32 + head) * 1024, l * 16);
  gld8<0>(kmx, ws + WS_KM, (l & 31) * 8);
  const bool side = b < 224;
  const int cgp = b % 28, kc = b / 28;
  {
    const u32 vo = l * 16;
    const char *kb = ws + WS_K, *vb = ws + WS_V;
    dma16<0>(lb + w * 1024, kb + w * 1024, vo);
    dma16<0>(lb + K2_V + w * 1024, vb + w * 1024, vo);
    const int p1 = (8 + 3 * w) * 1024;
    dma16<0>(lb + p1, kb + p1, vo); dma16<1024>(lb + p1, kb + p1, vo); dma16<2048>(lb + p1, kb + p1, vo);
    dma16<0>(lb + K2_V + p1, vb + p1, vo); dma16<1024>(lb + K2_V + p1, vb + p1, vo); dma16<2048>(lb + K2_V + p1, vb + p1, vo);
    {
      const int kk = side ? kc : 0;
      const size_t ro = (size_t)(64 * kk + 8 * w) * 3584 + cgp * 128;
      const u32 vs = (l >> 3) * 3584 + (l & 7) * 16;
      dma16<0>(lb + K2_STG + w * 1024, (const char*)wg + ro, vs);
      dma16<0>(lb + K2_STG + 8192 + w * 1024, (const char*)wu + ro, vs);
      dma16<0>(lb + K2_STG + 16384 + w * 1024, (const char*)wd + (size_t)(64 * cgp + 8 * w) * 1024 + kk * 128, (l >> 3) * 1024 + (l & 7) * 16);
    }
    const int p2 = (32 + 4 * w) * 1024;
    dma16x4(lb + p2, kb + p2, vo);
    dma16x4(lb + K2_V + p2, vb + p2, vo);
    if (w < 4) dma16<0>(lb + K2_V + (64 + w) * 1024, vb + (64 + w) * 1024, vo);
  }
  asm volatile("s_waitcnt vmcnt(19)" : "+v"(qf), "+v"(kmx)::"memory");
  const float km = xor16_max(row16_max(fmaxf(asf(kmx.x), asf(kmx.y))));
  float q2 = dot2(qf.x, qf.x, 0.f); q2 = dot2(qf.y, qf.y, q2); q2 = dot2(qf.z, qf.z, q2); q2 = dot2(qf.w, qf.w, q2);
  q2 = xor32_add(q2);
  const bool unsafe = __any(!(q2 * km * 1.05f <= SAFE_LOG2 * SAFE_LOG2));

  u32 ka = lb + l * 16;
  u32 va = lb + K2_V + h * 272 + (r < 16 ? r * 16 : (r < 20 ? 256 : (r - 16) * 16));
  f32x16 o = {};
  auto side_job = [&]() {
    if (side && w < 4) {
      const int blk = (l >> 4) & 1, q = (l & 15) >> 2, p = l & 3;
      AS3 const char* sp = lds + K2_STG + blk * 8192 + (8 * h + q) * 128 + (2 * w + (p >> 1)) * 16 + 8 * (p & 1);
      float cs = 0.f;
      char* dst = ws + WS_WT + ((size_t)(4 * cgp + w) * 32 + 4 * kc) * 1024 + l * 16;
#pragma unroll
      for (int ks = 0; ks < 4; ks++) {
        const u32x4 f = cat(ldstr(sp + ks * 2048), ldstr(sp + ks * 2048 + 512));
        cs = dot2(f.x, ONES2, cs); cs = dot2(f.y, ONES2, cs); cs = dot2(f.z, ONES2, cs); cs = dot2(f.w, ONES2, cs);
        asm volatile("global_store_dwordx4 %0, %1, off sc1\n\ts_nop 1" ::"v"(dst + ks * 1024), "v"(f) : "memory");
      }
      cs = xor32_add(cs);
      if (h == 0) ((float*)(ws + WS_CS))[((4 * cgp + w) * 32 + r) * 8 + kc] = cs;
    }
    if (side) {
      const int blk = (l >> 4) & 1, q = (l & 15) >> 2, p = l & 3, cb = w & 1, ks = w >> 1;
      AS3 const char* sp = lds + K2_STG + 16384 + (16 * ks + 4 * h + q) * 128 + cb * 64 + blk * 32 + (p & 1) * 16 + (p >> 1) * 8;
      const u32x4 f = cat(ldstr(sp), ldstr(sp + 1024));
      char* dst = ws + WS_WD + ((size_t)(2 * kc + cb) * 112 + 4 * cgp + ks) * 1024 + l * 16;
      asm volatile("global_store_dwordx4 %0, %1, off sc1\n\ts_nop 1" ::"v"(dst), "v"(f) : "memory");
    }
  };
  auto prefetch = [&]() {
    const char* src = (w < 7) ? (ws + WS_WT + ((size_t)(xcd & 1) * 224 + i * 7 + w) * 8192) : ((const char*)wo + (size_t)i * 16384);
    touch_lines(lb + K2_JUNK, src, l * 128);
    if (w == 7) touch_lines(lb + K2_JUNK, src + 8192, l * 128);
  };
  if (!unsafe) {
    f32x16 sa = {}, sb = {};
    u32x4 kf = {}, v0 = {}, v1 = {};
    u32 n = 1;
    asm volatile("s_waitcnt vmcnt(17)\n\ts_barrier\n\t"
                 "ds_read_b128 v[52:55], v65\n\ts_waitcnt lgkmcnt(0)\n\t"
                 "v_mfma_f32_32x32x16_bf16 v[16:31], v[52:55], v[48:51], 0\n\t"
                 "ds_read_b128 v[52:55], v65 offset:1024\n\tds_read_b128 v[56:59], v66\n\ts_nop 7\n\ts_nop 2\n\t"
                 ALOOP "s_waitcnt vmcnt(8) lgkmcnt(0)\n\ts_nop 7\n\ts_barrier"
                 : AREGS : AINS : "memory", "scc");
    PROF_AT(1)
    side_job();
    n = 6;
    asm volatile(ALOOP "s_waitcnt vmcnt(0) lgkmcnt(0)\n\ts_nop 7\n\ts_barrier" : AREGS : AINS : "memory", "scc");
    PROF_AT(2)
    prefetch();
    n = 9;
    asm volatile(ALOOP "s_waitcnt lgkmcnt(0)\n\t"
                 "v_smfmac_f32_32x32x32_bf16 v[0:15], v[60:63], v[40:47], v64\n\ts_nop 15" : AREGS : AINS : "memory", "scc");
    PROF_AT(3)
  } else {
    wait_bar<17>();
    wait_bar<8>();
    side_job();
    wait_bar<0>();
    prefetch();
    AS3 const char* kp = lds + l * 16;
    AS3 const char* vp = lds + (va - lb);
    float m = -3.0e38f;
#pragma unroll 1
    for (int t = 0; t < 64; t++) {
      const f32x16 z = {};
      const f32x16 s = mfma32(lds128(kp + t * 1024), qf, z);
#pragma unroll
      for (int j = 0; j < 16; j++) m = fmaxf(m, s[j]);
    }
    m = xor32_max(m);
#pragma unroll 1
    for (int it = 0; it < 16; it++) {
      attn_tiles<true>(kp, vp, qf, -m, o);
      kp += 4096; vp += 4352;
    }
  }
  const float inv = __builtin_amdgcn_rcpf(lo32(o[8]));
  const u32x4 res = {pk(o[0] * inv, o[1] * inv), pk(o[2] * inv, o[3] * inv), pk(o[4] * inv, o[5] * inv), pk(o[6] * inv, o[7] * inv)};
  const int t16 = 4 * tt + 2 * th + (r >> 4);
  *(u32x4*)(ws + WS_O + ((size_t)(t16 * 16 + (head >> 1)) * 64 + (2 * (head & 1) + h) * 16 + (r & 15)) * 16) = res;
  wait_vm<0>();
  PROF_END(1)
}

template <int NST, int RING, int AHEAD, bool IS_K3>
DI void gemm64(const char* __restrict__ act, const u16* __restrict__ wgt, const u16* __restrict__ xres, char* __restrict__ ws,
               u16* __restrict__ out, AS3 char* lds, const int tt, const int cg) {
  PROF_BEGIN
  const int l = threadIdx.x & 63, w8 = wave_id(), w = w8 & 3, kg = w8 >> 2;
  const u32 lb = (u32)(uintptr_t)lds + kg * (RING * 16384);
  const int lr = l >> 3, lc = l & 7;
  const int g = l >> 4, q = (l & 15) >> 2, p = l & 3, r16 = l & 15;
  constexpr int NB = (AHEAD + 1 < NST ? AHEAD + 1 : NST);
  u32x4 bf[NB * 4];
  u32x2 res[4] = {};
  const int tok = tt * 64 + 16 * w + r16;
  if (kg == 0) {
    if (IS_K3) {
      const char* xb = (const char*)xres + (size_t)(tt * 64 + 16 * w) * 1024 + cg * 128;
      const u32 vo = r16 * 1024 + g * 8;
      gld8<0>(res[0], xb, vo); gld8<32>(res[1], xb, vo); gld8<64>(res[2], xb, vo); gld8<96>(res[3], xb, vo);
    } else {
      const char* hb = ws + WS_H + (size_t)((tt * 2 + (w >> 1)) * 32 + cg * 4) * 1024;
      const u32 vo = ((g >> 1) * 32 + (w & 1) * 16 + r16) * 16 + (g & 1) * 8;
      gld8<0>(res[0], hb, vo); gld8<1024>(res[1], hb, vo); gld8<2048>(res[2], hb, vo); gld8<3072>(res[3], hb, vo);
    }
  }
  const char* ab = act + (size_t)(tt * 4 + w) * (2 * NST * 4096) + kg * (NST * 4096);
  const char* wb = (const char*)wgt + (size_t)(kg * NST * 128 + 32 * w) * 1024 + cg * 128;
  const u32 vw = lr * 1024 + ((lc ^ (((lr >> 1) & 3) << 1)) << 4);
  const u32 va = l * 16;
  auto issue = [&](const int s) {
    const u32 dst = lb + (s % RING) * 16384 + w * 4096;
    const char* src = wb + (size_t)s * 131072;
    dma16<0>(dst, src, vw); dma16<0>(dst + 1024, src + 8192, vw); dma16<0>(dst + 2048, src + 16384, vw); dma16<0>(dst + 3072, src + 24576, vw);
    const char* as = ab + s * 4096;
    const int o = (s % NB) * 4;
    gld16<0>(bf[o], as, va); gld16<1024>(bf[o + 1], as, va); gld16<2048>(bf[o + 2], as, va); gld16<3072>(bf[o + 3], as, va);
  };
#pragma unroll
  for (int s = 0; s < AHEAD && s < NST; s++) issue(s);
  const int swz = (2 * (g & 1) + (q >> 1)) << 1;
  AS3 const char* wa[4];
#pragma unroll
  for (int c = 0; c < 4; c++)
    wa[c] = lds + kg * (RING * 16384) + (16 * (g >> 1) + 4 * (g & 1) + q) * 128 + (((2 * c) ^ swz) + (p >> 1)) * 16 + 8 * (p & 1);
  f32x4 acc[4] = {};
#pragma unroll
  for (int s = 0; s < NST; s++) {
    const int last = (s + AHEAD - 1 < NST - 1) ? (s + AHEAD - 1) : (NST - 1);
    const int o = (s % NB) * 4;
    sched0();
    if (last - s == 0) wait_bar4<0>(bf[o], bf[o + 1], bf[o + 2], bf[o + 3]);
    else if (last - s == 1) wait_bar4<8>(bf[o], bf[o + 1], bf[o + 2], bf[o + 3]);
    else if (last - s == 2) wait_bar4<16>(bf[o], bf[o + 1], bf[o + 2], bf[o + 3]);
    else wait_bar4<24>(bf[o], bf[o + 1], bf[o + 2], bf[o + 3]);
    sched0();
    if (s == 0) { PROF_AT(1) }
    const int so = (s % RING) * 16384;
    u32x4 af[4][2];
    auto rd = [&](const int u) {
      const int ks = u >> 1, c = (u & 1) * 2;
      af[u & 3][0] = cat(ldstr(wa[c] + so + ks * 4096), ldstr(wa[c] + so + ks * 4096 + 1024));
      af[u & 3][1] = cat(ldstr(wa[c + 1] + so + ks * 4096), ldstr(wa[c + 1] + so + ks * 4096 + 1024));
    };
    rd(0); rd(1); rd(2);
    sched0();
    if (s + AHEAD < NST) issue(s + AHEAD);
#pragma unroll
    for (int u = 0; u < 8; u++) {
      sched0();
      const int ks = u >> 1, c = (u & 1) * 2;
      acc[c] = mfma16(af[u & 3][0], bf[o + ks], acc[c]);
      acc[c + 1] = mfma16(af[u & 3][1], bf[o + ks], acc[c + 1]);
      sched0();
      if (u + 3 < 8) rd(u + 3);
    }
  }
  sched0();
  PROF_AT(2)
  AS3 char* red = lds + (w * 4) * 1024 + l * 16;
  asm volatile("s_waitcnt lgkmcnt(0)\n\ts_barrier" ::: "memory");
  if (kg == 1) {
#pragma unroll
    for (int c = 0; c < 4; c++) *(AS3 f32x4*)(red + c * 1024) = acc[c];
    asm volatile("s_waitcnt lgkmcnt(0)\n\ts_barrier" ::: "memory");
  } else {
    asm volatile("s_barrier" : "+v"(res[0]), "+v"(res[1]), "+v"(res[2]), "+v"(res[3])::"memory");
#pragma unroll
    for (int c = 0; c < 4; c++) acc[c] += ldsf4(red + c * 1024);
    if (IS_K3) {
      float s1 = 0.f, s2 = 0.f;
      char* hb = ws + WS_H + (size_t)((tt * 2 + (w >> 1)) * 32 + cg * 4) * 1024 + ((g >> 1) * 32 + (w & 1) * 16 + r16) * 16 + (g & 1) * 8;
#pragma unroll
      for (int c = 0; c < 4; c++) {
        u32x2 hv;
        hv.x = pk(blo(res[c].x) + acc[c][0], bhi(res[c].x) + acc[c][1]);
        hv.y = pk(blo(res[c].y) + acc[c][2], bhi(res[c].y) + acc[c][3]);
        s1 = dot2(hv.x, ONES2, s1); s1 = dot2(hv.y, ONES2, s1);
        s2 = dot2(hv.x, hv.x, s2); s2 = dot2(hv.y, hv.y, s2);
        *(u32x2*)(hb + c * 1024) = hv;
      }
      s1 = xor32_add(xor16_add(s1)); s2 = xor32_add(xor16_add(s2));
      if (g == 0) { f32x2 v = {s1, s2}; *(f32x2*)(ws + WS_ST + ((size_t)tok * 8 + cg) * 8) = v; }
    } else {
      u16* ob = out + (size_t)tok * 512 + cg * 64 + 4 * g;
#pragma unroll
      for (int c = 0; c < 4; c++) {
        u32x2 ov;
        ov.x = pk(blo(res[c].x) + acc[c][0], bhi(res[c].x) + acc[c][1]);
        ov.y = pk(blo(res[c].y) + acc[c][2], bhi(res[c].y) + acc[c][3]);
        *(u32x2*)(ob + 16 * c) = ov;
      }
    }
  }
  PROF_END(IS_K3 ? 2 : 4)
}

constexpr int K3_LDS = 2 * 2 * 16384;
__global__ __launch_bounds__(512) void k3_oproj(const u16* __restrict__ x, const u16* __restrict__ wo, char* __restrict__ ws) {
  extern __shared__ __attribute__((aligned(16))) char smem[];
  const int b = blockIdx.x, xcd = b & 7, i = b >> 3;
  gemm64<2, 2, 2, true>(ws + WS_O, wo, x, ws, nullptr, (AS3 char*)smem, xcd * 4 + (i & 3), i >> 2);
}
constexpr int K5_WIN = 8, K5_LDS = 4 * 3 * 4096;
__global__ __launch_bounds__(256) void k5_down(char* __restrict__ ws, u16* __restrict__ out) {
  extern __shared__ __attribute__((aligned(16))) char smem[];
  AS3 char* lds = (AS3 char*)smem;
  PROF_BEGIN
  const int l = threadIdx.x & 63, w = wave_id();
  const int b = blockIdx.x, xcd = b & 7, i = b >> 3, tt = xcd * 4 + (i & 3), cg = i >> 2;
  const int tb = w >> 1, cb = w & 1;
  const u32 va = l * 16;
  const char* ax = ws + WS_WD + (size_t)((2 * cg + cb) * 112 + 28 * w) * 1024;
  const char* ay = ws + WS_WD + (size_t)((2 * cg + (cb ^ 1)) * 112 + 28 * w) * 1024;
  const char* bx = ws + WS_F + (size_t)((2 * tt + tb) * 112 + 28 * w) * 1024;
  const char* by = ws + WS_F + (size_t)((2 * tt + (tb ^ 1)) * 112 + 28 * w) * 1024;
  u32x4 fr[K5_WIN + 1][4];
  u32x4 res[2];
  auto ld = [&](auto sc, auto jc) {
    constexpr int s = decltype(sc)::value, j = decltype(jc)::value;
    const char* src = (j == 0 ? ax : (j == 1 ? bx : (j == 2 ? ay : by))) + (s >> 2) * 4096;
    gld16<(s & 3) * 1024>(fr[s % (K5_WIN + 1)][j], src, va);
  };
  sfor<0, K5_WIN>([&](auto sc) {
    sfor<0, 4>([&](auto jc) { ld(sc, jc); });
    if constexpr (decltype(sc)::value == 1) {
      const char* hb = ws + WS_H + (size_t)((2 * tt + tb) * 32 + 4 * cg + 2 * cb) * 1024;
      gld16<0>(res[0], hb, va); gld16<1024>(res[1], hb, va);
    }
  });
  f32x16 acc[4] = {};
  sfor<0, 28>([&](auto sc) {
    constexpr int s = decltype(sc)::value, o = s % (K5_WIN + 1);
    constexpr int young = 4 * ((s + K5_WIN - 1 < 27 ? s + K5_WIN - 1 : 27) - s) + (s < 1 ? 2 : 0);
    sched0();
    asm volatile("s_waitcnt vmcnt(%4)" : "+v"(fr[o][0]), "+v"(fr[o][1]), "+v"(fr[o][2]), "+v"(fr[o][3]) : "i"(young) : "memory");
    if constexpr (s == 0) { PROF_AT(1) }
    sched0();
    acc[0] = mfma32(fr[o][0], fr[o][1], acc[0]);
    if constexpr (s + K5_WIN < 28) { sched0(); ld(IntC<s + K5_WIN>{}, IntC<0>{}); sched0(); }
    acc[1] = mfma32(fr[o][2], fr[o][1], acc[1]);
    if constexpr (s + K5_WIN < 28) { sched0(); ld(IntC<s + K5_WIN>{}, IntC<1>{}); sched0(); }
    acc[2] = mfma32(fr[o][0], fr[o][3], acc[2]);
    if constexpr (s + K5_WIN < 28) { sched0(); ld(IntC<s + K5_WIN>{}, IntC<2>{}); sched0(); }
    acc[3] = mfma32(fr[o][2], fr[o][3], acc[3]);
    if constexpr (s + K5_WIN < 28) { sched0(); ld(IntC<s + K5_WIN>{}, IntC<3>{}); sched0(); }
  });
  sched0();
  PROF_AT(2)
#pragma unroll
  for (int j = 1; j < 4; j++)
#pragma unroll
    for (int a = 0; a < 4; a++) {
      const f32x4 v = {acc[j][4 * a], acc[j][4 * a + 1], acc[j][4 * a + 2], acc[j][4 * a + 3]};
      *(AS3 f32x4*)(lds + ((w ^ j) * 3 + j - 1) * 4096 + a * 1024 + l * 16) = v;
    }
  asm volatile("s_waitcnt lgkmcnt(0)\n\ts_barrier" : "+v"(res[0]), "+v"(res[1])::"memory");
  f32x16 o = acc[0];
#pragma unroll
  for (int j = 0; j < 3; j++)
#pragma unroll
    for (int a = 0; a < 4; a++) {
      const f32x4 v = ldsf4(lds + (w * 3 + j) * 4096 + a * 1024 + l * 16);
      o[4 * a] += v[0]; o[4 * a + 1] += v[1]; o[4 * a + 2] += v[2]; o[4 * a + 3] += v[3];
    }
  u16* ob = out + (size_t)((2 * tt + tb) * 32 + (l & 31)) * 512 + cg * 64 + cb * 32 + (l >> 5) * 8;
#pragma unroll
  for (int a = 0; a < 2; a++) {
    u32x4 v;
    v.x = pk(blo(res[a].x) + o[8 * a], bhi(res[a].x) + o[8 * a + 1]);
    v.y = pk(blo(res[a].y) + o[8 * a + 2], bhi(res[a].y) + o[8 * a + 3]);
    v.z = pk(blo(res[a].z) + o[8 * a + 4], bhi(res[a].z) + o[8 * a + 5]);
    v.w = pk(blo(res[a].w) + o[8 * a + 6], bhi(res[a].w) + o[8 * a + 7]);
    *(u32x4*)(ob + 16 * a) = v;
  }
  PROF_END(4)
}

constexpr int K4_RING = 9, K4_AHEAD = 6, K4_CS = K4_RING * 16384, K4_LDS = K4_CS + 1024, K4_PF = 8;
DI u32 geglu2(const f32x16& a, const int p, AS3 const char* cs, const float mu, const float rs) {
  const f32x2 cg = *(AS3 const f32x2*)(cs + (p >> 1) * 32 + (p & 1) * 8), cu = *(AS3 const f32x2*)(cs + 64 + (p >> 1) * 32 + (p & 1) * 8);
  constexpr float C1 = -2.f * 0.7978845608028654f * 1.4426950408889634f, C2 = C1 * 0.044715f;
  const f32x2 ag = {a[2 * p], a[2 * p + 1]}, au = {a[8 + 2 * p], a[9 + 2 * p]};
  const f32x2 gv = (ag - mu * cg) * rs, uv = (au - mu * cu) * rs;
  const f32x2 z = gv * (C2 * (gv * gv) + C1);
  const f32x2 e = {__builtin_amdgcn_exp2f(z.x), __builtin_amdgcn_exp2f(z.y)};
  const f32x2 d = e + 1.f;
  const f32x2 rc = {__builtin_amdgcn_rcpf(d.x), __builtin_amdgcn_rcpf(d.y)};
  const f32x2 o = gv * uv * rc;
  return pk(o.x, o.y);
}

__global__ __launch_bounds__(256) void k4_up(char* __restrict__ ws) {
  extern __shared__ __attribute__((aligned(16))) char smem[];
  AS3 char* lds = (AS3 char*)smem;
  PROF_BEGIN
  const int l = threadIdx.x & 63, w = wave_id();
  const int b = blockIdx.x, xcd = b & 7, i = b >> 3;
  const int tt = 4 * (xcd >> 1) + (i & 3), sl = 8 * (xcd & 1) + (i >> 2);
  const u32 lb = (u32)(uintptr_t)lds;
  const int r = l & 31, h = l >> 5;
  const int t32 = 4 * tt + w;
  u32x4 st[4], cp[2];
  {
    const char* sb = ws + WS_ST + (size_t)t32 * 32 * 64;
    gld16<0>(st[0], sb, r * 64); gld16<16>(st[1], sb, r * 64); gld16<32>(st[2], sb, r * 64); gld16<48>(st[3], sb, r * 64);
    const char* cb = ws + WS_CS + (size_t)(sl * 224 + 56 * w) * 32;
    const u32 co = (l < 56 ? l : 55) * 32;
    gld16<0>(cp[0], cb, co); gld16<16>(cp[1], cb, co);
  }
  u32x4 hf[32];
  const char* hb = ws + WS_H + (size_t)t32 * 32768;
  const char* wt = ws + WS_WT + (size_t)sl * 7 * 32768;
  const u32 va = l * 16;
  auto issue = [&](auto sc) {
    constexpr int s = decltype(sc)::value;
    const u32 dst = lb + (s % K4_RING) * 16384 + w * 4096;
    if constexpr (s < 8) {
      dma16x4(dst, wt + (size_t)w * 32768 + s * 4096, va);
      const char* hs = hb + s * 4096;
      gld16<0>(hf[4 * s], hs, va); gld16<1024>(hf[4 * s + 1], hs, va); gld16<2048>(hf[4 * s + 2], hs, va); gld16<3072>(hf[4 * s + 3], hs, va);
    } else {
      dma16x4(dst, wt + (size_t)(4 + ((s - 8) >> 1)) * 32768 + ((s - 8) & 1) * 16384 + w * 4096, va);
    }
  };
  auto arrive = [&](auto sc) {
    constexpr int s = decltype(sc)::value;
    constexpr int young = s <= 2 ? 40 : (s <= 7 ? 48 - 4 * s : 4 * (13 - s));
    if constexpr (s < 8) wait_bar4<young>(hf[4 * s], hf[4 * s + 1], hf[4 * s + 2], hf[4 * s + 3]);
    else wait_bar<young>();
    sched0();
    if constexpr (s + K4_AHEAD < 14) issue(IntC<s + K4_AHEAD>{});
  };
  sfor<0, K4_AHEAD>(issue);
  AS3 const char* wa = lds + l * 16;
  AS3 const char* csa = lds + K4_CS + h * 16;
  char* fo = ws + WS_F + (size_t)t32 * 112 * 1024 + l * 16;
  const int n0 = sl * 7;
  float mu, rs;
  f32x16 acc[7] = {};
  u32x4 af[K4_PF];
  auto rd = [&](auto Uc) {
    constexpr int U = decltype(Uc)::value, s = U >> 4, u = U & 15;
    af[U % K4_PF] = lds128(wa + (s % K4_RING) * 16384 + (s < 8 ? (u & 3) * 4096 + (u >> 2) * 1024 : u * 1024));
  };
  sched0();
  arrive(IntC<0>{});
  PROF_AT(1)
  sfor<0, K4_PF>(rd);
  {
    asm volatile("" : "+v"(st[0]), "+v"(st[1]), "+v"(st[2]), "+v"(st[3]), "+v"(cp[0]), "+v"(cp[1]));
    float s1 = 0.f, s2 = 0.f;
#pragma unroll
    for (int j = 0; j < 4; j++) {
      s1 += asf(st[j].x) + asf(st[j].z);
      s2 += asf(st[j].y) + asf(st[j].w);
    }
    mu = s1 * (1.f / 512.f);
    rs = __builtin_amdgcn_rsqf(fmaxf(s2 * (1.f / 512.f) - mu * mu, 0.f) + 1e-5f);
    float c = 0.f;
#pragma unroll
    for (int j = 0; j < 2; j++) c += (asf(cp[j].x) + asf(cp[j].y)) + (asf(cp[j].z) + asf(cp[j].w));
    if (l < 56) *(AS3 float*)(lds + K4_CS + (56 * w + l) * 4) = c;
  }
  u32x4 ob;
  sfor<0, 56>([&](auto Gc) {
    constexpr int G = decltype(Gc)::value;
    sched0();
    sfor<0, 4>([&](auto kc) {
      constexpr int U = 4 * G + decltype(kc)::value, s = U >> 4, u = U & 15;
      constexpr int t = s < 8 ? (u & 3) : 4 + ((s - 8) >> 1);
      constexpr int f = s < 8 ? 4 * s + (u >> 2) : 16 * ((s - 8) & 1) + u;
      acc[t] = mfma32(af[U % K4_PF], hf[f], acc[t]);
      constexpr int Un = U + K4_PF;
      if constexpr (Un < 224) {
        if constexpr ((Un & 15) == 0) { sched0(); arrive(IntC<(Un >> 4)>{}); }
        rd(IntC<Un>{});
      }
    });
    if constexpr (G >= 32) {
      constexpr int e = G - 32, t = e >> 2, p = e & 3;
      const u32 v = geglu2(acc[t], p, csa + t * 128, mu, rs);
      if (p == 0) ob.x = v; else if (p == 1) ob.y = v; else if (p == 2) ob.z = v; else ob.w = v;
      if (p == 3) *(u32x4*)(fo + (n0 + t) * 1024) = ob;
#pragma unroll
      for (int k = 0; k < 4; k++) {
        __builtin_amdgcn_sched_group_barrier(0x008, 1, 0);
        __builtin_amdgcn_sched_group_barrier(0x100, 1, 0);
        __builtin_amdgcn_sched_group_barrier(0x002, 5, 0);
      }
    }
    if constexpr (G == 31) { PROF_AT(2) }
  });
  sched0();
  PROF_AT(3)
  {
    ob.x = geglu2(acc[6], 0, csa + 6 * 128, mu, rs); ob.y = geglu2(acc[6], 1, csa + 6 * 128, mu, rs);
    ob.z = geglu2(acc[6], 2, csa + 6 * 128, mu, rs); ob.w = geglu2(acc[6], 3, csa + 6 * 128, mu, rs);
    *(u32x4*)(fo + (n0 + 6) * 1024) = ob;
  }
  PROF_END(3)
}

extern "C" void kernel_launch(void* const* d_in, const int* in_sizes, int n_in, void* d_out, int out_size, void* d_ws, size_t ws_size,
                              hipStream_t stream) {
  const u16 *x = (const u16*)d_in[0], *wq = (const u16*)d_in[1], *wk = (const u16*)d_in[2], *wv = (const u16*)d_in[3],
            *wo = (const u16*)d_in[4], *wg = (const u16*)d_in[6], *wu = (const u16*)d_in[7], *wd = (const u16*)d_in[8];
  char* ws = (char*)d_ws;
  (void)hipFuncSetAttribute((const void*)k1_qkv, hipFuncAttributeMaxDynamicSharedMemorySize, K1_LDS);
  (void)hipFuncSetAttribute((const void*)k2_attn, hipFuncAttributeMaxDynamicSharedMemorySize, K2_LDS);
  (void)hipFuncSetAttribute((const void*)k3_oproj, hipFuncAttributeMaxDynamicSharedMemorySize, K3_LDS);
  (void)hipFuncSetAttribute((const void*)k4_up, hipFuncAttributeMaxDynamicSharedMemorySize, K4_LDS);
  (void)hipFuncSetAttribute((const void*)k5_down, hipFuncAttributeMaxDynamicSharedMemorySize, K5_LDS);
  k1_qkv<<<256, 320, K1_LDS, stream>>>(x, wq, wk, wv, ws);
  k2_attn<<<256, 512, K2_LDS, stream>>>(wg, wu, wo, wd, ws);
  k3_oproj<<<256, 512, K3_LDS, stream>>>(x, wo, ws);
  k4_up<<<256, 256, K4_LDS, stream>>>(ws);
  k5_down<<<256, 256, K5_LDS, stream>>>(ws, (u16*)d_out);
}
```

```cpp
#include <hip/hip_runtime.h>
#include <stdint.h>

typedef unsigned int u32;
typedef unsigned short u16;
typedef unsigned long long u64;
typedef __bf16 bf16x2 __attribute__((ext_vector_type(2)));
typedef __bf16 bf16x8 __attribute__((ext_vector_type(8)));
typedef __bf16 bf16x16 __attribute__((ext_vector_type(16)));
typedef float f32x2 __attribute__((ext_vector_type(2)));
typedef float f32x4 __attribute__((ext_vector_type(4)));
typedef float f32x8 __attribute__((ext_vector_type(8)));
typedef float f32x16 __attribute__((ext_vector_type(16)));
typedef u32 u32x2 __attribute__((ext_vector_type(2)));
typedef u32 u32x4 __attribute__((ext_vector_type(4)));
typedef short s16x4 __attribute__((ext_vector_type(4)));

#define AS3 __attribute__((address_space(3)))
#define DI __device__ __forceinline__

constexpr int S_ = 2048, D_ = 512, F_ = 1792;

constexpr size_t WS_Q = 0;
constexpr size_t WS_HN = WS_Q + (2u << 20);
constexpr size_t WS_H = WS_HN + (2u << 20);
constexpr size_t WS_K = WS_H + (2u << 20);
constexpr size_t WS_V = WS_K + 65536;
constexpr size_t WS_KM = WS_V + 73728;
constexpr size_t WS_QM = WS_KM + 1024;
constexpr size_t WS_WO = WS_QM + 16384 + 3072;
constexpr size_t WS_WT = WS_WO + 524288;
constexpr size_t WS_WD = WS_WT + 112 * 32768;
constexpr size_t WS_F = WS_WD + 16 * 112 * 1024;
constexpr size_t WS_FLG = WS_F + (size_t)64 * 112 * 1024;
constexpr size_t WS_FLE = WS_FLG + 3 * 256 * 128;
constexpr size_t WS_END = WS_FLE + 256 * 1024;

template <int N> DI void wait_vm() { asm volatile("s_waitcnt vmcnt(%0)" ::"i"(N) : "memory"); }
DI void bar() { asm volatile("s_barrier" ::: "memory"); }
template <int N> DI void wait_bar4(u32x4& a, u32x4& b, u32x4& c, u32x4& d) {
  asm volatile("s_waitcnt vmcnt(%4)\n\ts_barrier" : "+v"(a), "+v"(b), "+v"(c), "+v"(d) : "i"(N) : "memory");
}
template <int N> DI void wait_bar() { asm volatile("s_waitcnt vmcnt(%0)\n\ts_barrier" ::"i"(N) : "memory"); }

template <int V> struct IntC { static constexpr int value = V; };
template <int I, int N, class F> DI void sfor(F&& f) { if constexpr (I < N) { f(IntC<I>{}); sfor<I + 1, N>(f); } }
DI void sched0() { __builtin_amdgcn_sched_barrier(0); }

template <int IMM> DI void dma16(u32 lds, const void* sbase, u32 voff) {
  asm volatile("s_mov_b32 m0, %0\n\ts_nop 0\n\tglobal_load_lds_dwordx4 %1, %2 offset:%3" ::"s"(lds), "v"(voff), "s"(sbase), "i"(IMM) : "memory");
}
DI void dma16x4(u32 lds, const void* sbase, u32 voff) {
  asm volatile("s_mov_b32 m0, %0\n\ts_nop 0\n\tglobal_load_lds_dwordx4 %1, %2\n\tglobal_load_lds_dwordx4 %1, %2 offset:1024\n\t"
               "global_load_lds_dwordx4 %1, %2 offset:2048\n\tglobal_load_lds_dwordx4 %1, %2 offset:3072" ::"s"(lds), "v"(voff), "s"(sbase) : "memory");
}
DI void touch_lines(u32 lds, const void* sbase, u32 voff) {
  asm volatile("s_mov_b32 m0, %0\n\ts_nop 0\n\tglobal_load_lds_dword %1, %2" ::"s"(lds), "v"(voff), "s"(sbase) : "memory");
}
template <int IMM> DI void gld16(u32x4& d, const void* sbase, u32 voff) {
  asm volatile("global_load_dwordx4 %0, %1, %2 offset:%3" : "=v"(d) : "v"(voff), "s"(sbase), "i"(IMM) : "memory");
}
template <int IMM> DI void gld8(u32x2& d, const void* sbase, u32 voff) {
  asm volatile("global_load_dwordx2 %0, %1, %2 offset:%3" : "=v"(d) : "v"(voff), "s"(sbase), "i"(IMM) : "memory");
}
DI void pin(u32x4& a) { asm volatile("" : "+v"(a)); }
DI void pin(u32x2& a) { asm volatile("" : "+v"(a)); }

DI u32x4 lds128(AS3 const char* p) { return *(AS3 const u32x4*)p; }
DI f32x4 ldsf4(AS3 const char* p) { return *(AS3 const f32x4*)p; }
DI u32x2 ldstr(AS3 const char* p) {
  return __builtin_bit_cast(u32x2, __builtin_amdgcn_ds_read_tr16_b64_v4i16((AS3 s16x4*)p));
}
DI u32x4 cat(u32x2 a, u32x2 b) { u32x4 r; r.x = a.x; r.y = a.y; r.z = b.x; r.w = b.y; return r; }
DI f32x16 mfma32(u32x4 a, u32x4 b, f32x16 c) {
  return __builtin_amdgcn_mfma_f32_32x32x16_bf16(__builtin_bit_cast(bf16x8, a), __builtin_bit_cast(bf16x8, b), c, 0, 0, 0);
}
DI f32x4 mfma16(u32x4 a, u32x4 b, f32x4 c) {
  return __builtin_amdgcn_mfma_f32_16x16x32_bf16(__builtin_bit_cast(bf16x8, a), __builtin_bit_cast(bf16x8, b), c, 0, 0, 0);
}
DI u32 pk(float a, float b) {
  f32x2 v = {a, b};
  return __builtin_bit_cast(u32, __builtin_convertvector(v, bf16x2));
}
DI float dot2(u32 a, u32 b, float c) {
  return __builtin_amdgcn_fdot2_f32_bf16(__builtin_bit_cast(bf16x2, a), __builtin_bit_cast(bf16x2, b), c, false);
}
DI float asf(u32 v) { return __builtin_bit_cast(float, v); }
DI float blo(u32 v) { return __builtin_bit_cast(float, v << 16); }
DI float bhi(u32 v) { return __builtin_bit_cast(float, v & 0xffff0000u); }
template <int CTRL> DI float dpp_f(float v) { return asf((u32)__builtin_amdgcn_update_dpp(0, (int)__builtin_bit_cast(u32, v), CTRL, 0xf, 0xf, true)); }
DI float row16_max(float v) {
  v = fmaxf(v, dpp_f<0x128>(v)); v = fmaxf(v, dpp_f<0x124>(v)); v = fmaxf(v, dpp_f<0x122>(v)); return fmaxf(v, dpp_f<0x121>(v));
}
DI float oct_max(float v) {
  v = fmaxf(v, dpp_f<0xB1>(v)); v = fmaxf(v, dpp_f<0x4E>(v)); return fmaxf(v, dpp_f<0x141>(v));
}
DI float oct_add(float v) { v += dpp_f<0xB1>(v); v += dpp_f<0x4E>(v); return v + dpp_f<0x141>(v); }
template <int A, int B, int C, int D> DI u32 u32_max(u32 v) {
  u32 t = (u32)__builtin_amdgcn_update_dpp(0, (int)v, A, 0xf, 0xf, true); v = v > t ? v : t;
  t = (u32)__builtin_amdgcn_update_dpp(0, (int)v, B, 0xf, 0xf, true); v = v > t ? v : t;
  t = (u32)__builtin_amdgcn_update_dpp(0, (int)v, C, 0xf, 0xf, true); v = v > t ? v : t;
  t = (u32)__builtin_amdgcn_update_dpp(0, (int)v, D, 0xf, 0xf, true); return v > t ? v : t;
}
DI float xor32_add(float v) { const auto r = __builtin_amdgcn_permlane32_swap(__builtin_bit_cast(u32, v), __builtin_bit_cast(u32, v), false, false); return asf(r[0]) + asf(r[1]); }
DI float xor16_add(float v) { const auto r = __builtin_amdgcn_permlane16_swap(__builtin_bit_cast(u32, v), __builtin_bit_cast(u32, v), false, false); return asf(r[0]) + asf(r[1]); }
DI float xor32_max(float v) { const auto r = __builtin_amdgcn_permlane32_swap(__builtin_bit_cast(u32, v), __builtin_bit_cast(u32, v), false, false); return fmaxf(asf(r[0]), asf(r[1])); }
DI float xor16_max(float v) { const auto r = __builtin_amdgcn_permlane16_swap(__builtin_bit_cast(u32, v), __builtin_bit_cast(u32, v), false, false); return fmaxf(asf(r[0]), asf(r[1])); }
DI float lo32(float v) { const auto r = __builtin_amdgcn_permlane32_swap(__builtin_bit_cast(u32, v), __builtin_bit_cast(u32, v), false, false); return asf(r[0]); }
DI int wave_id() { return __builtin_amdgcn_readfirstlane(threadIdx.x >> 6); }
constexpr u32 ONES2 = 0x3F803F80u;

#define PROF_BEGIN
#define PROF_AT(j)
#define PROF_END(kid)
#define PROF_WAVES

constexpr int K1_LDS = 147456 + 512;
#define K1_ASM \
    "v_readfirstlane_b32 s21, v0\n\t" \
    "v_and_b32 v1, 63, v0\n\t" \
    "s_and_b32 s22, s20, 7\n\t" \
    "s_lshl_b32 s22, s22, 2\n\t" \
    "s_bfe_u32 s24, s20, 0x20003\n\t" \
    "s_lshr_b32 s21, s21, 6\n\t" \
    "s_or_b32 s22, s22, s24\n\t" \
    "v_lshrrev_b32 v2, 3, v1\n\t" \
    "v_and_b32 v3, 7, v1\n\t" \
    "s_lshl_b32 s24, s22, 16\n\t" \
    "s_lshl_b32 s25, s21, 13\n\t" \
    "s_add_u32 s24, s24, s25\n\t" \
    "v_lshlrev_b32 v4, 10, v2\n\t" \
    "v_lshl_or_b32 v4, v3, 4, v4\n\t" \
    "s_add_u32 s26, s8, s24\n\t" \
    "s_addc_u32 s27, s9, 0\n\t" \
    "global_load_dwordx4 v[8:11], v4, s[26:27]\n\t" \
    "global_load_dwordx4 v[12:15], v4, s[26:27] offset:128\n\t" \
    "global_load_dwordx4 v[16:19], v4, s[26:27] offset:256\n\t" \
    "global_load_dwordx4 v[20:23], v4, s[26:27] offset:384\n\t" \
    "global_load_dwordx4 v[24:27], v4, s[26:27] offset:512\n\t" \
    "global_load_dwordx4 v[28:31], v4, s[26:27] offset:640\n\t" \
    "global_load_dwordx4 v[32:35], v4, s[26:27] offset:768\n\t" \
    "global_load_dwordx4 v[36:39], v4, s[26:27] offset:896\n\t" \
    "s_lshr_b32 s23, s20, 5\n\t" \
    "s_cmp_ge_u32 s23, 4\n\t" \
    "s_cbranch_scc1 .Lwo_%=\n\t" \
    "s_bitcmp1_b32 s23, 0\n\t" \
    "s_cselect_b32 s30, s14, s12\n\t" \
    "s_cselect_b32 s31, s15, s13\n\t" \
    "s_lshl_b32 s24, s21, 11\n\t" \
    "s_add_u32 s30, s30, s24\n\t" \
    "s_addc_u32 s31, s31, 0\n\t" \
    "s_add_u32 m0, s24, 131072\n\t" \
    "v_lshlrev_b32 v6, 4, v1\n\t" \
    "global_load_lds_dwordx4 v6, s[30:31]\n\t" \
    "global_load_lds_dwordx4 v6, s[30:31] offset:1024\n\t" \
    "s_branch .Lwq_%=\n\t" \
    ".Lwo_%=:\n\t" \
    "s_sub_u32 s24, s20, 128\n\t" \
    "s_lshr_b32 s25, s24, 3\n\t" \
    "s_lshl_b32 s25, s25, 15\n\t" \
    "s_and_b32 s24, s24, 7\n\t" \
    "s_lshl_b32 s24, s24, 7\n\t" \
    "s_add_u32 s24, s24, s25\n\t" \
    "s_lshl_b32 s25, s21, 12\n\t" \
    "s_add_u32 s24, s24, s25\n\t" \
    "s_add_u32 s30, s16, s24\n\t" \
    "s_addc_u32 s31, s17, 0\n\t" \
    "s_lshl_b32 s24, s21, 9\n\t" \
    "s_add_u32 m0, s24, 131072\n\t" \
    "s_mov_b64 exec, 0xffff\n\t" \
    "global_load_lds_dwordx4 v4, s[30:31]\n\t" \
    "s_add_u32 s30, s30, 2048\n\t" \
    "s_addc_u32 s31, s31, 0\n\t" \
    "s_add_u32 m0, m0, 256\n\t" \
    "s_nop 0\n\t" \
    "global_load_lds_dwordx4 v4, s[30:31]\n\t" \
    "s_mov_b64 exec, -1\n\t" \
    ".Lwq_%=:\n\t" \
    "s_lshl_b32 s24, s21, 13\n\t" \
    "s_lshl_b32 s25, s23, 7\n\t" \
    "s_add_u32 s24, s24, s25\n\t" \
    "s_add_u32 s28, s10, s24\n\t" \
    "s_addc_u32 s29, s11, 0\n\t" \
    "v_and_b32 v5, 16, v1\n\t" \
    "v_lshlrev_b32 v5, 2, v5\n\t" \
    "v_xor_b32 v5, v5, v4\n\t" \
    "s_lshl_b32 s24, s21, 10\n\t" \
    "s_add_u32 m0, s24, 65536\n\t" \
    "s_nop 0\n\t" \
    "global_load_lds_dwordx4 v5, s[28:29]\n\t" \
    "s_add_u32 m0, m0, 8192\n\t" \
    "v_add_u32 v5, 0x10000, v5\n\t" \
    "global_load_lds_dwordx4 v5, s[28:29]\n\t" \
    "s_add_u32 m0, m0, 8192\n\t" \
    "v_add_u32 v5, 0x10000, v5\n\t" \
    "global_load_lds_dwordx4 v5, s[28:29]\n\t" \
    "s_add_u32 m0, m0, 8192\n\t" \
    "v_add_u32 v5, 0x10000, v5\n\t" \
    "global_load_lds_dwordx4 v5, s[28:29]\n\t" \
    "s_add_u32 m0, m0, 8192\n\t" \
    "v_add_u32 v5, 0x10000, v5\n\t" \
    "global_load_lds_dwordx4 v5, s[28:29]\n\t" \
    "s_add_u32 m0, m0, 8192\n\t" \
    "v_add_u32 v5, 0x10000, v5\n\t" \
    "global_load_lds_dwordx4 v5, s[28:29]\n\t" \
    "s_add_u32 m0, m0, 8192\n\t" \
    "v_add_u32 v5, 0x10000, v5\n\t" \
    "global_load_lds_dwordx4 v5, s[28:29]\n\t" \
    "s_add_u32 m0, m0, 8192\n\t" \
    "v_add_u32 v5, 0x10000, v5\n\t" \
    "global_load_lds_dwordx4 v5, s[28:29]\n\t" \
    "s_and_b32 s24, s21, 1\n\t" \
    "s_lshl_b32 s24, s24, 2\n\t" \
    "v_lshrrev_b32 v7, 1, v2\n\t" \
    "v_add_u32 v7, s24, v7\n\t" \
    "v_xor_b32 v7, v7, v3\n\t" \
    "v_lshlrev_b32 v7, 4, v7\n\t" \
    "v_lshl_or_b32 v7, v2, 7, v7\n\t" \
    "s_lshl_b32 s24, s21, 10\n\t" \
    "v_add_u32 v56, s24, v7\n\t" \
    "s_lshl_b32 s24, s21, 5\n\t" \
    "s_add_u32 s24, s24, 147456\n\t" \
    "v_lshl_add_u32 v57, v2, 2, s24\n\t" \
    "v_and_b32 v2, 31, v1\n\t" \
    "v_lshrrev_b32 v3, 5, v1\n\t" \
    "v_bfe_u32 v4, v1, 2, 2\n\t" \
    "v_and_b32 v5, 3, v1\n\t" \
    "s_cmp_ge_u32 s21, 4\n\t" \
    "s_cbranch_scc1 .Lah_%=\n\t" \
    "s_and_b32 s24, s21, 1\n\t" \
    "s_lshr_b32 s25, s21, 1\n\t" \
    "v_bfe_u32 v7, v1, 1, 3\n\t" \
    "v_xor_b32 v7, v7, v3\n\t" \
    "v_lshlrev_b32 v7, 4, v7\n\t" \
    "v_lshl_or_b32 v7, v2, 7, v7\n\t" \
    "s_lshl_b32 s30, s24, 12\n\t" \
    "v_add_u32 v80, s30, v7\n\t" \
    "v_xor_b32 v81, 32, v80\n\t" \
    "v_xor_b32 v82, 64, v80\n\t" \
    "v_xor_b32 v83, 0x60, v80\n\t" \
    "v_lshl_add_u32 v7, v3, 3, v4\n\t" \
    "v_lshlrev_b32 v7, 7, v7\n\t" \
    "v_bfe_u32 v6, v1, 4, 1\n\t" \
    "v_lshlrev_b32 v6, 1, v6\n\t" \
    "v_lshrrev_b32 v50, 1, v5\n\t" \
    "v_add_u32 v6, v6, v50\n\t" \
    "s_lshl_b32 s30, s25, 2\n\t" \
    "v_add_u32 v6, s30, v6\n\t" \
    "v_bfe_u32 v50, v4, 1, 1\n\t" \
    "v_lshlrev_b32 v50, 2, v50\n\t" \
    "v_xor_b32 v6, v6, v50\n\t" \
    "v_lshl_add_u32 v7, v6, 4, v7\n\t" \
    "v_and_b32 v6, 1, v5\n\t" \
    "v_lshl_add_u32 v7, v6, 3, v7\n\t" \
    "v_add_u32 v84, 65536, v7\n\t" \
    "s_lshl_b32 s30, s24, 7\n\t" \
    "s_add_u32 s30, s30, 147456\n\t" \
    "v_lshl_add_u32 v85, v2, 2, s30\n\t" \
    "v_lshrrev_b32 v7, 3, v2\n\t" \
    "v_lshlrev_b32 v7, 13, v7\n\t" \
    "v_and_b32 v6, 7, v2\n\t" \
    "v_lshl_or_b32 v7, v6, 4, v7\n\t" \
    "v_lshl_or_b32 v86, v3, 9, v7\n\t" \
    "s_lshl_b32 s30, s22, 1\n\t" \
    "s_add_u32 s30, s30, s24\n\t" \
    "s_lshl_b32 s31, s30, 15\n\t" \
    "s_lshl_b32 s36, s23, 10\n\t" \
    "s_add_u32 s31, s31, s36\n\t" \
    "s_lshl_b32 s36, s25, 8\n\t" \
    "s_add_u32 s31, s31, s36\n\t" \
    "s_add_u32 s26, s18, s31\n\t" \
    "s_addc_u32 s27, s19, 0\n\t" \
    "s_lshl_b32 s31, s30, 8\n\t" \
    "s_lshl_b32 s36, s23, 3\n\t" \
    "s_add_u32 s31, s31, s36\n\t" \
    "s_lshl_b32 s36, s25, 2\n\t" \
    "s_add_u32 s31, s31, s36\n\t" \
    "s_add_u32 s31, s31, 6431744\n\t" \
    "s_add_u32 s28, s18, s31\n\t" \
    "s_addc_u32 s29, s19, 0\n\t" \
    "v_lshrrev_b32 v87, 3, v1\n\t" \
    "v_lshlrev_b32 v87, 6, v87\n\t" \
    "s_branch .Lsum_%=\n\t" \
    ".Lah_%=:\n\t" \
    "s_cmp_ge_u32 s21, 6\n\t" \
    "s_cbranch_scc1 .Lsum_%=\n\t" \
    "s_cmp_ge_u32 s23, 4\n\t" \
    "s_cbranch_scc1 .Lawo_%=\n\t" \
    "s_sub_u32 s24, s21, 4\n\t" \
    "s_lshr_b32 s25, s23, 1\n\t" \
    "v_and_b32 v2, 15, v1\n\t" \
    "v_lshrrev_b32 v3, 4, v1\n\t" \
    "s_lshl_b32 s30, s25, 1\n\t" \
    "s_add_u32 s30, s30, s24\n\t" \
    "s_lshl_b32 s31, s30, 11\n\t" \
    "v_lshrrev_b32 v7, 1, v2\n\t" \
    "v_xor_b32 v7, v7, v3\n\t" \
    "v_lshlrev_b32 v7, 4, v7\n\t" \
    "v_lshl_or_b32 v7, v2, 7, v7\n\t" \
    "v_add_u32 v80, s31, v7\n\t" \
    "v_xor_b32 v81, 64, v80\n\t" \
    "v_lshl_add_u32 v7, v3, 3, v4\n\t" \
    "v_lshlrev_b32 v7, 5, v7\n\t" \
    "v_lshl_add_u32 v7, v5, 3, v7\n\t" \
    "v_add_u32 v84, 131072, v7\n\t" \
    "s_lshl_b32 s36, s22, 1\n\t" \
    "s_add_u32 s36, s36, s25\n\t" \
    "s_lshl_b32 s30, s30, 6\n\t" \
    "s_add_u32 s30, s30, 147456\n\t" \
    "s_bitcmp1_b32 s23, 0\n\t" \
    "s_cbranch_scc1 .Lav_%=\n\t" \
    "v_lshl_add_u32 v85, v2, 2, s30\n\t" \
    "v_and_b32 v7, 3, v2\n\t" \
    "v_and_b32 v6, 4, v2\n\t" \
    "v_lshl_or_b32 v7, v6, 1, v7\n\t" \
    "v_and_b32 v6, 8, v2\n\t" \
    "v_lshrrev_b32 v6, 1, v6\n\t" \
    "v_or_b32 v7, v7, v6\n\t" \
    "s_lshl_b32 s31, s24, 4\n\t" \
    "v_or_b32 v7, s31, v7\n\t" \
    "v_and_b32 v6, 1, v3\n\t" \
    "v_lshl_or_b32 v7, v6, 5, v7\n\t" \
    "v_lshlrev_b32 v7, 4, v7\n\t" \
    "v_lshrrev_b32 v6, 1, v3\n\t" \
    "v_lshl_or_b32 v86, v6, 3, v7\n\t" \
    "s_lshl_b32 s31, s36, 10\n\t" \
    "s_add_u32 s31, s31, 6291456\n\t" \
    "s_add_u32 s26, s18, s31\n\t" \
    "s_addc_u32 s27, s19, 0\n\t" \
    "s_lshl_b32 s31, s36, 1\n\t" \
    "s_add_u32 s31, s31, s24\n\t" \
    "s_lshl_b32 s31, s31, 2\n\t" \
    "s_add_u32 s31, s31, 6430720\n\t" \
    "s_add_u32 s28, s18, s31\n\t" \
    "s_addc_u32 s29, s19, 0\n\t" \
    "s_branch .Lsum_%=\n\t" \
    ".Lav_%=:\n\t" \
    "v_lshl_add_u32 v85, v3, 4, s30\n\t" \
    "v_and_b32 v6, 1, v3\n\t" \
    "v_mul_u32_u24 v7, 288, v6\n\t" \
    "v_lshl_add_u32 v7, v2, 4, v7\n\t" \
    "v_lshrrev_b32 v6, 1, v3\n\t" \
    "v_lshl_add_u32 v86, v6, 3, v7\n\t" \
    "s_mul_i32 s31, s36, 1152\n\t" \
    "s_mul_i32 s37, s24, 576\n\t" \
    "s_add_u32 s31, s31, s37\n\t" \
    "s_add_u32 s31, s31, 6356992\n\t" \
    "s_add_u32 s26, s18, s31\n\t" \
    "s_addc_u32 s27, s19, 0\n\t" \
    "v_bfe_u32 v6, v1, 2, 1\n\t" \
    "v_mul_u32_u24 v7, 288, v6\n\t" \
    "v_and_b32 v6, 3, v1\n\t" \
    "v_lshl_add_u32 v87, v6, 3, v7\n\t" \
    "v_mov_b32 v7, 0x3f803f80\n\t" \
    "v_cmp_gt_u32 vcc, 2, v6\n\t" \
    "s_nop 1\n\t" \
    "v_cndmask_b32 v88, 0, v7, vcc\n\t" \
    "v_mov_b32 v89, v88\n\t" \
    "s_branch .Lsum_%=\n\t" \
    ".Lawo_%=:\n\t" \
    "v_lshrrev_b32 v7, 5, v1\n\t" \
    "v_lshlrev_b32 v7, 4, v7\n\t" \
    "v_bfe_u32 v6, v1, 4, 1\n\t" \
    "v_lshl_add_u32 v7, v6, 3, v7\n\t" \
    "v_lshrrev_b32 v6, 1, v4\n\t" \
    "v_lshl_add_u32 v7, v6, 2, v7\n\t" \
    "v_and_b32 v6, 1, v4\n\t" \
    "v_add_u32 v7, v7, v6\n\t" \
    "v_lshlrev_b32 v7, 7, v7\n\t" \
    "v_lshl_add_u32 v7, v5, 3, v7\n\t" \
    "v_add_u32 v84, 131072, v7\n\t" \
    "v_lshlrev_b32 v86, 4, v1\n\t" \
    "s_sub_u32 s24, s20, 128\n\t" \
    "s_and_b32 s25, s24, 7\n\t" \
    "s_lshl_b32 s25, s25, 16\n\t" \
    "s_lshr_b32 s30, s24, 4\n\t" \
    "s_lshl_b32 s30, s30, 3\n\t" \
    "s_bfe_u32 s31, s24, 0x10003\n\t" \
    "s_add_u32 s30, s30, s31\n\t" \
    "s_lshl_b32 s30, s30, 10\n\t" \
    "s_add_u32 s25, s25, s30\n\t" \
    "s_add_u32 s25, s25, 6451200\n\t" \
    "s_add_u32 s26, s18, s25\n\t" \
    "s_addc_u32 s27, s19, 0\n\t" \
    "s_add_u32 s26, s26, 4096\n\t" \
    "s_addc_u32 s27, s27, 0\n\t" \
    ".Lsum_%=:\n\t" \
    "s_mov_b32 s34, 0x00003f80\n\t" \
    "s_mov_b32 s35, 0x3f800000\n\t" \
    "v_mov_b32 v40, 0\n\t" \
    "v_mov_b32 v41, 0\n\t" \
    "v_mov_b32 v42, 0\n\t" \
    "v_mov_b32 v43, 0\n\t" \
    "v_mov_b32 v60, 0\n\t" \
    "v_mov_b32 v61, 0\n\t" \
    "v_mov_b32 v62, 0\n\t" \
    "v_mov_b32 v63, 0\n\t" \
    "s_waitcnt vmcnt(14)\n\t" \
    "v_dot2c_f32_bf16 v40, 0x3f803f80, v8\n\t" \
    "v_dot2c_f32_bf16 v41, 0x3f803f80, v9\n\t" \
    "v_dot2c_f32_bf16 v42, 0x3f803f80, v10\n\t" \
    "v_dot2c_f32_bf16 v43, 0x3f803f80, v11\n\t" \
    "v_dot2c_f32_bf16 v40, 0x3f803f80, v12\n\t" \
    "v_dot2c_f32_bf16 v41, 0x3f803f80, v13\n\t" \
    "v_dot2c_f32_bf16 v42, 0x3f803f80, v14\n\t" \
    "v_dot2c_f32_bf16 v43, 0x3f803f80, v15\n\t" \
    "v_dot2c_f32_bf16 v40, 0x3f803f80, v16\n\t" \
    "v_dot2c_f32_bf16 v41, 0x3f803f80, v17\n\t" \
    "v_dot2c_f32_bf16 v42, 0x3f803f80, v18\n\t" \
    "v_dot2c_f32_bf16 v43, 0x3f803f80, v19\n\t" \
    "v_dot2c_f32_bf16 v40, 0x3f803f80, v20\n\t" \
    "v_dot2c_f32_bf16 v41, 0x3f803f80, v21\n\t" \
    "v_dot2c_f32_bf16 v42, 0x3f803f80, v22\n\t" \
    "v_dot2c_f32_bf16 v43, 0x3f803f80, v23\n\t" \
    "s_waitcnt vmcnt(10)\n\t" \
    "v_dot2c_f32_bf16 v40, 0x3f803f80, v24\n\t" \
    "v_dot2c_f32_bf16 v41, 0x3f803f80, v25\n\t" \
    "v_dot2c_f32_bf16 v42, 0x3f803f80, v26\n\t" \
    "v_dot2c_f32_bf16 v43, 0x3f803f80, v27\n\t" \
    "v_dot2c_f32_bf16 v40, 0x3f803f80, v28\n\t" \
    "v_dot2c_f32_bf16 v41, 0x3f803f80, v29\n\t" \
    "v_dot2c_f32_bf16 v42, 0x3f803f80, v30\n\t" \
    "v_dot2c_f32_bf16 v43, 0x3f803f80, v31\n\t" \
    "v_dot2c_f32_bf16 v40, 0x3f803f80, v32\n\t" \
    "v_dot2c_f32_bf16 v41, 0x3f803f80, v33\n\t" \
    "v_dot2c_f32_bf16 v42, 0x3f803f80, v34\n\t" \
    "v_dot2c_f32_bf16 v43, 0x3f803f80, v35\n\t" \
    "v_dot2c_f32_bf16 v40, 0x3f803f80, v36\n\t" \
    "v_dot2c_f32_bf16 v41, 0x3f803f80, v37\n\t" \
    "v_dot2c_f32_bf16 v42, 0x3f803f80, v38\n\t" \
    "v_dot2c_f32_bf16 v43, 0x3f803f80, v39\n\t" \
    "s_nop 2\n\t" \
    "v_add_f32 v40, v40, v41\n\t" \
    "v_add_f32 v42, v42, v43\n\t" \
    "v_add_f32 v40, v40, v42\n\t" \
    "s_nop 1\n\t" \
    "v_add_f32_dpp v40, v40, v40 quad_perm:[1,0,3,2] row_mask:0xf bank_mask:0xf\n\t" \
    "s_nop 1\n\t" \
    "v_add_f32_dpp v40, v40, v40 quad_perm:[2,3,0,1] row_mask:0xf bank_mask:0xf\n\t" \
    "s_nop 1\n\t" \
    "v_add_f32_dpp v40, v40, v40 row_half_mirror row_mask:0xf bank_mask:0xf\n\t" \
    "s_nop 0\n\t" \
    "v_mul_f32 v58, 0xbb000000, v40\n\t" \
    "s_nop 0\n\t" \
    "v_dot2_f32_bf16 v40, v8, s34, v58\n\t" \
    "v_dot2_f32_bf16 v41, v8, s35, v58\n\t" \
    "v_dot2_f32_bf16 v42, v9, s34, v58\n\t" \
    "v_dot2_f32_bf16 v43, v9, s35, v58\n\t" \
    "v_dot2_f32_bf16 v44, v10, s34, v58\n\t" \
    "v_dot2_f32_bf16 v45, v10, s35, v58\n\t" \
    "v_dot2_f32_bf16 v46, v11, s34, v58\n\t" \
    "v_dot2_f32_bf16 v47, v11, s35, v58\n\t" \
    "v_dot2_f32_bf16 v48, v12, s34, v58\n\t" \
    "v_dot2_f32_bf16 v49, v12, s35, v58\n\t" \
    "v_dot2_f32_bf16 v50, v13, s34, v58\n\t" \
    "v_dot2_f32_bf16 v51, v13, s35, v58\n\t" \
    "v_dot2_f32_bf16 v52, v14, s34, v58\n\t" \
    "v_dot2_f32_bf16 v53, v14, s35, v58\n\t" \
    "v_dot2_f32_bf16 v54, v15, s34, v58\n\t" \
    "v_dot2_f32_bf16 v55, v15, s35, v58\n\t" \
    "v_cvt_pk_bf16_f32 v8, v40, v41\n\t" \
    "v_cvt_pk_bf16_f32 v9, v42, v43\n\t" \
    "v_cvt_pk_bf16_f32 v10, v44, v45\n\t" \
    "v_cvt_pk_bf16_f32 v11, v46, v47\n\t" \
    "ds_write_b128 v56, v[8:11]\n\t" \
    "v_pk_fma_f32 v[60:61], v[40:41], v[40:41], v[60:61]\n\t" \
    "v_pk_fma_f32 v[62:63], v[42:43], v[42:43], v[62:63]\n\t" \
    "v_pk_fma_f32 v[60:61], v[44:45], v[44:45], v[60:61]\n\t" \
    "v_pk_fma_f32 v[62:63], v[46:47], v[46:47], v[62:63]\n\t" \
    "v_dot2_f32_bf16 v40, v16, s34, v58\n\t" \
    "v_dot2_f32_bf16 v41, v16, s35, v58\n\t" \
    "v_dot2_f32_bf16 v42, v17, s34, v58\n\t" \
    "v_dot2_f32_bf16 v43, v17, s35, v58\n\t" \
    "v_dot2_f32_bf16 v44, v18, s34, v58\n\t" \
    "v_dot2_f32_bf16 v45, v18, s35, v58\n\t" \
    "v_dot2_f32_bf16 v46, v19, s34, v58\n\t" \
    "v_dot2_f32_bf16 v47, v19, s35, v58\n\t" \
    "v_cvt_pk_bf16_f32 v12, v48, v49\n\t" \
    "v_cvt_pk_bf16_f32 v13, v50, v51\n\t" \
    "v_cvt_pk_bf16_f32 v14, v52, v53\n\t" \
    "v_cvt_pk_bf16_f32 v15, v54, v55\n\t" \
    "ds_write_b128 v56, v[12:15] offset:8192\n\t" \
    "v_pk_fma_f32 v[60:61], v[48:49], v[48:49], v[60:61]\n\t" \
    "v_pk_fma_f32 v[62:63], v[50:51], v[50:51], v[62:63]\n\t" \
    "v_pk_fma_f32 v[60:61], v[52:53], v[52:53], v[60:61]\n\t" \
    "v_pk_fma_f32 v[62:63], v[54:55], v[54:55], v[62:63]\n\t" \
    "v_dot2_f32_bf16 v48, v20, s34, v58\n\t" \
    "v_dot2_f32_bf16 v49, v20, s35, v58\n\t" \
    "v_dot2_f32_bf16 v50, v21, s34, v58\n\t" \
    "v_dot2_f32_bf16 v51, v21, s35, v58\n\t" \
    "v_dot2_f32_bf16 v52, v22, s34, v58\n\t" \
    "v_dot2_f32_bf16 v53, v22, s35, v58\n\t" \
    "v_dot2_f32_bf16 v54, v23, s34, v58\n\t" \
    "v_dot2_f32_bf16 v55, v23, s35, v58\n\t" \
    "v_cvt_pk_bf16_f32 v16, v40, v41\n\t" \
    "v_cvt_pk_bf16_f32 v17, v42, v43\n\t" \
    "v_cvt_pk_bf16_f32 v18, v44, v45\n\t" \
    "v_cvt_pk_bf16_f32 v19, v46, v47\n\t" \
    "ds_write_b128 v56, v[16:19] offset:16384\n\t" \
    "v_pk_fma_f32 v[60:61], v[40:41], v[40:41], v[60:61]\n\t" \
    "v_pk_fma_f32 v[62:63], v[42:43], v[42:43], v[62:63]\n\t" \
    "v_pk_fma_f32 v[60:61], v[44:45], v[44:45], v[60:61]\n\t" \
    "v_pk_fma_f32 v[62:63], v[46:47], v[46:47], v[62:63]\n\t" \
    "v_dot2_f32_bf16 v40, v24, s34, v58\n\t" \
    "v_dot2_f32_bf16 v41, v24, s35, v58\n\t" \
    "v_dot2_f32_bf16 v42, v25, s34, v58\n\t" \
    "v_dot2_f32_bf16 v43, v25, s35, v58\n\t" \
    "v_dot2_f32_bf16 v44, v26, s34, v58\n\t" \
    "v_dot2_f32_bf16 v45, v26, s35, v58\n\t" \
    "v_dot2_f32_bf16 v46, v27, s34, v58\n\t" \
    "v_dot2_f32_bf16 v47, v27, s35, v58\n\t" \
    "v_cvt_pk_bf16_f32 v20, v48, v49\n\t" \
    "v_cvt_pk_bf16_f32 v21, v50, v51\n\t" \
    "v_cvt_pk_bf16_f32 v22, v52, v53\n\t" \
    "v_cvt_pk_bf16_f32 v23, v54, v55\n\t" \
    "ds_write_b128 v56, v[20:23] offset:24576\n\t" \
    "v_pk_fma_f32 v[60:61], v[48:49], v[48:49], v[60:61]\n\t" \
    "v_pk_fma_f32 v[62:63], v[50:51], v[50:51], v[62:63]\n\t" \
    "v_pk_fma_f32 v[60:61], v[52:53], v[52:53], v[60:61]\n\t" \
    "v_pk_fma_f32 v[62:63], v[54:55], v[54:55], v[62:63]\n\t" \
    "v_dot2_f32_bf16 v48, v28, s34, v58\n\t" \
    "v_dot2_f32_bf16 v49, v28, s35, v58\n\t" \
    "v_dot2_f32_bf16 v50, v29, s34, v58\n\t" \
    "v_dot2_f32_bf16 v51, v29, s35, v58\n\t" \
    "v_dot2_f32_bf16 v52, v30, s34, v58\n\t" \
    "v_dot2_f32_bf16 v53, v30, s35, v58\n\t" \
    "v_dot2_f32_bf16 v54, v31, s34, v58\n\t" \
    "v_dot2_f32_bf16 v55, v31, s35, v58\n\t" \
    "v_cvt_pk_bf16_f32 v24, v40, v41\n\t" \
    "v_cvt_pk_bf16_f32 v25, v42, v43\n\t" \
    "v_cvt_pk_bf16_f32 v26, v44, v45\n\t" \
    "v_cvt_pk_bf16_f32 v27, v46, v47\n\t" \
    "ds_write_b128 v56, v[24:27] offset:32768\n\t" \
    "v_pk_fma_f32 v[60:61], v[40:41], v[40:41], v[60:61]\n\t" \
    "v_pk_fma_f32 v[62:63], v[42:43], v[42:43], v[62:63]\n\t" \
    "v_pk_fma_f32 v[60:61], v[44:45], v[44:45], v[60:61]\n\t" \
    "v_pk_fma_f32 v[62:63], v[46:47], v[46:47], v[62:63]\n\t" \
    "v_dot2_f32_bf16 v40, v32, s34, v58\n\t" \
    "v_dot2_f32_bf16 v41, v32, s35, v58\n\t" \
    "v_dot2_f32_bf16 v42, v33, s34, v58\n\t" \
    "v_dot2_f32_bf16 v43, v33, s35, v58\n\t" \
    "v_dot2_f32_bf16 v44, v34, s34, v58\n\t" \
    "v_dot2_f32_bf16 v45, v34, s35, v58\n\t" \
    "v_dot2_f32_bf16 v46, v35, s34, v58\n\t" \
    "v_dot2_f32_bf16 v47, v35, s35, v58\n\t" \
    "v_cvt_pk_bf16_f32 v28, v48, v49\n\t" \
    "v_cvt_pk_bf16_f32 v29, v50, v51\n\t" \
    "v_cvt_pk_bf16_f32 v30, v52, v53\n\t" \
    "v_cvt_pk_bf16_f32 v31, v54, v55\n\t" \
    "ds_write_b128 v56, v[28:31] offset:40960\n\t" \
    "v_pk_fma_f32 v[60:61], v[48:49], v[48:49], v[60:61]\n\t" \
    "v_pk_fma_f32 v[62:63], v[50:51], v[50:51], v[62:63]\n\t" \
    "v_pk_fma_f32 v[60:61], v[52:53], v[52:53], v[60:61]\n\t" \
    "v_pk_fma_f32 v[62:63], v[54:55], v[54:55], v[62:63]\n\t" \
    "v_dot2_f32_bf16 v48, v36, s34, v58\n\t" \
    "v_dot2_f32_bf16 v49, v36, s35, v58\n\t" \
    "v_dot2_f32_bf16 v50, v37, s34, v58\n\t" \
    "v_dot2_f32_bf16 v51, v37, s35, v58\n\t" \
    "v_dot2_f32_bf16 v52, v38, s34, v58\n\t" \
    "v_dot2_f32_bf16 v53, v38, s35, v58\n\t" \
    "v_dot2_f32_bf16 v54, v39, s34, v58\n\t" \
    "v_dot2_f32_bf16 v55, v39, s35, v58\n\t" \
    "v_cvt_pk_bf16_f32 v32, v40, v41\n\t" \
    "v_cvt_pk_bf16_f32 v33, v42, v43\n\t" \
    "v_cvt_pk_bf16_f32 v34, v44, v45\n\t" \
    "v_cvt_pk_bf16_f32 v35, v46, v47\n\t" \
    "ds_write_b128 v56, v[32:35] offset:49152\n\t" \
    "v_pk_fma_f32 v[60:61], v[40:41], v[40:41], v[60:61]\n\t" \
    "v_pk_fma_f32 v[62:63], v[42:43], v[42:43], v[62:63]\n\t" \
    "v_pk_fma_f32 v[60:61], v[44:45], v[44:45], v[60:61]\n\t" \
    "v_pk_fma_f32 v[62:63], v[46:47], v[46:47], v[62:63]\n\t" \
    "v_cvt_pk_bf16_f32 v36, v48, v49\n\t" \
    "v_cvt_pk_bf16_f32 v37, v50, v51\n\t" \
    "v_cvt_pk_bf16_f32 v38, v52, v53\n\t" \
    "v_cvt_pk_bf16_f32 v39, v54, v55\n\t" \
    "ds_write_b128 v56, v[36:39] offset:57344\n\t" \
    "v_pk_fma_f32 v[60:61], v[48:49], v[48:49], v[60:61]\n\t" \
    "v_pk_fma_f32 v[62:63], v[50:51], v[50:51], v[62:63]\n\t" \
    "v_pk_fma_f32 v[60:61], v[52:53], v[52:53], v[60:61]\n\t" \
    "v_pk_fma_f32 v[62:63], v[54:55], v[54:55], v[62:63]\n\t" \
    "v_add_f32 v60, v60, v61\n\t" \
    "v_add_f32 v62, v62, v63\n\t" \
    "v_add_f32 v60, v60, v62\n\t" \
    "s_nop 1\n\t" \
    "v_add_f32_dpp v60, v60, v60 quad_perm:[1,0,3,2] row_mask:0xf bank_mask:0xf\n\t" \
    "s_nop 1\n\t" \
    "v_add_f32_dpp v60, v60, v60 quad_perm:[2,3,0,1] row_mask:0xf bank_mask:0xf\n\t" \
    "s_nop 1\n\t" \
    "v_add_f32_dpp v60, v60, v60 row_half_mirror row_mask:0xf bank_mask:0xf\n\t" \
    "s_nop 0\n\t" \
    "v_mul_f32 v61, 0x3b000000, v60\n\t" \
    "v_cvt_pk_bf16_f32 v61, v61, v61\n\t" \
    "v_and_b32 v61, 0xffff0000, v61\n\t" \
    "v_add_f32 v61, 0x3727c5ac, v61\n\t" \
    "v_cvt_pk_bf16_f32 v61, v61, v61\n\t" \
    "v_and_b32 v61, 0xffff0000, v61\n\t" \
    "v_sqrt_f32 v61, v61\n\t" \
    "s_nop 0\n\t" \
    "v_cvt_pk_bf16_f32 v61, v61, v61\n\t" \
    "v_and_b32 v61, 0xffff0000, v61\n\t" \
    "v_rcp_f32 v61, v61\n\t" \
    "s_nop 0\n\t" \
    "ds_write_b32 v57, v61\n\t" \
    "s_waitcnt vmcnt(6) lgkmcnt(0)\n\t" \
    "s_barrier\n\t" \
    "s_cmp_ge_u32 s21, 4\n\t" \
    "s_cbranch_scc1 .Lhelp_%=\n\t" \
    "ds_read_b128 v[8:11], v80\n\t" \
    "ds_read_b64_tr_b16 v[24:25], v84\n\t" \
    "ds_read_b64_tr_b16 v[26:27], v84 offset:512\n\t" \
    "ds_read_b128 v[12:15], v81\n\t" \
    "ds_read_b64_tr_b16 v[28:29], v84 offset:2048\n\t" \
    "ds_read_b64_tr_b16 v[30:31], v84 offset:2560\n\t" \
    "ds_read_b128 v[16:19], v82\n\t" \
    "ds_read_b64_tr_b16 v[32:33], v84 offset:4096\n\t" \
    "ds_read_b64_tr_b16 v[34:35], v84 offset:4608\n\t" \
    "ds_read_b128 v[20:23], v83\n\t" \
    "ds_read_b64_tr_b16 v[36:37], v84 offset:6144\n\t" \
    "ds_read_b64_tr_b16 v[38:39], v84 offset:6656\n\t" \
    "s_waitcnt lgkmcnt(9)\n\t" \
    "v_mfma_f32_32x32x16_bf16 v[64:79], v[24:27], v[8:11], 0\n\t" \
    "ds_read_b128 v[8:11], v80 offset:8192\n\t" \
    "ds_read_b64_tr_b16 v[24:25], v84 offset:8192\n\t" \
    "ds_read_b64_tr_b16 v[26:27], v84 offset:8704\n\t" \
    "s_waitcnt lgkmcnt(9)\n\t" \
    "v_mfma_f32_32x32x16_bf16 v[64:79], v[28:31], v[12:15], v[64:79]\n\t" \
    "ds_read_b128 v[12:15], v81 offset:8192\n\t" \
    "ds_read_b64_tr_b16 v[28:29], v84 offset:10240\n\t" \
    "ds_read_b64_tr_b16 v[30:31], v84 offset:10752\n\t" \
    "s_waitcnt lgkmcnt(9)\n\t" \
    "v_mfma_f32_32x32x16_bf16 v[64:79], v[32:35], v[16:19], v[64:79]\n\t" \
    "ds_read_b128 v[16:19], v82 offset:8192\n\t" \
    "ds_read_b64_tr_b16 v[32:33], v84 offset:12288\n\t" \
    "ds_read_b64_tr_b16 v[34:35], v84 offset:12800\n\t" \
    "s_waitcnt lgkmcnt(9)\n\t" \
    "v_mfma_f32_32x32x16_bf16 v[64:79], v[36:39], v[20:23], v[64:79]\n\t" \
    "ds_read_b128 v[20:23], v83 offset:8192\n\t" \
    "ds_read_b64_tr_b16 v[36:37], v84 offset:14336\n\t" \
    "ds_read_b64_tr_b16 v[38:39], v84 offset:14848\n\t" \
    "s_waitcnt lgkmcnt(9)\n\t" \
    "v_mfma_f32_32x32x16_bf16 v[64:79], v[24:27], v[8:11], v[64:79]\n\t" \
    "s_waitcnt vmcnt(4)\n\t" \
    "s_barrier\n\t" \
    "ds_read_b128 v[8:11], v80 offset:16384\n\t" \
    "ds_read_b64_tr_b16 v[24:25], v84 offset:16384\n\t" \
    "ds_read_b64_tr_b16 v[26:27], v84 offset:16896\n\t" \
    "s_waitcnt lgkmcnt(9)\n\t" \
    "v_mfma_f32_32x32x16_bf16 v[64:79], v[28:31], v[12:15], v[64:79]\n\t" \
    "ds_read_b128 v[12:15], v81 offset:16384\n\t" \
    "ds_read_b64_tr_b16 v[28:29], v84 offset:18432\n\t" \
    "ds_read_b64_tr_b16 v[30:31], v84 offset:18944\n\t" \
    "s_waitcnt lgkmcnt(9)\n\t" \
    "v_mfma_f32_32x32x16_bf16 v[64:79], v[32:35], v[16:19], v[64:79]\n\t" \
    "ds_read_b128 v[16:19], v82 offset:16384\n\t" \
    "ds_read_b64_tr_b16 v[32:33], v84 offset:20480\n\t" \
    "ds_read_b64_tr_b16 v[34:35], v84 offset:20992\n\t" \
    "s_waitcnt lgkmcnt(9)\n\t" \
    "v_mfma_f32_32x32x16_bf16 v[64:79], v[36:39], v[20:23], v[64:79]\n\t" \
    "ds_read_b128 v[20:23], v83 offset:16384\n\t" \
    "ds_read_b64_tr_b16 v[36:37], v84 offset:22528\n\t" \
    "ds_read_b64_tr_b16 v[38:39], v84 offset:23040\n\t" \
    "s_waitcnt lgkmcnt(9)\n\t" \
    "v_mfma_f32_32x32x16_bf16 v[64:79], v[24:27], v[8:11], v[64:79]\n\t" \
    "ds_read_b128 v[8:11], v80 offset:24576\n\t" \
    "ds_read_b64_tr_b16 v[24:25], v84 offset:24576\n\t" \
    "ds_read_b64_tr_b16 v[26:27], v84 offset:25088\n\t" \
    "s_waitcnt lgkmcnt(9)\n\t" \
    "v_mfma_f32_32x32x16_bf16 v[64:79], v[28:31], v[12:15], v[64:79]\n\t" \
    "ds_read_b128 v[12:15], v81 offset:24576\n\t" \
    "ds_read_b64_tr_b16 v[28:29], v84 offset:26624\n\t" \
    "ds_read_b64_tr_b16 v[30:31], v84 offset:27136\n\t" \
    "s_waitcnt lgkmcnt(9)\n\t" \
    "v_mfma_f32_32x32x16_bf16 v[64:79], v[32:35], v[16:19], v[64:79]\n\t" \
    "ds_read_b128 v[16:19], v82 offset:24576\n\t" \
    "ds_read_b64_tr_b16 v[32:33], v84 offset:28672\n\t" \
    "ds_read_b64_tr_b16 v[34:35], v84 offset:29184\n\t" \
    "s_waitcnt lgkmcnt(9)\n\t" \
    "v_mfma_f32_32x32x16_bf16 v[64:79], v[36:39], v[20:23], v[64:79]\n\t" \
    "ds_read_b128 v[20:23], v83 offset:24576\n\t" \
    "ds_read_b64_tr_b16 v[36:37], v84 offset:30720\n\t" \
    "ds_read_b64_tr_b16 v[38:39], v84 offset:31232\n\t" \
    "s_waitcnt lgkmcnt(9)\n\t" \
    "v_mfma_f32_32x32x16_bf16 v[64:79], v[24:27], v[8:11], v[64:79]\n\t" \
    "s_waitcnt vmcnt(2)\n\t" \
    "s_barrier\n\t" \
    "ds_read_b128 v[8:11], v80 offset:32768\n\t" \
    "ds_read_b64_tr_b16 v[24:25], v84 offset:32768\n\t" \
    "ds_read_b64_tr_b16 v[26:27], v84 offset:33280\n\t" \
    "s_waitcnt lgkmcnt(9)\n\t" \
    "v_mfma_f32_32x32x16_bf16 v[64:79], v[28:31], v[12:15], v[64:79]\n\t" \
    "ds_read_b128 v[12:15], v81 offset:32768\n\t" \
    "ds_read_b64_tr_b16 v[28:29], v84 offset:34816\n\t" \
    "ds_read_b64_tr_b16 v[30:31], v84 offset:35328\n\t" \
    "s_waitcnt lgkmcnt(9)\n\t" \
    "v_mfma_f32_32x32x16_bf16 v[64:79], v[32:35], v[16:19], v[64:79]\n\t" \
    "ds_read_b128 v[16:19], v82 offset:32768\n\t" \
    "ds_read_b64_tr_b16 v[32:33], v84 offset:36864\n\t" \
    "ds_read_b64_tr_b16 v[34:35], v84 offset:37376\n\t" \
    "s_waitcnt lgkmcnt(9)\n\t" \
    "v_mfma_f32_32x32x16_bf16 v[64:79], v[36:39], v[20:23], v[64:79]\n\t" \
    "ds_read_b128 v[20:23], v83 offset:32768\n\t" \
    "ds_read_b64_tr_b16 v[36:37], v84 offset:38912\n\t" \
    "ds_read_b64_tr_b16 v[38:39], v84 offset:39424\n\t" \
    "s_waitcnt lgkmcnt(9)\n\t" \
    "v_mfma_f32_32x32x16_bf16 v[64:79], v[24:27], v[8:11], v[64:79]\n\t" \
    "ds_read_b128 v[8:11], v80 offset:40960\n\t" \
    "ds_read_b64_tr_b16 v[24:25], v84 offset:40960\n\t" \
    "ds_read_b64_tr_b16 v[26:27], v84 offset:41472\n\t" \
    "s_waitcnt lgkmcnt(9)\n\t" \
    "v_mfma_f32_32x32x16_bf16 v[64:79], v[28:31], v[12:15], v[64:79]\n\t" \
    "ds_read_b128 v[12:15], v81 offset:40960\n\t" \
    "ds_read_b64_tr_b16 v[28:29], v84 offset:43008\n\t" \
    "ds_read_b64_tr_b16 v[30:31], v84 offset:43520\n\t" \
    "s_waitcnt lgkmcnt(9)\n\t" \
    "v_mfma_f32_32x32x16_bf16 v[64:79], v[32:35], v[16:19], v[64:79]\n\t" \
    "ds_read_b128 v[16:19], v82 offset:40960\n\t" \
    "ds_read_b64_tr_b16 v[32:33], v84 offset:45056\n\t" \
    "ds_read_b64_tr_b16 v[34:35], v84 offset:45568\n\t" \
    "s_waitcnt lgkmcnt(9)\n\t" \
    "v_mfma_f32_32x32x16_bf16 v[64:79], v[36:39], v[20:23], v[64:79]\n\t" \
    "ds_read_b128 v[20:23], v83 offset:40960\n\t" \
    "ds_read_b64_tr_b16 v[36:37], v84 offset:47104\n\t" \
    "ds_read_b64_tr_b16 v[38:39], v84 offset:47616\n\t" \
    "s_waitcnt lgkmcnt(9)\n\t" \
    "v_mfma_f32_32x32x16_bf16 v[64:79], v[24:27], v[8:11], v[64:79]\n\t" \
    "s_waitcnt vmcnt(0)\n\t" \
    "s_barrier\n\t" \
    "ds_read_b128 v[8:11], v80 offset:49152\n\t" \
    "ds_read_b64_tr_b16 v[24:25], v84 offset:49152\n\t" \
    "ds_read_b64_tr_b16 v[26:27], v84 offset:49664\n\t" \
    "s_waitcnt lgkmcnt(9)\n\t" \
    "v_mfma_f32_32x32x16_bf16 v[64:79], v[28:31], v[12:15], v[64:79]\n\t" \
    "ds_read_b128 v[12:15], v81 offset:49152\n\t" \
    "ds_read_b64_tr_b16 v[28:29], v84 offset:51200\n\t" \
    "ds_read_b64_tr_b16 v[30:31], v84 offset:51712\n\t" \
    "s_waitcnt lgkmcnt(9)\n\t" \
    "v_mfma_f32_32x32x16_bf16 v[64:79], v[32:35], v[16:19], v[64:79]\n\t" \
    "ds_read_b128 v[16:19], v82 offset:49152\n\t" \
    "ds_read_b64_tr_b16 v[32:33], v84 offset:53248\n\t" \
    "ds_read_b64_tr_b16 v[34:35], v84 offset:53760\n\t" \
    "s_waitcnt lgkmcnt(9)\n\t" \
    "v_mfma_f32_32x32x16_bf16 v[64:79], v[36:39], v[20:23], v[64:79]\n\t" \
    "ds_read_b128 v[20:23], v83 offset:49152\n\t" \
    "ds_read_b64_tr_b16 v[36:37], v84 offset:55296\n\t" \
    "ds_read_b64_tr_b16 v[38:39], v84 offset:55808\n\t" \
    "s_waitcnt lgkmcnt(9)\n\t" \
    "v_mfma_f32_32x32x16_bf16 v[64:79], v[24:27], v[8:11], v[64:79]\n\t" \
    "ds_read_b128 v[8:11], v80 offset:57344\n\t" \
    "ds_read_b64_tr_b16 v[24:25], v84 offset:57344\n\t" \
    "ds_read_b64_tr_b16 v[26:27], v84 offset:57856\n\t" \
    "s_waitcnt lgkmcnt(9)\n\t" \
    "v_mfma_f32_32x32x16_bf16 v[64:79], v[28:31], v[12:15], v[64:79]\n\t" \
    "ds_read_b128 v[12:15], v81 offset:57344\n\t" \
    "ds_read_b64_tr_b16 v[28:29], v84 offset:59392\n\t" \
    "ds_read_b64_tr_b16 v[30:31], v84 offset:59904\n\t" \
    "s_waitcnt lgkmcnt(9)\n\t" \
    "v_mfma_f32_32x32x16_bf16 v[64:79], v[32:35], v[16:19], v[64:79]\n\t" \
    "ds_read_b128 v[16:19], v82 offset:57344\n\t" \
    "ds_read_b64_tr_b16 v[32:33], v84 offset:61440\n\t" \
    "ds_read_b64_tr_b16 v[34:35], v84 offset:61952\n\t" \
    "s_waitcnt lgkmcnt(9)\n\t" \
    "v_mfma_f32_32x32x16_bf16 v[64:79], v[36:39], v[20:23], v[64:79]\n\t" \
    "ds_read_b128 v[20:23], v83 offset:57344\n\t" \
    "ds_read_b64_tr_b16 v[36:37], v84 offset:63488\n\t" \
    "ds_read_b64_tr_b16 v[38:39], v84 offset:64000\n\t" \
    "ds_read_b32 v48, v85\n\t" \
    "s_waitcnt lgkmcnt(10)\n\t" \
    "v_mfma_f32_32x32x16_bf16 v[64:79], v[24:27], v[8:11], v[64:79]\n\t" \
    "s_waitcnt lgkmcnt(7)\n\t" \
    "v_mfma_f32_32x32x16_bf16 v[64:79], v[28:31], v[12:15], v[64:79]\n\t" \
    "s_waitcnt lgkmcnt(4)\n\t" \
    "v_mfma_f32_32x32x16_bf16 v[64:79], v[32:35], v[16:19], v[64:79]\n\t" \
    "s_waitcnt lgkmcnt(1)\n\t" \
    "v_mfma_f32_32x32x16_bf16 v[64:79], v[36:39], v[20:23], v[64:79]\n\t" \
    "s_waitcnt lgkmcnt(0)\n\t" \
    "v_mul_f32 v48, 0x3eb8aa3b, v48\n\t" \
    "s_nop 10\n\t" \
    "v_mul_f32 v64, v64, v48\n\t" \
    "v_mul_f32 v65, v65, v48\n\t" \
    "v_mul_f32 v66, v66, v48\n\t" \
    "v_mul_f32 v67, v67, v48\n\t" \
    "v_mul_f32 v68, v68, v48\n\t" \
    "v_mul_f32 v69, v69, v48\n\t" \
    "v_mul_f32 v70, v70, v48\n\t" \
    "v_mul_f32 v71, v71, v48\n\t" \
    "v_mul_f32 v72, v72, v48\n\t" \
    "v_mul_f32 v73, v73, v48\n\t" \
    "v_mul_f32 v74, v74, v48\n\t" \
    "v_mul_f32 v75, v75, v48\n\t" \
    "v_mul_f32 v76, v76, v48\n\t" \
    "v_mul_f32 v77, v77, v48\n\t" \
    "v_mul_f32 v78, v78, v48\n\t" \
    "v_mul_f32 v79, v79, v48\n\t" \
    "v_cvt_pk_bf16_f32 v8, v64, v65\n\t" \
    "v_cvt_pk_bf16_f32 v9, v66, v67\n\t" \
    "v_cvt_pk_bf16_f32 v10, v68, v69\n\t" \
    "v_cvt_pk_bf16_f32 v11, v70, v71\n\t" \
    "global_store_dwordx4 v86, v[8:11], s[26:27]\n\t" \
    "v_cvt_pk_bf16_f32 v12, v72, v73\n\t" \
    "v_cvt_pk_bf16_f32 v13, v74, v75\n\t" \
    "v_cvt_pk_bf16_f32 v14, v76, v77\n\t" \
    "v_cvt_pk_bf16_f32 v15, v78, v79\n\t" \
    "global_store_dwordx4 v86, v[12:15], s[26:27] offset:128\n\t" \
    "v_mul_f32 v40, v64, v64\n\t" \
    "v_mul_f32 v41, v72, v72\n\t" \
    "v_fmac_f32 v40, v65, v65\n\t" \
    "v_fmac_f32 v41, v73, v73\n\t" \
    "v_fmac_f32 v40, v66, v66\n\t" \
    "v_fmac_f32 v41, v74, v74\n\t" \
    "v_fmac_f32 v40, v67, v67\n\t" \
    "v_fmac_f32 v41, v75, v75\n\t" \
    "v_fmac_f32 v40, v68, v68\n\t" \
    "v_fmac_f32 v41, v76, v76\n\t" \
    "v_fmac_f32 v40, v69, v69\n\t" \
    "v_fmac_f32 v41, v77, v77\n\t" \
    "v_fmac_f32 v40, v70, v70\n\t" \
    "v_fmac_f32 v41, v78, v78\n\t" \
    "v_fmac_f32 v40, v71, v71\n\t" \
    "v_fmac_f32 v41, v79, v79\n\t" \
    "v_mov_b32 v42, v40\n\t" \
    "v_mov_b32 v43, v41\n\t" \
    "s_nop 0\n\t" \
    "v_permlane32_swap_b32 v42, v40\n\t" \
    "v_permlane32_swap_b32 v43, v41\n\t" \
    "s_nop 1\n\t" \
    "v_add_f32 v40, v40, v42\n\t" \
    "v_add_f32 v41, v41, v43\n\t" \
    "v_max_f32 v40, v40, v41\n\t" \
    "s_nop 1\n\t" \
    "v_max_f32_dpp v40, v40, v40 quad_perm:[1,0,3,2] row_mask:0xf bank_mask:0xf\n\t" \
    "s_nop 1\n\t" \
    "v_max_f32_dpp v40, v40, v40 quad_perm:[2,3,0,1] row_mask:0xf bank_mask:0xf\n\t" \
    "s_nop 1\n\t" \
    "v_max_f32_dpp v40, v40, v40 row_half_mirror row_mask:0xf bank_mask:0xf\n\t" \
    "s_nop 0\n\t" \
    "s_mov_b64 exec, 0x01010101\n\t" \
    "global_store_dword v87, v40, s[28:29]\n\t" \
    "s_branch .Lend_%=\n\t" \
    ".Lhelp_%=:\n\t" \
    "s_cmp_ge_u32 s21, 6\n\t" \
    "s_cbranch_scc1 .Lidle_%=\n\t" \
    "s_cmp_ge_u32 s23, 4\n\t" \
    "s_cbranch_scc1 .Lwor_%=\n\t" \
    "s_bitcmp1_b32 s23, 0\n\t" \
    "s_cbranch_scc1 .Lv_%=\n\t" \
    "ds_read_b128 v[8:11], v80\n\t" \
    "ds_read_b64_tr_b16 v[24:25], v84\n\t" \
    "ds_read_b64_tr_b16 v[26:27], v84 offset:128\n\t" \
    "ds_read_b128 v[12:15], v81\n\t" \
    "ds_read_b64_tr_b16 v[28:29], v84 offset:1024\n\t" \
    "ds_read_b64_tr_b16 v[30:31], v84 offset:1152\n\t" \
    "ds_read_b128 v[16:19], v80 offset:8192\n\t" \
    "ds_read_b64_tr_b16 v[32:33], v84 offset:2048\n\t" \
    "ds_read_b64_tr_b16 v[34:35], v84 offset:2176\n\t" \
    "ds_read_b128 v[20:23], v81 offset:8192\n\t" \
    "ds_read_b64_tr_b16 v[36:37], v84 offset:3072\n\t" \
    "ds_read_b64_tr_b16 v[38:39], v84 offset:3200\n\t" \
    "s_waitcnt lgkmcnt(9)\n\t" \
    "v_mfma_f32_16x16x32_bf16 v[64:67], v[24:27], v[8:11], 0\n\t" \
    "ds_read_b128 v[8:11], v80 offset:16384\n\t" \
    "ds_read_b64_tr_b16 v[24:25], v84 offset:4096\n\t" \
    "ds_read_b64_tr_b16 v[26:27], v84 offset:4224\n\t" \
    "s_waitcnt lgkmcnt(9)\n\t" \
    "v_mfma_f32_16x16x32_bf16 v[64:67], v[28:31], v[12:15], v[64:67]\n\t" \
    "ds_read_b128 v[12:15], v81 offset:16384\n\t" \
    "ds_read_b64_tr_b16 v[28:29], v84 offset:5120\n\t" \
    "ds_read_b64_tr_b16 v[30:31], v84 offset:5248\n\t" \
    "s_waitcnt lgkmcnt(9)\n\t" \
    "v_mfma_f32_16x16x32_bf16 v[64:67], v[32:35], v[16:19], v[64:67]\n\t" \
    "ds_read_b128 v[16:19], v80 offset:24576\n\t" \
    "ds_read_b64_tr_b16 v[32:33], v84 offset:6144\n\t" \
    "ds_read_b64_tr_b16 v[34:35], v84 offset:6272\n\t" \
    "s_waitcnt vmcnt(4)\n\t" \
    "s_barrier\n\t" \
    "s_waitcnt lgkmcnt(9)\n\t" \
    "v_mfma_f32_16x16x32_bf16 v[64:67], v[36:39], v[20:23], v[64:67]\n\t" \
    "ds_read_b128 v[20:23], v81 offset:24576\n\t" \
    "ds_read_b64_tr_b16 v[36:37], v84 offset:7168\n\t" \
    "ds_read_b64_tr_b16 v[38:39], v84 offset:7296\n\t" \
    "s_waitcnt lgkmcnt(9)\n\t" \
    "v_mfma_f32_16x16x32_bf16 v[64:67], v[24:27], v[8:11], v[64:67]\n\t" \
    "ds_read_b128 v[8:11], v80 offset:32768\n\t" \
    "ds_read_b64_tr_b16 v[24:25], v84 offset:8192\n\t" \
    "ds_read_b64_tr_b16 v[26:27], v84 offset:8320\n\t" \
    "s_waitcnt lgkmcnt(9)\n\t" \
    "v_mfma_f32_16x16x32_bf16 v[64:67], v[28:31], v[12:15], v[64:67]\n\t" \
    "ds_read_b128 v[12:15], v81 offset:32768\n\t" \
    "ds_read_b64_tr_b16 v[28:29], v84 offset:9216\n\t" \
    "ds_read_b64_tr_b16 v[30:31], v84 offset:9344\n\t" \
    "s_waitcnt lgkmcnt(9)\n\t" \
    "v_mfma_f32_16x16x32_bf16 v[64:67], v[32:35], v[16:19], v[64:67]\n\t" \
    "ds_read_b128 v[16:19], v80 offset:40960\n\t" \
    "ds_read_b64_tr_b16 v[32:33], v84 offset:10240\n\t" \
    "ds_read_b64_tr_b16 v[34:35], v84 offset:10368\n\t" \
    "s_waitcnt lgkmcnt(9)\n\t" \
    "v_mfma_f32_16x16x32_bf16 v[64:67], v[36:39], v[20:23], v[64:67]\n\t" \
    "ds_read_b128 v[20:23], v81 offset:40960\n\t" \
    "ds_read_b64_tr_b16 v[36:37], v84 offset:11264\n\t" \
    "ds_read_b64_tr_b16 v[38:39], v84 offset:11392\n\t" \
    "s_waitcnt vmcnt(2)\n\t" \
    "s_barrier\n\t" \
    "s_waitcnt lgkmcnt(9)\n\t" \
    "v_mfma_f32_16x16x32_bf16 v[64:67], v[24:27], v[8:11], v[64:67]\n\t" \
    "ds_read_b128 v[8:11], v80 offset:49152\n\t" \
    "ds_read_b64_tr_b16 v[24:25], v84 offset:12288\n\t" \
    "ds_read_b64_tr_b16 v[26:27], v84 offset:12416\n\t" \
    "s_waitcnt lgkmcnt(9)\n\t" \
    "v_mfma_f32_16x16x32_bf16 v[64:67], v[28:31], v[12:15], v[64:67]\n\t" \
    "ds_read_b128 v[12:15], v81 offset:49152\n\t" \
    "ds_read_b64_tr_b16 v[28:29], v84 offset:13312\n\t" \
    "ds_read_b64_tr_b16 v[30:31], v84 offset:13440\n\t" \
    "s_waitcnt lgkmcnt(9)\n\t" \
    "v_mfma_f32_16x16x32_bf16 v[64:67], v[32:35], v[16:19], v[64:67]\n\t" \
    "ds_read_b128 v[16:19], v80 offset:57344\n\t" \
    "ds_read_b64_tr_b16 v[32:33], v84 offset:14336\n\t" \
    "ds_read_b64_tr_b16 v[34:35], v84 offset:14464\n\t" \
    "s_waitcnt lgkmcnt(9)\n\t" \
    "v_mfma_f32_16x16x32_bf16 v[64:67], v[36:39], v[20:23], v[64:67]\n\t" \
    "ds_read_b128 v[20:23], v81 offset:57344\n\t" \
    "ds_read_b64_tr_b16 v[36:37], v84 offset:15360\n\t" \
    "ds_read_b64_tr_b16 v[38:39], v84 offset:15488\n\t" \
    "ds_read_b32 v44, v85\n\t" \
    "s_waitcnt lgkmcnt(10)\n\t" \
    "v_mfma_f32_16x16x32_bf16 v[64:67], v[24:27], v[8:11], v[64:67]\n\t" \
    "s_waitcnt vmcnt(0)\n\t" \
    "s_barrier\n\t" \
    "s_waitcnt lgkmcnt(7)\n\t" \
    "v_mfma_f32_16x16x32_bf16 v[64:67], v[28:31], v[12:15], v[64:67]\n\t" \
    "s_waitcnt lgkmcnt(4)\n\t" \
    "v_mfma_f32_16x16x32_bf16 v[64:67], v[32:35], v[16:19], v[64:67]\n\t" \
    "s_waitcnt lgkmcnt(1)\n\t" \
    "v_mfma_f32_16x16x32_bf16 v[64:67], v[36:39], v[20:23], v[64:67]\n\t" \
    "s_waitcnt lgkmcnt(0)\n\t" \
    "s_nop 7\n\t" \
    "v_mul_f32 v64, v64, v44\n\t" \
    "v_mul_f32 v65, v65, v44\n\t" \
    "v_mul_f32 v66, v66, v44\n\t" \
    "v_mul_f32 v67, v67, v44\n\t" \
    "v_cvt_pk_bf16_f32 v8, v64, v65\n\t" \
    "v_cvt_pk_bf16_f32 v9, v66, v67\n\t" \
    "global_store_dwordx2 v86, v[8:9], s[26:27]\n\t" \
    "v_mul_f32 v40, v64, v64\n\t" \
    "v_fmac_f32 v40, v65, v65\n\t" \
    "v_fmac_f32 v40, v66, v66\n\t" \
    "v_fmac_f32 v40, v67, v67\n\t" \
    "v_mov_b32 v41, v40\n\t" \
    "s_nop 1\n\t" \
    "v_permlane16_swap_b32 v41, v40\n\t" \
    "s_nop 1\n\t" \
    "v_add_f32 v40, v40, v41\n\t" \
    "v_mov_b32 v41, v40\n\t" \
    "s_nop 1\n\t" \
    "v_permlane32_swap_b32 v41, v40\n\t" \
    "s_nop 1\n\t" \
    "v_add_f32 v40, v40, v41\n\t" \
    "s_nop 1\n\t" \
    "v_max_f32_dpp v40, v40, v40 row_ror:8 row_mask:0xf bank_mask:0xf\n\t" \
    "s_nop 1\n\t" \
    "v_max_f32_dpp v40, v40, v40 row_ror:4 row_mask:0xf bank_mask:0xf\n\t" \
    "s_nop 1\n\t" \
    "v_max_f32_dpp v40, v40, v40 row_ror:2 row_mask:0xf bank_mask:0xf\n\t" \
    "s_nop 1\n\t" \
    "v_max_f32_dpp v40, v40, v40 row_ror:1 row_mask:0xf bank_mask:0xf\n\t" \
    "s_nop 1\n\t" \
    "v_mov_b32 v7, 0\n\t" \
    "s_mov_b64 exec, 1\n\t" \
    "global_store_dword v7, v40, s[28:29]\n\t" \
    "s_branch .Lend_%=\n\t" \
    ".Lv_%=:\n\t" \
    "ds_read_b128 v[8:11], v80\n\t" \
    "ds_read_b64_tr_b16 v[24:25], v84\n\t" \
    "ds_read_b64_tr_b16 v[26:27], v84 offset:128\n\t" \
    "ds_read_b128 v[12:15], v81\n\t" \
    "ds_read_b64_tr_b16 v[28:29], v84 offset:1024\n\t" \
    "ds_read_b64_tr_b16 v[30:31], v84 offset:1152\n\t" \
    "ds_read_b128 v[16:19], v80 offset:8192\n\t" \
    "ds_read_b64_tr_b16 v[32:33], v84 offset:2048\n\t" \
    "ds_read_b64_tr_b16 v[34:35], v84 offset:2176\n\t" \
    "ds_read_b128 v[20:23], v81 offset:8192\n\t" \
    "ds_read_b64_tr_b16 v[36:37], v84 offset:3072\n\t" \
    "ds_read_b64_tr_b16 v[38:39], v84 offset:3200\n\t" \
    "s_waitcnt lgkmcnt(9)\n\t" \
    "v_mfma_f32_16x16x32_bf16 v[64:67], v[8:11], v[24:27], 0\n\t" \
    "ds_read_b128 v[8:11], v80 offset:16384\n\t" \
    "ds_read_b64_tr_b16 v[24:25], v84 offset:4096\n\t" \
    "ds_read_b64_tr_b16 v[26:27], v84 offset:4224\n\t" \
    "s_waitcnt lgkmcnt(9)\n\t" \
    "v_mfma_f32_16x16x32_bf16 v[64:67], v[12:15], v[28:31], v[64:67]\n\t" \
    "ds_read_b128 v[12:15], v81 offset:16384\n\t" \
    "ds_read_b64_tr_b16 v[28:29], v84 offset:5120\n\t" \
    "ds_read_b64_tr_b16 v[30:31], v84 offset:5248\n\t" \
    "s_waitcnt lgkmcnt(9)\n\t" \
    "v_mfma_f32_16x16x32_bf16 v[64:67], v[16:19], v[32:35], v[64:67]\n\t" \
    "ds_read_b128 v[16:19], v80 offset:24576\n\t" \
    "ds_read_b64_tr_b16 v[32:33], v84 offset:6144\n\t" \
    "ds_read_b64_tr_b16 v[34:35], v84 offset:6272\n\t" \
    "s_waitcnt vmcnt(4)\n\t" \
    "s_barrier\n\t" \
    "s_waitcnt lgkmcnt(9)\n\t" \
    "v_mfma_f32_16x16x32_bf16 v[64:67], v[20:23], v[36:39], v[64:67]\n\t" \
    "ds_read_b128 v[20:23], v81 offset:24576\n\t" \
    "ds_read_b64_tr_b16 v[36:37], v84 offset:7168\n\t" \
    "ds_read_b64_tr_b16 v[38:39], v84 offset:7296\n\t" \
    "s_waitcnt lgkmcnt(9)\n\t" \
    "v_mfma_f32_16x16x32_bf16 v[64:67], v[8:11], v[24:27], v[64:67]\n\t" \
    "ds_read_b128 v[8:11], v80 offset:32768\n\t" \
    "ds_read_b64_tr_b16 v[24:25], v84 offset:8192\n\t" \
    "ds_read_b64_tr_b16 v[26:27], v84 offset:8320\n\t" \
    "s_waitcnt lgkmcnt(9)\n\t" \
    "v_mfma_f32_16x16x32_bf16 v[64:67], v[12:15], v[28:31], v[64:67]\n\t" \
    "ds_read_b128 v[12:15], v81 offset:32768\n\t" \
    "ds_read_b64_tr_b16 v[28:29], v84 offset:9216\n\t" \
    "ds_read_b64_tr_b16 v[30:31], v84 offset:9344\n\t" \
    "s_waitcnt lgkmcnt(9)\n\t" \
    "v_mfma_f32_16x16x32_bf16 v[64:67], v[16:19], v[32:35], v[64:67]\n\t" \
    "ds_read_b128 v[16:19], v80 offset:40960\n\t" \
    "ds_read_b64_tr_b16 v[32:33], v84 offset:10240\n\t" \
    "ds_read_b64_tr_b16 v[34:35], v84 offset:10368\n\t" \
    "s_waitcnt lgkmcnt(9)\n\t" \
    "v_mfma_f32_16x16x32_bf16 v[64:67], v[20:23], v[36:39], v[64:67]\n\t" \
    "ds_read_b128 v[20:23], v81 offset:40960\n\t" \
    "ds_read_b64_tr_b16 v[36:37], v84 offset:11264\n\t" \
    "ds_read_b64_tr_b16 v[38:39], v84 offset:11392\n\t" \
    "s_waitcnt vmcnt(2)\n\t" \
    "s_barrier\n\t" \
    "s_waitcnt lgkmcnt(9)\n\t" \
    "v_mfma_f32_16x16x32_bf16 v[64:67], v[8:11], v[24:27], v[64:67]\n\t" \
    "ds_read_b128 v[8:11], v80 offset:49152\n\t" \
    "ds_read_b64_tr_b16 v[24:25], v84 offset:12288\n\t" \
    "ds_read_b64_tr_b16 v[26:27], v84 offset:12416\n\t" \
    "s_waitcnt lgkmcnt(9)\n\t" \
    "v_mfma_f32_16x16x32_bf16 v[64:67], v[12:15], v[28:31], v[64:67]\n\t" \
    "ds_read_b128 v[12:15], v81 offset:49152\n\t" \
    "ds_read_b64_tr_b16 v[28:29], v84 offset:13312\n\t" \
    "ds_read_b64_tr_b16 v[30:31], v84 offset:13440\n\t" \
    "s_waitcnt lgkmcnt(9)\n\t" \
    "v_mfma_f32_16x16x32_bf16 v[64:67], v[16:19], v[32:35], v[64:67]\n\t" \
    "ds_read_b128 v[16:19], v80 offset:57344\n\t" \
    "ds_read_b64_tr_b16 v[32:33], v84 offset:14336\n\t" \
    "ds_read_b64_tr_b16 v[34:35], v84 offset:14464\n\t" \
    "s_waitcnt lgkmcnt(9)\n\t" \
    "v_mfma_f32_16x16x32_bf16 v[64:67], v[20:23], v[36:39], v[64:67]\n\t" \
    "ds_read_b128 v[20:23], v81 offset:57344\n\t" \
    "ds_read_b64_tr_b16 v[36:37], v84 offset:15360\n\t" \
    "ds_read_b64_tr_b16 v[38:39], v84 offset:15488\n\t" \
    "ds_read_b128 v[44:47], v85\n\t" \
    "s_waitcnt lgkmcnt(10)\n\t" \
    "v_mfma_f32_16x16x32_bf16 v[64:67], v[8:11], v[24:27], v[64:67]\n\t" \
    "s_waitcnt vmcnt(0)\n\t" \
    "s_barrier\n\t" \
    "s_waitcnt lgkmcnt(7)\n\t" \
    "v_mfma_f32_16x16x32_bf16 v[64:67], v[12:15], v[28:31], v[64:67]\n\t" \
    "s_waitcnt lgkmcnt(4)\n\t" \
    "v_mfma_f32_16x16x32_bf16 v[64:67], v[16:19], v[32:35], v[64:67]\n\t" \
    "s_waitcnt lgkmcnt(1)\n\t" \
    "v_mfma_f32_16x16x32_bf16 v[64:67], v[20:23], v[36:39], v[64:67]\n\t" \
    "s_waitcnt lgkmcnt(0)\n\t" \
    "s_nop 7\n\t" \
    "v_mul_f32 v64, v64, v44\n\t" \
    "v_mul_f32 v65, v65, v45\n\t" \
    "v_mul_f32 v66, v66, v46\n\t" \
    "v_mul_f32 v67, v67, v47\n\t" \
    "v_cvt_pk_bf16_f32 v8, v64, v65\n\t" \
    "v_cvt_pk_bf16_f32 v9, v66, v67\n\t" \
    "global_store_dwordx2 v86, v[8:9], s[26:27]\n\t" \
    "s_mov_b64 exec, 0xff\n\t" \
    "global_store_dwordx2 v87, v[88:89], s[26:27] offset:256\n\t" \
    "s_branch .Lend_%=\n\t" \
    ".Lwor_%=:\n\t" \
    "s_cmp_eq_u32 s21, 5\n\t" \
    "s_cbranch_scc1 .Lidle_%=\n\t" \
    "ds_read_b64_tr_b16 v[8:9], v84\n\t" \
    "ds_read_b64_tr_b16 v[10:11], v84 offset:256\n\t" \
    "ds_read_b64_tr_b16 v[12:13], v84 offset:32\n\t" \
    "ds_read_b64_tr_b16 v[14:15], v84 offset:288\n\t" \
    "ds_read_b64_tr_b16 v[16:17], v84 offset:64\n\t" \
    "ds_read_b64_tr_b16 v[18:19], v84 offset:320\n\t" \
    "ds_read_b64_tr_b16 v[20:21], v84 offset:96\n\t" \
    "ds_read_b64_tr_b16 v[22:23], v84 offset:352\n\t" \
    "s_waitcnt vmcnt(4)\n\t" \
    "s_barrier\n\t" \
    "s_waitcnt vmcnt(2)\n\t" \
    "s_barrier\n\t" \
    "s_waitcnt vmcnt(0)\n\t" \
    "s_barrier\n\t" \
    "s_waitcnt lgkmcnt(0)\n\t" \
    "global_store_dwordx4 v86, v[8:11], s[26:27] offset:-4096\n\t" \
    "global_store_dwordx4 v86, v[12:15], s[26:27] offset:-2048\n\t" \
    "global_store_dwordx4 v86, v[16:19], s[26:27] offset:0\n\t" \
    "global_store_dwordx4 v86, v[20:23], s[26:27] offset:2048\n\t" \
    "s_branch .Lend_%=\n\t" \
    ".Lidle_%=:\n\t" \
    "s_waitcnt vmcnt(4)\n\t" \
    "s_barrier\n\t" \
    "s_waitcnt vmcnt(2)\n\t" \
    "s_barrier\n\t" \
    "s_waitcnt vmcnt(0)\n\t" \
    "s_barrier\n\t" \
    "s_cmp_lg_u32 s21, 7\n\t" \
    "s_cbranch_scc1 .Lend_%=\n\t" \
    "s_lshl_b32 s24, s20, 7\n\t" \
    "s_add_u32 s24, s24, 19820544\n\t" \
    "s_add_u32 s26, s18, s24\n\t" \
    "s_addc_u32 s27, s19, 0\n\t" \
    "v_lshlrev_b32 v7, 15, v1\n\t" \
    "v_mov_b32 v8, 0\n\t" \
    "s_mov_b64 exec, 7\n\t" \
    "global_store_dword v7, v8, s[26:27]\n\t" \
    "s_mul_i32 s24, s20, 896\n\t" \
    "s_add_u32 s24, s24, 98304\n\t" \
    "s_add_u32 s26, s26, s24\n\t" \
    "s_addc_u32 s27, s27, 0\n\t" \
    "s_mov_b64 exec, 0xff\n\t" \
    "v_lshlrev_b32 v7, 7, v1\n\t" \
    "global_store_dword v7, v8, s[26:27]\n\t" \
    ".Lend_%=:\n\t" \
    "s_mov_b64 exec, -1\n\t" \
    ""

#define K23_A8(n) "a" #n "0", "a" #n "1", "a" #n "2", "a" #n "3", "a" #n "4", "a" #n "5", "a" #n "6", "a" #n "7", "a" #n "8", "a" #n "9"
#define K23_S8(n) "s" #n "0", "s" #n "1", "s" #n "2", "s" #n "3", "s" #n "4", "s" #n "5", "s" #n "6", "s" #n "7", "s" #n "8", "s" #n "9"
#define K23_V8(n) "v" #n "0", "v" #n "1", "v" #n "2", "v" #n "3", "v" #n "4", "v" #n "5", "v" #n "6", "v" #n "7", "v" #n "8", "v" #n "9"
#pragma clang diagnostic ignored "-Winline-asm"
#define CPF
#define CPF2
#define CPFSEL "0x20003"
#define CPFSEL2 "0x20003"
#define CPF2A "0"
__global__ __launch_bounds__(512, 2) void k1_qkv(const u16* __restrict__ x, const u16* __restrict__ wq, const u16* __restrict__ wk,
                                                 const u16* __restrict__ wv, const u16* __restrict__ wo, char* __restrict__ ws) {
  PROF_BEGIN
  u32 tid = threadIdx.x;
  u32 junk;
  asm volatile("s_getpc_b64 s[4:5]\n"
               ".Lk1_here:\n\t"
               "s_bfe_u32 s6, %2, " CPFSEL "\n\tv_lshlrev_b32 %0, 7, %1\n\ts_mov_b32 s7, .Lk1_last-.Lk1_here\n\t"
               "s_cmp_eq_u32 s6, 0\n\ts_cbranch_scc1 .Lk1_cpf\n\t"
               "s_branch .Lk1_nocpf\n"
               ".Lk1_cpf:\n\t"
               "v_cmp_gt_u32 vcc, s7, %0\n\ts_and_saveexec_b64 s[6:7], vcc\n\ts_cbranch_execz .Lk1_nocpf0\n\t"
               "global_load_dword %0, %0, s[4:5]\n"
               ".Lk1_nocpf0:\n\ts_mov_b64 exec, s[6:7]\n"
               ".Lk1_nocpf:"
               : "=&{v89}"(junk) : "v"(tid), "s"(blockIdx.x) : "memory", "scc", "vcc", "s4", "s5", "s6", "s7");
  asm volatile(K1_ASM
               ".Lk1_last:"
               : "+{v0}"(tid)
               , "+{v89}"(junk)
               : "{s[8:9]}"(x), "{s[10:11]}"(wq), "{s[12:13]}"(wk), "{s[14:15]}"(wv), "{s[16:17]}"(wo), "{s[18:19]}"(ws), "{s20}"(blockIdx.x)
               : "memory", "scc", "vcc", "s21", "s22", "s23", "s24", "s25", "s26", "s27", "s28", "s29", "s30", "s31", "s34", "s35", "s36", "s37",
                 "v1",
                 "v2", "v3", "v4", "v5", "v6", "v7", "v8", "v9", K23_V8(1), K23_V8(2), K23_V8(3), K23_V8(4), K23_V8(5), K23_V8(6), K23_V8(7), K23_V8(8));
  PROF_END(0)
}

constexpr int K2_V = 65536, K2_STG = 139264, K2_OBUF = K2_STG + 16384, K2_STAT = 49152, K2_LDS = K2_STG + 24576;
constexpr float SAFE_LOG2 = 60.f;

DI void st_wt_s(const char* base, u32 off, u32x4 v) {
  asm volatile("global_store_dwordx4 %0, %1, %2 sc1\n\ts_nop 1" ::"v"(off), "v"(v), "s"(base) : "memory");
}
DI void st_wt(char* p, u32x4 v) {
  asm volatile("global_store_dwordx4 %0, %1, off sc1\n\ts_nop 1" ::"v"(p), "v"(v) : "memory");
}
DI void attn_tiles(AS3 const char* ka, AS3 const char* va, const u32x4 qf, const float negm, f32x16& o) {
#pragma unroll
  for (int u = 0; u < 2; u++) {
    const u32x4 kf = lds128(ka + u * 1024);
    const u32x4 v0 = lds128(va + u * 1152), v1 = lds128(va + u * 1152 + 576);
    f32x16 c;
#pragma unroll
    for (int j = 0; j < 16; j++) c[j] = negm;
    f32x16 s = mfma32(kf, qf, c);
#pragma unroll
    for (int j = 0; j < 16; j++) s[j] = __builtin_amdgcn_exp2f(s[j]);
    const f32x8 p0 = __builtin_shufflevector(s, s, 0, 1, 2, 3, 4, 5, 6, 7);
    const f32x8 p1 = __builtin_shufflevector(s, s, 8, 9, 10, 11, 12, 13, 14, 15);
    o = __builtin_amdgcn_smfmac_f32_32x32x32_bf16(__builtin_bit_cast(bf16x8, v0), __builtin_bit_cast(bf16x16, p0), o, (int)0xDDDDDDDD, 0, 0);
    o = __builtin_amdgcn_smfmac_f32_32x32x32_bf16(__builtin_bit_cast(bf16x8, v1), __builtin_bit_cast(bf16x16, p1), o, (int)0xDDDDDDDD, 0, 0);
  }
}

#define K23_ASM1 \
    "global_load_dwordx2 v[22:23], v26, s[74:75]\n\t" \
    "global_load_dword v25, v27, s[76:77]\n\t" \
    "global_load_dwordx4 v[48:51], v67, s[78:79]\n\t" \
    "s_mul_i32 s46, s43, 1024\n\t" \
    "s_add_u32 m0, s46, 0\n\t" \
    "s_add_u32 s44, s40, m0\n\t" \
    "s_addc_u32 s45, s41, 0\n\t" \
    "global_load_lds_dwordx4 v67, s[44:45]\n\t" \
    "s_mul_i32 s46, s43, 1024\n\t" \
    "s_add_u32 m0, s46, 65536\n\t" \
    "s_add_u32 s44, s40, m0\n\t" \
    "s_addc_u32 s45, s41, 0\n\t" \
    "global_load_lds_dwordx4 v67, s[44:45]\n\t" \
    "s_mul_i32 s46, s43, 3072\n\t" \
    "s_add_u32 m0, s46, 8192\n\t" \
    "s_add_u32 s44, s40, m0\n\t" \
    "s_addc_u32 s45, s41, 0\n\t" \
    "global_load_lds_dwordx4 v67, s[44:45]\n\t" \
    "global_load_lds_dwordx4 v67, s[44:45] offset:1024\n\t" \
    "global_load_lds_dwordx4 v67, s[44:45] offset:2048\n\t" \
    "s_mul_i32 s46, s43, 3072\n\t" \
    "s_add_u32 m0, s46, 73728\n\t" \
    "s_add_u32 s44, s40, m0\n\t" \
    "s_addc_u32 s45, s41, 0\n\t" \
    "global_load_lds_dwordx4 v67, s[44:45]\n\t" \
    "global_load_lds_dwordx4 v67, s[44:45] offset:1024\n\t" \
    "global_load_lds_dwordx4 v67, s[44:45] offset:2048\n\t" \
    ""
#define K23_ASM2 \
    "s_waitcnt vmcnt(6)\n\t" \
    "s_barrier\n\t" \
    "ds_read_b128 v[52:55], v67\n\t" \
    "s_waitcnt lgkmcnt(0)\n\t" \
    "v_mfma_f32_32x32x16_bf16 v[16:31], v[52:55], v[48:51], 0\n\t" \
    "ds_read_b128 v[52:55], v67 offset:1024\n\t" \
    "ds_read_b128 v[56:59], v66\n\t" \
    "v_mov_b32 v0, 0\n\t" \
    "v_mov_b32 v1, 0\n\t" \
    "v_mov_b32 v2, 0\n\t" \
    "v_mov_b32 v3, 0\n\t" \
    "v_mov_b32 v4, 0\n\t" \
    "v_mov_b32 v5, 0\n\t" \
    "v_mov_b32 v6, 0\n\t" \
    "v_mov_b32 v7, 0\n\t" \
    "v_mov_b32 v8, 0\n\t" \
    "v_mov_b32 v9, 0\n\t" \
    "v_mov_b32 v10, 0\n\t" \
    "v_mov_b32 v11, 0\n\t" \
    "v_mov_b32 v12, 0\n\t" \
    "v_mov_b32 v13, 0\n\t" \
    "v_mov_b32 v14, 0\n\t" \
    "v_mov_b32 v15, 0\n\t" \
    "v_mov_b32 v64, 0xdddddddd\n\t" \
    "s_setprio 3\n\t" \
    "v_exp_f32 v16, v16\n\t" \
    "v_exp_f32 v17, v17\n\t" \
    "ds_read_b128 v[60:63], v66 offset:576\n\t" \
    "v_exp_f32 v18, v18\n\t" \
    "v_exp_f32 v19, v19\n\t" \
    "s_mul_i32 s46, s43, 4096\n\t" \
    "s_add_u32 m0, s46, 32768\n\t" \
    "s_add_u32 s44, s40, m0\n\t" \
    "s_addc_u32 s45, s41, 0\n\t" \
    "global_load_lds_dwordx4 v67, s[44:45]\n\t" \
    "global_load_lds_dwordx4 v67, s[44:45] offset:1024\n\t" \
    "v_exp_f32 v20, v20\n\t" \
    "v_exp_f32 v21, v21\n\t" \
    "s_waitcnt lgkmcnt(2)\n\t" \
    "v_mfma_f32_32x32x16_bf16 v[32:47], v[52:55], v[48:51], 0\n\t" \
    "ds_read_b128 v[52:55], v67 offset:2048\n\t" \
    "v_exp_f32 v22, v22\n\t" \
    "v_exp_f32 v23, v23\n\t" \
    "v_exp_f32 v24, v24\n\t" \
    "v_exp_f32 v25, v25\n\t" \
    "s_waitcnt lgkmcnt(2)\n\t" \
    "v_smfmac_f32_32x32x32_bf16 v[0:15], v[56:59], v[16:23], v64\n\t" \
    "ds_read_b128 v[56:59], v66 offset:1152\n\t" \
    "v_exp_f32 v26, v26\n\t" \
    "v_exp_f32 v27, v27\n\t" \
    "v_exp_f32 v28, v28\n\t" \
    "v_exp_f32 v29, v29\n\t" \
    "v_exp_f32 v30, v30\n\t" \
    "v_exp_f32 v31, v31\n\t" \
    "s_setprio 2\n\t" \
    "v_exp_f32 v32, v32\n\t" \
    "v_exp_f32 v33, v33\n\t" \
    "s_waitcnt lgkmcnt(2)\n\t" \
    "v_smfmac_f32_32x32x32_bf16 v[0:15], v[60:63], v[24:31], v64\n\t" \
    "ds_read_b128 v[60:63], v66 offset:1728\n\t" \
    "v_exp_f32 v34, v34\n\t" \
    "v_exp_f32 v35, v35\n\t" \
    "global_load_lds_dwordx4 v67, s[44:45] offset:2048\n\t" \
    "global_load_lds_dwordx4 v67, s[44:45] offset:3072\n\t" \
    "v_exp_f32 v36, v36\n\t" \
    "v_exp_f32 v37, v37\n\t" \
    "s_waitcnt lgkmcnt(2)\n\t" \
    "v_mfma_f32_32x32x16_bf16 v[16:31], v[52:55], v[48:51], 0\n\t" \
    "ds_read_b128 v[52:55], v67 offset:3072\n\t" \
    "v_exp_f32 v38, v38\n\t" \
    "v_exp_f32 v39, v39\n\t" \
    "v_exp_f32 v40, v40\n\t" \
    "v_exp_f32 v41, v41\n\t" \
    "s_waitcnt lgkmcnt(2)\n\t" \
    "v_smfmac_f32_32x32x32_bf16 v[0:15], v[56:59], v[32:39], v64\n\t" \
    "ds_read_b128 v[56:59], v66 offset:2304\n\t" \
    "v_exp_f32 v42, v42\n\t" \
    "v_exp_f32 v43, v43\n\t" \
    "v_exp_f32 v44, v44\n\t" \
    "v_exp_f32 v45, v45\n\t" \
    "v_exp_f32 v46, v46\n\t" \
    "v_exp_f32 v47, v47\n\t" \
    "s_setprio 1\n\t" \
    "v_exp_f32 v16, v16\n\t" \
    "v_exp_f32 v17, v17\n\t" \
    "s_waitcnt lgkmcnt(2)\n\t" \
    "v_smfmac_f32_32x32x32_bf16 v[0:15], v[60:63], v[40:47], v64\n\t" \
    "ds_read_b128 v[60:63], v66 offset:2880\n\t" \
    "v_exp_f32 v18, v18\n\t" \
    "v_exp_f32 v19, v19\n\t" \
    "s_mul_i32 s46, s43, 5120\n\t" \
    "s_add_u32 m0, s46, 98304\n\t" \
    "s_add_u32 s44, s40, m0\n\t" \
    "s_addc_u32 s45, s41, 0\n\t" \
    "global_load_lds_dwordx4 v67, s[44:45]\n\t" \
    "global_load_lds_dwordx4 v67, s[44:45] offset:1024\n\t" \
    "v_exp_f32 v20, v20\n\t" \
    "v_exp_f32 v21, v21\n\t" \
    "s_waitcnt lgkmcnt(2)\n\t" \
    "v_mfma_f32_32x32x16_bf16 v[32:47], v[52:55], v[48:51], 0\n\t" \
    "ds_read_b128 v[52:55], v67 offset:4096\n\t" \
    "v_exp_f32 v22, v22\n\t" \
    "v_exp_f32 v23, v23\n\t" \
    "v_exp_f32 v24, v24\n\t" \
    "v_exp_f32 v25, v25\n\t" \
    "s_waitcnt lgkmcnt(2)\n\t" \
    "v_smfmac_f32_32x32x32_bf16 v[0:15], v[56:59], v[16:23], v64\n\t" \
    "ds_read_b128 v[56:59], v66 offset:3456\n\t" \
    "v_exp_f32 v26, v26\n\t" \
    "v_exp_f32 v27, v27\n\t" \
    "v_exp_f32 v28, v28\n\t" \
    "v_exp_f32 v29, v29\n\t" \
    "v_exp_f32 v30, v30\n\t" \
    "v_exp_f32 v31, v31\n\t" \
    "s_setprio 0\n\t" \
    "v_exp_f32 v32, v32\n\t" \
    "v_exp_f32 v33, v33\n\t" \
    "s_waitcnt lgkmcnt(2)\n\t" \
    "v_smfmac_f32_32x32x32_bf16 v[0:15], v[60:63], v[24:31], v64\n\t" \
    "ds_read_b128 v[60:63], v66 offset:4032\n\t" \
    "v_exp_f32 v34, v34\n\t" \
    "v_exp_f32 v35, v35\n\t" \
    "global_load_lds_dwordx4 v67, s[44:45] offset:2048\n\t" \
    "global_load_lds_dwordx4 v67, s[44:45] offset:3072\n\t" \
    "v_exp_f32 v36, v36\n\t" \
    "v_exp_f32 v37, v37\n\t" \
    "s_waitcnt lgkmcnt(2)\n\t" \
    "v_mfma_f32_32x32x16_bf16 v[16:31], v[52:55], v[48:51], 0\n\t" \
    "ds_read_b128 v[52:55], v67 offset:5120\n\t" \
    "v_exp_f32 v38, v38\n\t" \
    "v_exp_f32 v39, v39\n\t" \
    "v_exp_f32 v40, v40\n\t" \
    "v_exp_f32 v41, v41\n\t" \
    "s_waitcnt lgkmcnt(2)\n\t" \
    "v_smfmac_f32_32x32x32_bf16 v[0:15], v[56:59], v[32:39], v64\n\t" \
    "ds_read_b128 v[56:59], v66 offset:4608\n\t" \
    "v_exp_f32 v42, v42\n\t" \
    "v_exp_f32 v43, v43\n\t" \
    "v_exp_f32 v44, v44\n\t" \
    "v_exp_f32 v45, v45\n\t" \
    "v_exp_f32 v46, v46\n\t" \
    "v_exp_f32 v47, v47\n\t" \
    "s_setprio 3\n\t" \
    "v_exp_f32 v16, v16\n\t" \
    "v_exp_f32 v17, v17\n\t" \
    "s_waitcnt lgkmcnt(2)\n\t" \
    "v_smfmac_f32_32x32x32_bf16 v[0:15], v[60:63], v[40:47], v64\n\t" \
    "ds_read_b128 v[60:63], v66 offset:5184\n\t" \
    "v_exp_f32 v18, v18\n\t" \
    "v_exp_f32 v19, v19\n\t" \
    "s_mul_i32 s46, s43, 5120\n\t" \
    "s_add_u32 m0, s46, 102400\n\t" \
    "s_add_u32 s44, s40, m0\n\t" \
    "s_addc_u32 s45, s41, 0\n\t" \
    "global_load_lds_dwordx4 v67, s[44:45]\n\t" \
    "v_exp_f32 v20, v20\n\t" \
    "v_exp_f32 v21, v21\n\t" \
    "s_waitcnt lgkmcnt(2)\n\t" \
    "v_mfma_f32_32x32x16_bf16 v[32:47], v[52:55], v[48:51], 0\n\t" \
    "ds_read_b128 v[52:55], v67 offset:6144\n\t" \
    "v_exp_f32 v22, v22\n\t" \
    "v_exp_f32 v23, v23\n\t" \
    "v_exp_f32 v24, v24\n\t" \
    "v_exp_f32 v25, v25\n\t" \
    "s_waitcnt lgkmcnt(2)\n\t" \
    "v_smfmac_f32_32x32x32_bf16 v[0:15], v[56:59], v[16:23], v64\n\t" \
    "ds_read_b128 v[56:59], v66 offset:5760\n\t" \
    "v_exp_f32 v26, v26\n\t" \
    "v_exp_f32 v27, v27\n\t" \
    "v_exp_f32 v28, v28\n\t" \
    "v_exp_f32 v29, v29\n\t" \
    "v_exp_f32 v30, v30\n\t" \
    "v_exp_f32 v31, v31\n\t" \
    "s_setprio 2\n\t" \
    "v_exp_f32 v32, v32\n\t" \
    "v_exp_f32 v33, v33\n\t" \
    "s_waitcnt lgkmcnt(2)\n\t" \
    "v_smfmac_f32_32x32x32_bf16 v[0:15], v[60:63], v[24:31], v64\n\t" \
    "ds_read_b128 v[60:63], v66 offset:6336\n\t" \
    "v_exp_f32 v34, v34\n\t" \
    "v_exp_f32 v35, v35\n\t" \
    "v_exp_f32 v36, v36\n\t" \
    "v_exp_f32 v37, v37\n\t" \
    "s_waitcnt lgkmcnt(2)\n\t" \
    "v_mfma_f32_32x32x16_bf16 v[16:31], v[52:55], v[48:51], 0\n\t" \
    "ds_read_b128 v[52:55], v67 offset:7168\n\t" \
    "v_exp_f32 v38, v38\n\t" \
    "v_exp_f32 v39, v39\n\t" \
    "v_exp_f32 v40, v40\n\t" \
    "v_exp_f32 v41, v41\n\t" \
    "s_waitcnt lgkmcnt(2)\n\t" \
    "v_smfmac_f32_32x32x32_bf16 v[0:15], v[56:59], v[32:39], v64\n\t" \
    "ds_read_b128 v[56:59], v66 offset:6912\n\t" \
    "v_exp_f32 v42, v42\n\t" \
    "v_exp_f32 v43, v43\n\t" \
    "v_exp_f32 v44, v44\n\t" \
    "v_exp_f32 v45, v45\n\t" \
    "v_exp_f32 v46, v46\n\t" \
    "v_exp_f32 v47, v47\n\t" \
    "s_waitcnt vmcnt(9)\n\t" \
    "s_barrier\n\t" \
    "s_setprio 1\n\t" \
    "v_exp_f32 v16, v16\n\t" \
    "v_exp_f32 v17, v17\n\t" \
    "s_waitcnt lgkmcnt(2)\n\t" \
    "v_smfmac_f32_32x32x32_bf16 v[0:15], v[60:63], v[40:47], v64\n\t" \
    "ds_read_b128 v[60:63], v66 offset:7488\n\t" \
    "v_exp_f32 v18, v18\n\t" \
    "v_exp_f32 v19, v19\n\t" \
    "v_exp_f32 v20, v20\n\t" \
    "v_exp_f32 v21, v21\n\t" \
    "s_waitcnt lgkmcnt(2)\n\t" \
    "v_mfma_f32_32x32x16_bf16 v[32:47], v[52:55], v[48:51], 0\n\t" \
    "ds_read_b128 v[52:55], v67 offset:8192\n\t" \
    "v_exp_f32 v22, v22\n\t" \
    "v_exp_f32 v23, v23\n\t" \
    "v_exp_f32 v24, v24\n\t" \
    "v_exp_f32 v25, v25\n\t" \
    "s_waitcnt lgkmcnt(2)\n\t" \
    "v_smfmac_f32_32x32x32_bf16 v[0:15], v[56:59], v[16:23], v64\n\t" \
    "ds_read_b128 v[56:59], v66 offset:8064\n\t" \
    "v_exp_f32 v26, v26\n\t" \
    "v_exp_f32 v27, v27\n\t" \
    "v_exp_f32 v28, v28\n\t" \
    "v_exp_f32 v29, v29\n\t" \
    "v_exp_f32 v30, v30\n\t" \
    "v_exp_f32 v31, v31\n\t" \
    "s_setprio 0\n\t" \
    "v_exp_f32 v32, v32\n\t" \
    "v_exp_f32 v33, v33\n\t" \
    "s_waitcnt lgkmcnt(2)\n\t" \
    "v_smfmac_f32_32x32x32_bf16 v[0:15], v[60:63], v[24:31], v64\n\t" \
    "ds_read_b128 v[60:63], v66 offset:8640\n\t" \
    "v_exp_f32 v34, v34\n\t" \
    "v_exp_f32 v35, v35\n\t" \
    "s_lshl_b32 s46, s43, 10\n\t" \
    "s_add_u32 m0, s46, 139264\n\t" \
    "s_nop 0\n\t" \
    "global_load_lds_dwordx4 v65, s[48:49]\n\t" \
    "v_exp_f32 v36, v36\n\t" \
    "v_exp_f32 v37, v37\n\t" \
    "s_waitcnt lgkmcnt(2)\n\t" \
    "v_mfma_f32_32x32x16_bf16 v[16:31], v[52:55], v[48:51], 0\n\t" \
    "ds_read_b128 v[52:55], v67 offset:9216\n\t" \
    "v_exp_f32 v38, v38\n\t" \
    "v_exp_f32 v39, v39\n\t" \
    "v_exp_f32 v40, v40\n\t" \
    "v_exp_f32 v41, v41\n\t" \
    "s_waitcnt lgkmcnt(2)\n\t" \
    "v_smfmac_f32_32x32x32_bf16 v[0:15], v[56:59], v[32:39], v64\n\t" \
    "ds_read_b128 v[56:59], v66 offset:9216\n\t" \
    "v_exp_f32 v42, v42\n\t" \
    "v_exp_f32 v43, v43\n\t" \
    "v_exp_f32 v44, v44\n\t" \
    "v_exp_f32 v45, v45\n\t" \
    "v_exp_f32 v46, v46\n\t" \
    "v_exp_f32 v47, v47\n\t" \
    "s_setprio 3\n\t" \
    "v_exp_f32 v16, v16\n\t" \
    "v_exp_f32 v17, v17\n\t" \
    "s_waitcnt lgkmcnt(2)\n\t" \
    "v_smfmac_f32_32x32x32_bf16 v[0:15], v[60:63], v[40:47], v64\n\t" \
    "ds_read_b128 v[60:63], v66 offset:9792\n\t" \
    "v_exp_f32 v18, v18\n\t" \
    "v_exp_f32 v19, v19\n\t" \
    "s_lshl_b32 s46, s43, 10\n\t" \
    "s_add_u32 m0, s46, 147456\n\t" \
    "s_nop 0\n\t" \
    "global_load_lds_dwordx4 v65, s[50:51]\n\t" \
    "v_exp_f32 v20, v20\n\t" \
    "v_exp_f32 v21, v21\n\t" \
    "s_waitcnt lgkmcnt(2)\n\t" \
    "v_mfma_f32_32x32x16_bf16 v[32:47], v[52:55], v[48:51], 0\n\t" \
    "ds_read_b128 v[52:55], v67 offset:10240\n\t" \
    "v_exp_f32 v22, v22\n\t" \
    "v_exp_f32 v23, v23\n\t" \
    "v_exp_f32 v24, v24\n\t" \
    "v_exp_f32 v25, v25\n\t" \
    "s_waitcnt lgkmcnt(2)\n\t" \
    "v_smfmac_f32_32x32x32_bf16 v[0:15], v[56:59], v[16:23], v64\n\t" \
    "ds_read_b128 v[56:59], v66 offset:10368\n\t" \
    "v_exp_f32 v26, v26\n\t" \
    "v_exp_f32 v27, v27\n\t" \
    "v_exp_f32 v28, v28\n\t" \
    "v_exp_f32 v29, v29\n\t" \
    "v_exp_f32 v30, v30\n\t" \
    "v_exp_f32 v31, v31\n\t" \
    "s_setprio 2\n\t" \
    "v_exp_f32 v32, v32\n\t" \
    "v_exp_f32 v33, v33\n\t" \
    "s_waitcnt lgkmcnt(2)\n\t" \
    "v_smfmac_f32_32x32x32_bf16 v[0:15], v[60:63], v[24:31], v64\n\t" \
    "ds_read_b128 v[60:63], v66 offset:10944\n\t" \
    "v_exp_f32 v34, v34\n\t" \
    "v_exp_f32 v35, v35\n\t" \
    "s_lshl_b32 s46, s43, 10\n\t" \
    "s_add_u32 m0, s46, 155648\n\t" \
    "s_nop 0\n\t" \
    "global_load_lds_dwordx4 v68, s[52:53]\n\t" \
    "v_exp_f32 v36, v36\n\t" \
    "v_exp_f32 v37, v37\n\t" \
    "s_waitcnt lgkmcnt(2)\n\t" \
    "v_mfma_f32_32x32x16_bf16 v[16:31], v[52:55], v[48:51], 0\n\t" \
    "ds_read_b128 v[52:55], v67 offset:11264\n\t" \
    "v_exp_f32 v38, v38\n\t" \
    "v_exp_f32 v39, v39\n\t" \
    "v_exp_f32 v40, v40\n\t" \
    "v_exp_f32 v41, v41\n\t" \
    "s_waitcnt lgkmcnt(2)\n\t" \
    "v_smfmac_f32_32x32x32_bf16 v[0:15], v[56:59], v[32:39], v64\n\t" \
    "ds_read_b128 v[56:59], v66 offset:11520\n\t" \
    "v_exp_f32 v42, v42\n\t" \
    "v_exp_f32 v43, v43\n\t" \
    "v_exp_f32 v44, v44\n\t" \
    "v_exp_f32 v45, v45\n\t" \
    "v_exp_f32 v46, v46\n\t" \
    "v_exp_f32 v47, v47\n\t" \
    "s_setprio 1\n\t" \
    "v_exp_f32 v16, v16\n\t" \
    "v_exp_f32 v17, v17\n\t" \
    "s_waitcnt lgkmcnt(2)\n\t" \
    "v_smfmac_f32_32x32x32_bf16 v[0:15], v[60:63], v[40:47], v64\n\t" \
    "ds_read_b128 v[60:63], v66 offset:12096\n\t" \
    "v_exp_f32 v18, v18\n\t" \
    "v_exp_f32 v19, v19\n\t" \
    "v_exp_f32 v20, v20\n\t" \
    "v_exp_f32 v21, v21\n\t" \
    "s_waitcnt lgkmcnt(2)\n\t" \
    "v_mfma_f32_32x32x16_bf16 v[32:47], v[52:55], v[48:51], 0\n\t" \
    "ds_read_b128 v[52:55], v67 offset:12288\n\t" \
    "v_exp_f32 v22, v22\n\t" \
    "v_exp_f32 v23, v23\n\t" \
    "v_exp_f32 v24, v24\n\t" \
    "v_exp_f32 v25, v25\n\t" \
    "s_waitcnt lgkmcnt(2)\n\t" \
    "v_smfmac_f32_32x32x32_bf16 v[0:15], v[56:59], v[16:23], v64\n\t" \
    "ds_read_b128 v[56:59], v66 offset:12672\n\t" \
    "v_exp_f32 v26, v26\n\t" \
    "v_exp_f32 v27, v27\n\t" \
    "v_exp_f32 v28, v28\n\t" \
    "v_exp_f32 v29, v29\n\t" \
    "v_exp_f32 v30, v30\n\t" \
    "v_exp_f32 v31, v31\n\t" \
    "s_setprio 0\n\t" \
    "v_exp_f32 v32, v32\n\t" \
    "v_exp_f32 v33, v33\n\t" \
    "s_waitcnt lgkmcnt(2)\n\t" \
    "v_smfmac_f32_32x32x32_bf16 v[0:15], v[60:63], v[24:31], v64\n\t" \
    "ds_read_b128 v[60:63], v66 offset:13248\n\t" \
    "v_exp_f32 v34, v34\n\t" \
    "v_exp_f32 v35, v35\n\t" \
    "v_exp_f32 v36, v36\n\t" \
    "v_exp_f32 v37, v37\n\t" \
    "s_waitcnt lgkmcnt(2)\n\t" \
    "v_mfma_f32_32x32x16_bf16 v[16:31], v[52:55], v[48:51], 0\n\t" \
    "ds_read_b128 v[52:55], v67 offset:13312\n\t" \
    "v_exp_f32 v38, v38\n\t" \
    "v_exp_f32 v39, v39\n\t" \
    "v_exp_f32 v40, v40\n\t" \
    "v_exp_f32 v41, v41\n\t" \
    "s_waitcnt lgkmcnt(2)\n\t" \
    "v_smfmac_f32_32x32x32_bf16 v[0:15], v[56:59], v[32:39], v64\n\t" \
    "ds_read_b128 v[56:59], v66 offset:13824\n\t" \
    "v_exp_f32 v42, v42\n\t" \
    "v_exp_f32 v43, v43\n\t" \
    "v_exp_f32 v44, v44\n\t" \
    "v_exp_f32 v45, v45\n\t" \
    "v_exp_f32 v46, v46\n\t" \
    "v_exp_f32 v47, v47\n\t" \
    "s_setprio 3\n\t" \
    "v_exp_f32 v16, v16\n\t" \
    "v_exp_f32 v17, v17\n\t" \
    "s_waitcnt lgkmcnt(2)\n\t" \
    "v_smfmac_f32_32x32x32_bf16 v[0:15], v[60:63], v[40:47], v64\n\t" \
    "ds_read_b128 v[60:63], v66 offset:14400\n\t" \
    "v_exp_f32 v18, v18\n\t" \
    "v_exp_f32 v19, v19\n\t" \
    "v_exp_f32 v20, v20\n\t" \
    "v_exp_f32 v21, v21\n\t" \
    "s_waitcnt lgkmcnt(2)\n\t" \
    "v_mfma_f32_32x32x16_bf16 v[32:47], v[52:55], v[48:51], 0\n\t" \
    "ds_read_b128 v[52:55], v67 offset:14336\n\t" \
    "v_exp_f32 v22, v22\n\t" \
    "v_exp_f32 v23, v23\n\t" \
    "v_exp_f32 v24, v24\n\t" \
    "v_exp_f32 v25, v25\n\t" \
    "s_waitcnt lgkmcnt(2)\n\t" \
    "v_smfmac_f32_32x32x32_bf16 v[0:15], v[56:59], v[16:23], v64\n\t" \
    "ds_read_b128 v[56:59], v66 offset:14976\n\t" \
    "v_exp_f32 v26, v26\n\t" \
    "v_exp_f32 v27, v27\n\t" \
    "v_exp_f32 v28, v28\n\t" \
    "v_exp_f32 v29, v29\n\t" \
    "v_exp_f32 v30, v30\n\t" \
    "v_exp_f32 v31, v31\n\t" \
    "s_setprio 2\n\t" \
    "v_exp_f32 v32, v32\n\t" \
    "v_exp_f32 v33, v33\n\t" \
    "s_waitcnt lgkmcnt(2)\n\t" \
    "v_smfmac_f32_32x32x32_bf16 v[0:15], v[60:63], v[24:31], v64\n\t" \
    "ds_read_b128 v[60:63], v66 offset:15552\n\t" \
    "v_exp_f32 v34, v34\n\t" \
    "v_exp_f32 v35, v35\n\t" \
    "v_exp_f32 v36, v36\n\t" \
    "v_exp_f32 v37, v37\n\t" \
    "s_waitcnt lgkmcnt(2)\n\t" \
    "v_mfma_f32_32x32x16_bf16 v[16:31], v[52:55], v[48:51], 0\n\t" \
    "ds_read_b128 v[52:55], v67 offset:15360\n\t" \
    "v_exp_f32 v38, v38\n\t" \
    "v_exp_f32 v39, v39\n\t" \
    "v_exp_f32 v40, v40\n\t" \
    "v_exp_f32 v41, v41\n\t" \
    "s_waitcnt lgkmcnt(2)\n\t" \
    "v_smfmac_f32_32x32x32_bf16 v[0:15], v[56:59], v[32:39], v64\n\t" \
    "ds_read_b128 v[56:59], v66 offset:16128\n\t" \
    "v_exp_f32 v42, v42\n\t" \
    "v_exp_f32 v43, v43\n\t" \
    "v_exp_f32 v44, v44\n\t" \
    "v_exp_f32 v45, v45\n\t" \
    "v_exp_f32 v46, v46\n\t" \
    "v_exp_f32 v47, v47\n\t" \
    "s_setprio 1\n\t" \
    "v_exp_f32 v16, v16\n\t" \
    "v_exp_f32 v17, v17\n\t" \
    "s_waitcnt lgkmcnt(2)\n\t" \
    "v_smfmac_f32_32x32x32_bf16 v[0:15], v[60:63], v[40:47], v64\n\t" \
    "ds_read_b128 v[60:63], v66 offset:16704\n\t" \
    "v_exp_f32 v18, v18\n\t" \
    "v_exp_f32 v19, v19\n\t" \
    "v_exp_f32 v20, v20\n\t" \
    "v_exp_f32 v21, v21\n\t" \
    "s_waitcnt lgkmcnt(2)\n\t" \
    "v_mfma_f32_32x32x16_bf16 v[32:47], v[52:55], v[48:51], 0\n\t" \
    "ds_read_b128 v[52:55], v67 offset:16384\n\t" \
    "v_exp_f32 v22, v22\n\t" \
    "v_exp_f32 v23, v23\n\t" \
    "v_exp_f32 v24, v24\n\t" \
    "v_exp_f32 v25, v25\n\t" \
    "s_waitcnt lgkmcnt(2)\n\t" \
    "v_smfmac_f32_32x32x32_bf16 v[0:15], v[56:59], v[16:23], v64\n\t" \
    "ds_read_b128 v[56:59], v66 offset:17280\n\t" \
    "v_exp_f32 v26, v26\n\t" \
    "v_exp_f32 v27, v27\n\t" \
    "v_exp_f32 v28, v28\n\t" \
    "v_exp_f32 v29, v29\n\t" \
    "v_exp_f32 v30, v30\n\t" \
    "v_exp_f32 v31, v31\n\t" \
    "s_setprio 0\n\t" \
    "v_exp_f32 v32, v32\n\t" \
    "v_exp_f32 v33, v33\n\t" \
    "s_waitcnt lgkmcnt(2)\n\t" \
    "v_smfmac_f32_32x32x32_bf16 v[0:15], v[60:63], v[24:31], v64\n\t" \
    "ds_read_b128 v[60:63], v66 offset:17856\n\t" \
    "v_exp_f32 v34, v34\n\t" \
    "v_exp_f32 v35, v35\n\t" \
    "v_exp_f32 v36, v36\n\t" \
    "v_exp_f32 v37, v37\n\t" \
    "s_waitcnt lgkmcnt(2)\n\t" \
    "v_mfma_f32_32x32x16_bf16 v[16:31], v[52:55], v[48:51], 0\n\t" \
    "ds_read_b128 v[52:55], v67 offset:17408\n\t" \
    "v_exp_f32 v38, v38\n\t" \
    "v_exp_f32 v39, v39\n\t" \
    "v_exp_f32 v40, v40\n\t" \
    "v_exp_f32 v41, v41\n\t" \
    "s_waitcnt lgkmcnt(2)\n\t" \
    "v_smfmac_f32_32x32x32_bf16 v[0:15], v[56:59], v[32:39], v64\n\t" \
    "ds_read_b128 v[56:59], v66 offset:18432\n\t" \
    "v_exp_f32 v42, v42\n\t" \
    "v_exp_f32 v43, v43\n\t" \
    "v_exp_f32 v44, v44\n\t" \
    "v_exp_f32 v45, v45\n\t" \
    "v_exp_f32 v46, v46\n\t" \
    "v_exp_f32 v47, v47\n\t" \
    "s_setprio 3\n\t" \
    "v_exp_f32 v16, v16\n\t" \
    "v_exp_f32 v17, v17\n\t" \
    "s_waitcnt lgkmcnt(2)\n\t" \
    "v_smfmac_f32_32x32x32_bf16 v[0:15], v[60:63], v[40:47], v64\n\t" \
    "ds_read_b128 v[60:63], v66 offset:19008\n\t" \
    "v_exp_f32 v18, v18\n\t" \
    "v_exp_f32 v19, v19\n\t" \
    "v_exp_f32 v20, v20\n\t" \
    "v_exp_f32 v21, v21\n\t" \
    "s_waitcnt lgkmcnt(2)\n\t" \
    "v_mfma_f32_32x32x16_bf16 v[32:47], v[52:55], v[48:51], 0\n\t" \
    "ds_read_b128 v[52:55], v67 offset:18432\n\t" \
    "v_exp_f32 v22, v22\n\t" \
    "v_exp_f32 v23, v23\n\t" \
    "v_exp_f32 v24, v24\n\t" \
    "v_exp_f32 v25, v25\n\t" \
    "s_waitcnt lgkmcnt(2)\n\t" \
    "v_smfmac_f32_32x32x32_bf16 v[0:15], v[56:59], v[16:23], v64\n\t" \
    "ds_read_b128 v[56:59], v66 offset:19584\n\t" \
    "v_exp_f32 v26, v26\n\t" \
    "v_exp_f32 v27, v27\n\t" \
    "v_exp_f32 v28, v28\n\t" \
    "v_exp_f32 v29, v29\n\t" \
    "v_exp_f32 v30, v30\n\t" \
    "v_exp_f32 v31, v31\n\t" \
    "s_setprio 2\n\t" \
    "v_exp_f32 v32, v32\n\t" \
    "v_exp_f32 v33, v33\n\t" \
    "s_waitcnt lgkmcnt(2)\n\t" \
    "v_smfmac_f32_32x32x32_bf16 v[0:15], v[60:63], v[24:31], v64\n\t" \
    "ds_read_b128 v[60:63], v66 offset:20160\n\t" \
    "v_exp_f32 v34, v34\n\t" \
    "v_exp_f32 v35, v35\n\t" \
    "v_exp_f32 v36, v36\n\t" \
    "v_exp_f32 v37, v37\n\t" \
    "s_waitcnt lgkmcnt(2)\n\t" \
    "v_mfma_f32_32x32x16_bf16 v[16:31], v[52:55], v[48:51], 0\n\t" \
    "ds_read_b128 v[52:55], v67 offset:19456\n\t" \
    "v_exp_f32 v38, v38\n\t" \
    "v_exp_f32 v39, v39\n\t" \
    "v_exp_f32 v40, v40\n\t" \
    "v_exp_f32 v41, v41\n\t" \
    "s_waitcnt lgkmcnt(2)\n\t" \
    "v_smfmac_f32_32x32x32_bf16 v[0:15], v[56:59], v[32:39], v64\n\t" \
    "ds_read_b128 v[56:59], v66 offset:20736\n\t" \
    "v_exp_f32 v42, v42\n\t" \
    "v_exp_f32 v43, v43\n\t" \
    "v_exp_f32 v44, v44\n\t" \
    "v_exp_f32 v45, v45\n\t" \
    "v_exp_f32 v46, v46\n\t" \
    "v_exp_f32 v47, v47\n\t" \
    "s_setprio 1\n\t" \
    "v_exp_f32 v16, v16\n\t" \
    "v_exp_f32 v17, v17\n\t" \
    "s_waitcnt lgkmcnt(2)\n\t" \
    "v_smfmac_f32_32x32x32_bf16 v[0:15], v[60:63], v[40:47], v64\n\t" \
    "ds_read_b128 v[60:63], v66 offset:21312\n\t" \
    "v_exp_f32 v18, v18\n\t" \
    "v_exp_f32 v19, v19\n\t" \
    "global_load_dwordx4 a[0:3], v67, s[54:55] offset:-4096\n\t" \
    "global_load_dwordx4 a[4:7], v67, s[54:55] offset:-3072\n\t" \
    "v_exp_f32 v20, v20\n\t" \
    "v_exp_f32 v21, v21\n\t" \
    "s_waitcnt lgkmcnt(2)\n\t" \
    "v_mfma_f32_32x32x16_bf16 v[32:47], v[52:55], v[48:51], 0\n\t" \
    "ds_read_b128 v[52:55], v67 offset:20480\n\t" \
    "v_exp_f32 v22, v22\n\t" \
    "v_exp_f32 v23, v23\n\t" \
    "v_exp_f32 v24, v24\n\t" \
    "v_exp_f32 v25, v25\n\t" \
    "s_waitcnt lgkmcnt(2)\n\t" \
    "v_smfmac_f32_32x32x32_bf16 v[0:15], v[56:59], v[16:23], v64\n\t" \
    "ds_read_b128 v[56:59], v66 offset:21888\n\t" \
    "v_exp_f32 v26, v26\n\t" \
    "v_exp_f32 v27, v27\n\t" \
    "v_exp_f32 v28, v28\n\t" \
    "v_exp_f32 v29, v29\n\t" \
    "v_exp_f32 v30, v30\n\t" \
    "v_exp_f32 v31, v31\n\t" \
    "s_setprio 0\n\t" \
    "v_exp_f32 v32, v32\n\t" \
    "v_exp_f32 v33, v33\n\t" \
    "s_waitcnt lgkmcnt(2)\n\t" \
    "v_smfmac_f32_32x32x32_bf16 v[0:15], v[60:63], v[24:31], v64\n\t" \
    "ds_read_b128 v[60:63], v66 offset:22464\n\t" \
    "v_exp_f32 v34, v34\n\t" \
    "v_exp_f32 v35, v35\n\t" \
    "global_load_dwordx4 a[8:11], v67, s[54:55] offset:-2048\n\t" \
    "global_load_dwordx4 a[12:15], v67, s[54:55] offset:-1024\n\t" \
    "v_exp_f32 v36, v36\n\t" \
    "v_exp_f32 v37, v37\n\t" \
    "s_waitcnt lgkmcnt(2)\n\t" \
    "v_mfma_f32_32x32x16_bf16 v[16:31], v[52:55], v[48:51], 0\n\t" \
    "ds_read_b128 v[52:55], v67 offset:21504\n\t" \
    "v_exp_f32 v38, v38\n\t" \
    "v_exp_f32 v39, v39\n\t" \
    "v_exp_f32 v40, v40\n\t" \
    "v_exp_f32 v41, v41\n\t" \
    "s_waitcnt lgkmcnt(2)\n\t" \
    "v_smfmac_f32_32x32x32_bf16 v[0:15], v[56:59], v[32:39], v64\n\t" \
    "ds_read_b128 v[56:59], v66 offset:23040\n\t" \
    "v_exp_f32 v42, v42\n\t" \
    "v_exp_f32 v43, v43\n\t" \
    "v_exp_f32 v44, v44\n\t" \
    "v_exp_f32 v45, v45\n\t" \
    "v_exp_f32 v46, v46\n\t" \
    "v_exp_f32 v47, v47\n\t" \
    "s_setprio 3\n\t" \
    "v_exp_f32 v16, v16\n\t" \
    "v_exp_f32 v17, v17\n\t" \
    "s_waitcnt lgkmcnt(2)\n\t" \
    "v_smfmac_f32_32x32x32_bf16 v[0:15], v[60:63], v[40:47], v64\n\t" \
    "ds_read_b128 v[60:63], v66 offset:23616\n\t" \
    "v_exp_f32 v18, v18\n\t" \
    "v_exp_f32 v19, v19\n\t" \
    "global_load_dwordx4 a[16:19], v67, s[54:55] offset:0\n\t" \
    "global_load_dwordx4 a[20:23], v67, s[54:55] offset:1024\n\t" \
    "v_exp_f32 v20, v20\n\t" \
    "v_exp_f32 v21, v21\n\t" \
    "s_waitcnt lgkmcnt(2)\n\t" \
    "v_mfma_f32_32x32x16_bf16 v[32:47], v[52:55], v[48:51], 0\n\t" \
    "ds_read_b128 v[52:55], v67 offset:22528\n\t" \
    "v_exp_f32 v22, v22\n\t" \
    "v_exp_f32 v23, v23\n\t" \
    "v_exp_f32 v24, v24\n\t" \
    "v_exp_f32 v25, v25\n\t" \
    "s_waitcnt lgkmcnt(2)\n\t" \
    "v_smfmac_f32_32x32x32_bf16 v[0:15], v[56:59], v[16:23], v64\n\t" \
    "ds_read_b128 v[56:59], v66 offset:24192\n\t" \
    "v_exp_f32 v26, v26\n\t" \
    "v_exp_f32 v27, v27\n\t" \
    "v_exp_f32 v28, v28\n\t" \
    "v_exp_f32 v29, v29\n\t" \
    "v_exp_f32 v30, v30\n\t" \
    "v_exp_f32 v31, v31\n\t" \
    "s_setprio 2\n\t" \
    "v_exp_f32 v32, v32\n\t" \
    "v_exp_f32 v33, v33\n\t" \
    "s_waitcnt lgkmcnt(2)\n\t" \
    "v_smfmac_f32_32x32x32_bf16 v[0:15], v[60:63], v[24:31], v64\n\t" \
    "ds_read_b128 v[60:63], v66 offset:24768\n\t" \
    "v_exp_f32 v34, v34\n\t" \
    "v_exp_f32 v35, v35\n\t" \
    "s_add_u32 s54, s54, 8192\n\t" \
    "s_addc_u32 s55, s55, 0\n\t" \
    "global_load_dwordx4 a[24:27], v67, s[54:55] offset:-4096\n\t" \
    "global_load_dwordx4 a[28:31], v67, s[54:55] offset:-3072\n\t" \
    "v_exp_f32 v36, v36\n\t" \
    "v_exp_f32 v37, v37\n\t" \
    "s_waitcnt lgkmcnt(2)\n\t" \
    "v_mfma_f32_32x32x16_bf16 v[16:31], v[52:55], v[48:51], 0\n\t" \
    "ds_read_b128 v[52:55], v67 offset:23552\n\t" \
    "v_exp_f32 v38, v38\n\t" \
    "v_exp_f32 v39, v39\n\t" \
    "v_exp_f32 v40, v40\n\t" \
    "v_exp_f32 v41, v41\n\t" \
    "s_waitcnt lgkmcnt(2)\n\t" \
    "v_smfmac_f32_32x32x32_bf16 v[0:15], v[56:59], v[32:39], v64\n\t" \
    "ds_read_b128 v[56:59], v66 offset:25344\n\t" \
    "v_exp_f32 v42, v42\n\t" \
    "v_exp_f32 v43, v43\n\t" \
    "v_exp_f32 v44, v44\n\t" \
    "v_exp_f32 v45, v45\n\t" \
    "v_exp_f32 v46, v46\n\t" \
    "v_exp_f32 v47, v47\n\t" \
    "s_setprio 1\n\t" \
    "v_exp_f32 v16, v16\n\t" \
    "v_exp_f32 v17, v17\n\t" \
    "s_waitcnt lgkmcnt(2)\n\t" \
    "v_smfmac_f32_32x32x32_bf16 v[0:15], v[60:63], v[40:47], v64\n\t" \
    "ds_read_b128 v[60:63], v66 offset:25920\n\t" \
    "v_exp_f32 v18, v18\n\t" \
    "v_exp_f32 v19, v19\n\t" \
    "global_load_dwordx4 a[32:35], v67, s[54:55] offset:-2048\n\t" \
    "global_load_dwordx4 a[36:39], v67, s[54:55] offset:-1024\n\t" \
    "v_exp_f32 v20, v20\n\t" \
    "v_exp_f32 v21, v21\n\t" \
    "s_waitcnt lgkmcnt(2)\n\t" \
    "v_mfma_f32_32x32x16_bf16 v[32:47], v[52:55], v[48:51], 0\n\t" \
    "ds_read_b128 v[52:55], v67 offset:24576\n\t" \
    "v_exp_f32 v22, v22\n\t" \
    "v_exp_f32 v23, v23\n\t" \
    "v_exp_f32 v24, v24\n\t" \
    "v_exp_f32 v25, v25\n\t" \
    "s_waitcnt lgkmcnt(2)\n\t" \
    "v_smfmac_f32_32x32x32_bf16 v[0:15], v[56:59], v[16:23], v64\n\t" \
    "ds_read_b128 v[56:59], v66 offset:26496\n\t" \
    "v_exp_f32 v26, v26\n\t" \
    "v_exp_f32 v27, v27\n\t" \
    "v_exp_f32 v28, v28\n\t" \
    "v_exp_f32 v29, v29\n\t" \
    "v_exp_f32 v30, v30\n\t" \
    "v_exp_f32 v31, v31\n\t" \
    "s_setprio 0\n\t" \
    "v_exp_f32 v32, v32\n\t" \
    "v_exp_f32 v33, v33\n\t" \
    "s_waitcnt lgkmcnt(2)\n\t" \
    "v_smfmac_f32_32x32x32_bf16 v[0:15], v[60:63], v[24:31], v64\n\t" \
    "ds_read_b128 v[60:63], v66 offset:27072\n\t" \
    "v_exp_f32 v34, v34\n\t" \
    "v_exp_f32 v35, v35\n\t" \
    "global_load_dwordx4 a[40:43], v67, s[54:55] offset:0\n\t" \
    "global_load_dwordx4 a[44:47], v67, s[54:55] offset:1024\n\t" \
    "v_exp_f32 v36, v36\n\t" \
    "v_exp_f32 v37, v37\n\t" \
    "s_waitcnt lgkmcnt(2)\n\t" \
    "v_mfma_f32_32x32x16_bf16 v[16:31], v[52:55], v[48:51], 0\n\t" \
    "ds_read_b128 v[52:55], v67 offset:25600\n\t" \
    "v_exp_f32 v38, v38\n\t" \
    "v_exp_f32 v39, v39\n\t" \
    "v_exp_f32 v40, v40\n\t" \
    "v_exp_f32 v41, v41\n\t" \
    "s_waitcnt lgkmcnt(2)\n\t" \
    "v_smfmac_f32_32x32x32_bf16 v[0:15], v[56:59], v[32:39], v64\n\t" \
    "ds_read_b128 v[56:59], v66 offset:27648\n\t" \
    "v_exp_f32 v42, v42\n\t" \
    "v_exp_f32 v43, v43\n\t" \
    "v_exp_f32 v44, v44\n\t" \
    "v_exp_f32 v45, v45\n\t" \
    "v_exp_f32 v46, v46\n\t" \
    "v_exp_f32 v47, v47\n\t" \
    "s_setprio 3\n\t" \
    "v_exp_f32 v16, v16\n\t" \
    "v_exp_f32 v17, v17\n\t" \
    "s_waitcnt lgkmcnt(2)\n\t" \
    "v_smfmac_f32_32x32x32_bf16 v[0:15], v[60:63], v[40:47], v64\n\t" \
    "ds_read_b128 v[60:63], v66 offset:28224\n\t" \
    "v_exp_f32 v18, v18\n\t" \
    "v_exp_f32 v19, v19\n\t" \
    "s_add_u32 s54, s54, 8192\n\t" \
    "s_addc_u32 s55, s55, 0\n\t" \
    "global_load_dwordx4 a[48:51], v67, s[54:55] offset:-4096\n\t" \
    "global_load_dwordx4 a[52:55], v67, s[54:55] offset:-3072\n\t" \
    "v_exp_f32 v20, v20\n\t" \
    "v_exp_f32 v21, v21\n\t" \
    "s_waitcnt lgkmcnt(2)\n\t" \
    "v_mfma_f32_32x32x16_bf16 v[32:47], v[52:55], v[48:51], 0\n\t" \
    "ds_read_b128 v[52:55], v67 offset:26624\n\t" \
    "v_exp_f32 v22, v22\n\t" \
    "v_exp_f32 v23, v23\n\t" \
    "v_exp_f32 v24, v24\n\t" \
    "v_exp_f32 v25, v25\n\t" \
    "s_waitcnt lgkmcnt(2)\n\t" \
    "v_smfmac_f32_32x32x32_bf16 v[0:15], v[56:59], v[16:23], v64\n\t" \
    "ds_read_b128 v[56:59], v66 offset:28800\n\t" \
    "v_exp_f32 v26, v26\n\t" \
    "v_exp_f32 v27, v27\n\t" \
    "v_exp_f32 v28, v28\n\t" \
    "v_exp_f32 v29, v29\n\t" \
    "v_exp_f32 v30, v30\n\t" \
    "v_exp_f32 v31, v31\n\t" \
    "s_setprio 2\n\t" \
    "v_exp_f32 v32, v32\n\t" \
    "v_exp_f32 v33, v33\n\t" \
    "s_waitcnt lgkmcnt(2)\n\t" \
    "v_smfmac_f32_32x32x32_bf16 v[0:15], v[60:63], v[24:31], v64\n\t" \
    "ds_read_b128 v[60:63], v66 offset:29376\n\t" \
    "v_exp_f32 v34, v34\n\t" \
    "v_exp_f32 v35, v35\n\t" \
    "global_load_dwordx4 a[56:59], v67, s[54:55] offset:-2048\n\t" \
    "global_load_dwordx4 a[60:63], v67, s[54:55] offset:-1024\n\t" \
    "v_exp_f32 v36, v36\n\t" \
    "v_exp_f32 v37, v37\n\t" \
    "s_waitcnt lgkmcnt(2)\n\t" \
    "v_mfma_f32_32x32x16_bf16 v[16:31], v[52:55], v[48:51], 0\n\t" \
    "ds_read_b128 v[52:55], v67 offset:27648\n\t" \
    "v_exp_f32 v38, v38\n\t" \
    "v_exp_f32 v39, v39\n\t" \
    "v_exp_f32 v40, v40\n\t" \
    "v_exp_f32 v41, v41\n\t" \
    "s_waitcnt lgkmcnt(2)\n\t" \
    "v_smfmac_f32_32x32x32_bf16 v[0:15], v[56:59], v[32:39], v64\n\t" \
    "ds_read_b128 v[56:59], v66 offset:29952\n\t" \
    "v_exp_f32 v42, v42\n\t" \
    "v_exp_f32 v43, v43\n\t" \
    "v_exp_f32 v44, v44\n\t" \
    "v_exp_f32 v45, v45\n\t" \
    "v_exp_f32 v46, v46\n\t" \
    "v_exp_f32 v47, v47\n\t" \
    "s_setprio 1\n\t" \
    "v_exp_f32 v16, v16\n\t" \
    "v_exp_f32 v17, v17\n\t" \
    "s_waitcnt lgkmcnt(2)\n\t" \
    "v_smfmac_f32_32x32x32_bf16 v[0:15], v[60:63], v[40:47], v64\n\t" \
    "ds_read_b128 v[60:63], v66 offset:30528\n\t" \
    "v_exp_f32 v18, v18\n\t" \
    "v_exp_f32 v19, v19\n\t" \
    "global_load_dwordx4 a[64:67], v67, s[54:55] offset:0\n\t" \
    "global_load_dwordx4 a[68:71], v67, s[54:55] offset:1024\n\t" \
    "v_exp_f32 v20, v20\n\t" \
    "v_exp_f32 v21, v21\n\t" \
    "s_waitcnt lgkmcnt(2)\n\t" \
    "v_mfma_f32_32x32x16_bf16 v[32:47], v[52:55], v[48:51], 0\n\t" \
    "ds_read_b128 v[52:55], v67 offset:28672\n\t" \
    "v_exp_f32 v22, v22\n\t" \
    "v_exp_f32 v23, v23\n\t" \
    "v_exp_f32 v24, v24\n\t" \
    "v_exp_f32 v25, v25\n\t" \
    "s_waitcnt lgkmcnt(2)\n\t" \
    "v_smfmac_f32_32x32x32_bf16 v[0:15], v[56:59], v[16:23], v64\n\t" \
    "ds_read_b128 v[56:59], v66 offset:31104\n\t" \
    "v_exp_f32 v26, v26\n\t" \
    "v_exp_f32 v27, v27\n\t" \
    "v_exp_f32 v28, v28\n\t" \
    "v_exp_f32 v29, v29\n\t" \
    "v_exp_f32 v30, v30\n\t" \
    "v_exp_f32 v31, v31\n\t" \
    "s_setprio 0\n\t" \
    "v_exp_f32 v32, v32\n\t" \
    "v_exp_f32 v33, v33\n\t" \
    "s_waitcnt lgkmcnt(2)\n\t" \
    "v_smfmac_f32_32x32x32_bf16 v[0:15], v[60:63], v[24:31], v64\n\t" \
    "ds_read_b128 v[60:63], v66 offset:31680\n\t" \
    "v_exp_f32 v34, v34\n\t" \
    "v_exp_f32 v35, v35\n\t" \
    "s_add_u32 s54, s54, 8192\n\t" \
    "s_addc_u32 s55, s55, 0\n\t" \
    "global_load_dwordx4 a[72:75], v67, s[54:55] offset:-4096\n\t" \
    "global_load_dwordx4 a[76:79], v67, s[54:55] offset:-3072\n\t" \
    "v_exp_f32 v36, v36\n\t" \
    "v_exp_f32 v37, v37\n\t" \
    "s_waitcnt lgkmcnt(2)\n\t" \
    "v_mfma_f32_32x32x16_bf16 v[16:31], v[52:55], v[48:51], 0\n\t" \
    "ds_read_b128 v[52:55], v67 offset:29696\n\t" \
    "v_exp_f32 v38, v38\n\t" \
    "v_exp_f32 v39, v39\n\t" \
    "v_exp_f32 v40, v40\n\t" \
    "v_exp_f32 v41, v41\n\t" \
    "s_waitcnt lgkmcnt(2)\n\t" \
    "v_smfmac_f32_32x32x32_bf16 v[0:15], v[56:59], v[32:39], v64\n\t" \
    "ds_read_b128 v[56:59], v66 offset:32256\n\t" \
    "v_exp_f32 v42, v42\n\t" \
    "v_exp_f32 v43, v43\n\t" \
    "v_exp_f32 v44, v44\n\t" \
    "v_exp_f32 v45, v45\n\t" \
    "v_exp_f32 v46, v46\n\t" \
    "v_exp_f32 v47, v47\n\t" \
    "s_waitcnt vmcnt(20)\n\t" \
    "s_barrier\n\t" \
    "s_setprio 3\n\t" \
    "v_exp_f32 v16, v16\n\t" \
    "v_exp_f32 v17, v17\n\t" \
    "s_waitcnt lgkmcnt(2)\n\t" \
    "v_smfmac_f32_32x32x32_bf16 v[0:15], v[60:63], v[40:47], v64\n\t" \
    "ds_read_b128 v[60:63], v66 offset:32832\n\t" \
    "v_exp_f32 v18, v18\n\t" \
    "v_exp_f32 v19, v19\n\t" \
    "global_load_dwordx4 a[80:83], v67, s[54:55] offset:-2048\n\t" \
    "global_load_dwordx4 a[84:87], v67, s[54:55] offset:-1024\n\t" \
    "ds_read_b64_tr_b16 a[172:173], v69 offset:0\n\t" \
    "ds_read_b64_tr_b16 a[174:175], v69 offset:512\n\t" \
    "v_exp_f32 v20, v20\n\t" \
    "v_exp_f32 v21, v21\n\t" \
    "s_waitcnt lgkmcnt(4)\n\t" \
    "v_mfma_f32_32x32x16_bf16 v[32:47], v[52:55], v[48:51], 0\n\t" \
    "ds_read_b128 v[52:55], v67 offset:30720\n\t" \
    "v_exp_f32 v22, v22\n\t" \
    "v_exp_f32 v23, v23\n\t" \
    "v_exp_f32 v24, v24\n\t" \
    "v_exp_f32 v25, v25\n\t" \
    "s_waitcnt lgkmcnt(4)\n\t" \
    "v_smfmac_f32_32x32x32_bf16 v[0:15], v[56:59], v[16:23], v64\n\t" \
    "ds_read_b128 v[56:59], v66 offset:33408\n\t" \
    "v_exp_f32 v26, v26\n\t" \
    "v_exp_f32 v27, v27\n\t" \
    "v_exp_f32 v28, v28\n\t" \
    "v_exp_f32 v29, v29\n\t" \
    "v_exp_f32 v30, v30\n\t" \
    "v_exp_f32 v31, v31\n\t" \
    "s_setprio 2\n\t" \
    "v_exp_f32 v32, v32\n\t" \
    "v_exp_f32 v33, v33\n\t" \
    "s_waitcnt lgkmcnt(4)\n\t" \
    "v_smfmac_f32_32x32x32_bf16 v[0:15], v[60:63], v[24:31], v64\n\t" \
    "ds_read_b128 v[60:63], v66 offset:33984\n\t" \
    "v_exp_f32 v34, v34\n\t" \
    "v_exp_f32 v35, v35\n\t" \
    "global_load_dwordx4 a[88:91], v67, s[54:55] offset:0\n\t" \
    "global_load_dwordx4 a[92:95], v67, s[54:55] offset:1024\n\t" \
    "s_waitcnt lgkmcnt(3)\n\t" \
    "global_store_dwordx4 v67, a[172:175], s[56:57] sc1\n\t" \
    "v_exp_f32 v36, v36\n\t" \
    "v_exp_f32 v37, v37\n\t" \
    "s_waitcnt lgkmcnt(2)\n\t" \
    "v_mfma_f32_32x32x16_bf16 v[16:31], v[52:55], v[48:51], 0\n\t" \
    "ds_read_b128 v[52:55], v67 offset:31744\n\t" \
    "v_exp_f32 v38, v38\n\t" \
    "v_exp_f32 v39, v39\n\t" \
    "v_exp_f32 v40, v40\n\t" \
    "v_exp_f32 v41, v41\n\t" \
    "s_waitcnt lgkmcnt(2)\n\t" \
    "v_smfmac_f32_32x32x32_bf16 v[0:15], v[56:59], v[32:39], v64\n\t" \
    "ds_read_b128 v[56:59], v66 offset:34560\n\t" \
    "v_exp_f32 v42, v42\n\t" \
    "v_exp_f32 v43, v43\n\t" \
    "v_exp_f32 v44, v44\n\t" \
    "v_exp_f32 v45, v45\n\t" \
    "v_exp_f32 v46, v46\n\t" \
    "v_exp_f32 v47, v47\n\t" \
    "s_setprio 1\n\t" \
    "v_exp_f32 v16, v16\n\t" \
    "v_exp_f32 v17, v17\n\t" \
    "s_waitcnt lgkmcnt(2)\n\t" \
    "v_smfmac_f32_32x32x32_bf16 v[0:15], v[60:63], v[40:47], v64\n\t" \
    "ds_read_b128 v[60:63], v66 offset:35136\n\t" \
    "v_exp_f32 v18, v18\n\t" \
    "v_exp_f32 v19, v19\n\t" \
    "s_add_u32 s54, s54, 8192\n\t" \
    "s_addc_u32 s55, s55, 0\n\t" \
    "global_load_dwordx4 a[96:99], v67, s[54:55] offset:-4096\n\t" \
    "global_load_dwordx4 a[100:103], v67, s[54:55] offset:-3072\n\t" \
    "v_exp_f32 v20, v20\n\t" \
    "v_exp_f32 v21, v21\n\t" \
    "s_waitcnt lgkmcnt(2)\n\t" \
    "v_mfma_f32_32x32x16_bf16 v[32:47], v[52:55], v[48:51], 0\n\t" \
    "ds_read_b128 v[52:55], v67 offset:32768\n\t" \
    "v_exp_f32 v22, v22\n\t" \
    "v_exp_f32 v23, v23\n\t" \
    "v_exp_f32 v24, v24\n\t" \
    "v_exp_f32 v25, v25\n\t" \
    "s_waitcnt lgkmcnt(2)\n\t" \
    "v_smfmac_f32_32x32x32_bf16 v[0:15], v[56:59], v[16:23], v64\n\t" \
    "ds_read_b128 v[56:59], v66 offset:35712\n\t" \
    "v_exp_f32 v26, v26\n\t" \
    "v_exp_f32 v27, v27\n\t" \
    "v_exp_f32 v28, v28\n\t" \
    "v_exp_f32 v29, v29\n\t" \
    "v_exp_f32 v30, v30\n\t" \
    "v_exp_f32 v31, v31\n\t" \
    "s_setprio 0\n\t" \
    "v_exp_f32 v32, v32\n\t" \
    "v_exp_f32 v33, v33\n\t" \
    "s_waitcnt lgkmcnt(2)\n\t" \
    "v_smfmac_f32_32x32x32_bf16 v[0:15], v[60:63], v[24:31], v64\n\t" \
    "ds_read_b128 v[60:63], v66 offset:36288\n\t" \
    "v_exp_f32 v34, v34\n\t" \
    "v_exp_f32 v35, v35\n\t" \
    "global_load_dwordx4 a[104:107], v67, s[54:55] offset:-2048\n\t" \
    "global_load_dwordx4 a[108:111], v67, s[54:55] offset:-1024\n\t" \
    "v_exp_f32 v36, v36\n\t" \
    "v_exp_f32 v37, v37\n\t" \
    "s_waitcnt lgkmcnt(2)\n\t" \
    "v_mfma_f32_32x32x16_bf16 v[16:31], v[52:55], v[48:51], 0\n\t" \
    "ds_read_b128 v[52:55], v67 offset:33792\n\t" \
    "v_exp_f32 v38, v38\n\t" \
    "v_exp_f32 v39, v39\n\t" \
    "v_exp_f32 v40, v40\n\t" \
    "v_exp_f32 v41, v41\n\t" \
    "s_waitcnt lgkmcnt(2)\n\t" \
    "v_smfmac_f32_32x32x32_bf16 v[0:15], v[56:59], v[32:39], v64\n\t" \
    "ds_read_b128 v[56:59], v66 offset:36864\n\t" \
    "v_exp_f32 v42, v42\n\t" \
    "v_exp_f32 v43, v43\n\t" \
    "v_exp_f32 v44, v44\n\t" \
    "v_exp_f32 v45, v45\n\t" \
    "v_exp_f32 v46, v46\n\t" \
    "v_exp_f32 v47, v47\n\t" \
    "v_add_u32 v66, 0x9000, v66\n\t" \
    "s_setprio 3\n\t" \
    "v_exp_f32 v16, v16\n\t" \
    "v_exp_f32 v17, v17\n\t" \
    "s_waitcnt lgkmcnt(2)\n\t" \
    "v_smfmac_f32_32x32x32_bf16 v[0:15], v[60:63], v[40:47], v64\n\t" \
    "ds_read_b128 v[60:63], v66 offset:576\n\t" \
    "v_exp_f32 v18, v18\n\t" \
    "v_exp_f32 v19, v19\n\t" \
    "global_load_dwordx4 a[112:115], v67, s[54:55] offset:0\n\t" \
    "global_load_dwordx4 a[116:119], v67, s[54:55] offset:1024\n\t" \
    "v_exp_f32 v20, v20\n\t" \
    "v_exp_f32 v21, v21\n\t" \
    "s_waitcnt lgkmcnt(2)\n\t" \
    "v_mfma_f32_32x32x16_bf16 v[32:47], v[52:55], v[48:51], 0\n\t" \
    "ds_read_b128 v[52:55], v67 offset:34816\n\t" \
    "v_exp_f32 v22, v22\n\t" \
    "v_exp_f32 v23, v23\n\t" \
    "v_exp_f32 v24, v24\n\t" \
    "v_exp_f32 v25, v25\n\t" \
    "s_waitcnt lgkmcnt(2)\n\t" \
    "v_smfmac_f32_32x32x32_bf16 v[0:15], v[56:59], v[16:23], v64\n\t" \
    "ds_read_b128 v[56:59], v66 offset:1152\n\t" \
    "v_exp_f32 v26, v26\n\t" \
    "v_exp_f32 v27, v27\n\t" \
    "v_exp_f32 v28, v28\n\t" \
    "v_exp_f32 v29, v29\n\t" \
    "v_exp_f32 v30, v30\n\t" \
    "v_exp_f32 v31, v31\n\t" \
    "s_setprio 2\n\t" \
    "v_exp_f32 v32, v32\n\t" \
    "v_exp_f32 v33, v33\n\t" \
    "s_waitcnt lgkmcnt(2)\n\t" \
    "v_smfmac_f32_32x32x32_bf16 v[0:15], v[60:63], v[24:31], v64\n\t" \
    "ds_read_b128 v[60:63], v66 offset:1728\n\t" \
    "v_exp_f32 v34, v34\n\t" \
    "v_exp_f32 v35, v35\n\t" \
    "s_add_u32 s54, s54, 8192\n\t" \
    "s_addc_u32 s55, s55, 0\n\t" \
    "global_load_dwordx4 a[120:123], v67, s[54:55] offset:-4096\n\t" \
    "global_load_dwordx4 a[124:127], v67, s[54:55] offset:-3072\n\t" \
    "ds_read_b64_tr_b16 a[176:177], v69 offset:8192\n\t" \
    "ds_read_b64_tr_b16 a[178:179], v69 offset:8704\n\t" \
    "v_exp_f32 v36, v36\n\t" \
    "v_exp_f32 v37, v37\n\t" \
    "s_waitcnt lgkmcnt(4)\n\t" \
    "v_mfma_f32_32x32x16_bf16 v[16:31], v[52:55], v[48:51], 0\n\t" \
    "ds_read_b128 v[52:55], v67 offset:35840\n\t" \
    "v_exp_f32 v38, v38\n\t" \
    "v_exp_f32 v39, v39\n\t" \
    "v_exp_f32 v40, v40\n\t" \
    "v_exp_f32 v41, v41\n\t" \
    "s_waitcnt lgkmcnt(4)\n\t" \
    "v_smfmac_f32_32x32x32_bf16 v[0:15], v[56:59], v[32:39], v64\n\t" \
    "ds_read_b128 v[56:59], v66 offset:2304\n\t" \
    "v_exp_f32 v42, v42\n\t" \
    "v_exp_f32 v43, v43\n\t" \
    "v_exp_f32 v44, v44\n\t" \
    "v_exp_f32 v45, v45\n\t" \
    "v_exp_f32 v46, v46\n\t" \
    "v_exp_f32 v47, v47\n\t" \
    "s_setprio 1\n\t" \
    "v_exp_f32 v16, v16\n\t" \
    "v_exp_f32 v17, v17\n\t" \
    "s_waitcnt lgkmcnt(4)\n\t" \
    "v_smfmac_f32_32x32x32_bf16 v[0:15], v[60:63], v[40:47], v64\n\t" \
    "ds_read_b128 v[60:63], v66 offset:2880\n\t" \
    "v_exp_f32 v18, v18\n\t" \
    "v_exp_f32 v19, v19\n\t" \
    "global_load_dwordx4 a[128:131], v67, s[54:55] offset:-2048\n\t" \
    "global_load_dwordx4 a[132:135], v67, s[54:55] offset:-1024\n\t" \
    "s_waitcnt lgkmcnt(3)\n\t" \
    "global_store_dwordx4 v67, a[176:179], s[56:57] offset:1024 sc1\n\t" \
    "v_exp_f32 v20, v20\n\t" \
    "v_exp_f32 v21, v21\n\t" \
    "s_waitcnt lgkmcnt(2)\n\t" \
    "v_mfma_f32_32x32x16_bf16 v[32:47], v[52:55], v[48:51], 0\n\t" \
    "ds_read_b128 v[52:55], v67 offset:36864\n\t" \
    "v_exp_f32 v22, v22\n\t" \
    "v_exp_f32 v23, v23\n\t" \
    "v_exp_f32 v24, v24\n\t" \
    "v_exp_f32 v25, v25\n\t" \
    "s_waitcnt lgkmcnt(2)\n\t" \
    "v_smfmac_f32_32x32x32_bf16 v[0:15], v[56:59], v[16:23], v64\n\t" \
    "ds_read_b128 v[56:59], v66 offset:3456\n\t" \
    "v_exp_f32 v26, v26\n\t" \
    "v_exp_f32 v27, v27\n\t" \
    "v_exp_f32 v28, v28\n\t" \
    "v_exp_f32 v29, v29\n\t" \
    "v_exp_f32 v30, v30\n\t" \
    "v_exp_f32 v31, v31\n\t" \
    "s_setprio 0\n\t" \
    "v_exp_f32 v32, v32\n\t" \
    "v_exp_f32 v33, v33\n\t" \
    "s_waitcnt lgkmcnt(2)\n\t" \
    "v_smfmac_f32_32x32x32_bf16 v[0:15], v[60:63], v[24:31], v64\n\t" \
    "ds_read_b128 v[60:63], v66 offset:4032\n\t" \
    "v_exp_f32 v34, v34\n\t" \
    "v_exp_f32 v35, v35\n\t" \
    "global_load_dwordx4 a[136:139], v67, s[54:55] offset:0\n\t" \
    "global_load_dwordx4 a[140:143], v67, s[54:55] offset:1024\n\t" \
    "v_exp_f32 v36, v36\n\t" \
    "v_exp_f32 v37, v37\n\t" \
    "s_waitcnt lgkmcnt(2)\n\t" \
    "v_mfma_f32_32x32x16_bf16 v[16:31], v[52:55], v[48:51], 0\n\t" \
    "ds_read_b128 v[52:55], v67 offset:37888\n\t" \
    "v_exp_f32 v38, v38\n\t" \
    "v_exp_f32 v39, v39\n\t" \
    "v_exp_f32 v40, v40\n\t" \
    "v_exp_f32 v41, v41\n\t" \
    "s_waitcnt lgkmcnt(2)\n\t" \
    "v_smfmac_f32_32x32x32_bf16 v[0:15], v[56:59], v[32:39], v64\n\t" \
    "ds_read_b128 v[56:59], v66 offset:4608\n\t" \
    "v_exp_f32 v42, v42\n\t" \
    "v_exp_f32 v43, v43\n\t" \
    "v_exp_f32 v44, v44\n\t" \
    "v_exp_f32 v45, v45\n\t" \
    "v_exp_f32 v46, v46\n\t" \
    "v_exp_f32 v47, v47\n\t" \
    "s_setprio 3\n\t" \
    "v_exp_f32 v16, v16\n\t" \
    "v_exp_f32 v17, v17\n\t" \
    "s_waitcnt lgkmcnt(2)\n\t" \
    "v_smfmac_f32_32x32x32_bf16 v[0:15], v[60:63], v[40:47], v64\n\t" \
    "ds_read_b128 v[60:63], v66 offset:5184\n\t" \
    "v_exp_f32 v18, v18\n\t" \
    "v_exp_f32 v19, v19\n\t" \
    "s_add_u32 s54, s54, 8192\n\t" \
    "s_addc_u32 s55, s55, 0\n\t" \
    "global_load_dwordx4 a[144:147], v67, s[54:55] offset:-4096\n\t" \
    "v_exp_f32 v20, v20\n\t" \
    "v_exp_f32 v21, v21\n\t" \
    "s_waitcnt lgkmcnt(2)\n\t" \
    "v_mfma_f32_32x32x16_bf16 v[32:47], v[52:55], v[48:51], 0\n\t" \
    "ds_read_b128 v[52:55], v67 offset:38912\n\t" \
    "v_exp_f32 v22, v22\n\t" \
    "v_exp_f32 v23, v23\n\t" \
    "v_exp_f32 v24, v24\n\t" \
    "v_exp_f32 v25, v25\n\t" \
    "s_waitcnt lgkmcnt(2)\n\t" \
    "v_smfmac_f32_32x32x32_bf16 v[0:15], v[56:59], v[16:23], v64\n\t" \
    "ds_read_b128 v[56:59], v66 offset:5760\n\t" \
    "v_exp_f32 v26, v26\n\t" \
    "v_exp_f32 v27, v27\n\t" \
    "v_exp_f32 v28, v28\n\t" \
    "v_exp_f32 v29, v29\n\t" \
    "v_exp_f32 v30, v30\n\t" \
    "v_exp_f32 v31, v31\n\t" \
    "s_setprio 2\n\t" \
    "v_exp_f32 v32, v32\n\t" \
    "v_exp_f32 v33, v33\n\t" \
    "s_waitcnt lgkmcnt(2)\n\t" \
    "v_smfmac_f32_32x32x32_bf16 v[0:15], v[60:63], v[24:31], v64\n\t" \
    "ds_read_b128 v[60:63], v66 offset:6336\n\t" \
    "v_exp_f32 v34, v34\n\t" \
    "v_exp_f32 v35, v35\n\t" \
    "global_load_dwordx4 a[148:151], v67, s[54:55] offset:-3072\n\t" \
    "v_exp_f32 v36, v36\n\t" \
    "v_exp_f32 v37, v37\n\t" \
    "s_waitcnt lgkmcnt(2)\n\t" \
    "v_mfma_f32_32x32x16_bf16 v[16:31], v[52:55], v[48:51], 0\n\t" \
    "ds_read_b128 v[52:55], v67 offset:39936\n\t" \
    "v_exp_f32 v38, v38\n\t" \
    "v_exp_f32 v39, v39\n\t" \
    "v_exp_f32 v40, v40\n\t" \
    "v_exp_f32 v41, v41\n\t" \
    "s_waitcnt lgkmcnt(2)\n\t" \
    "v_smfmac_f32_32x32x32_bf16 v[0:15], v[56:59], v[32:39], v64\n\t" \
    "ds_read_b128 v[56:59], v66 offset:6912\n\t" \
    "v_exp_f32 v42, v42\n\t" \
    "v_exp_f32 v43, v43\n\t" \
    "v_exp_f32 v44, v44\n\t" \
    "v_exp_f32 v45, v45\n\t" \
    "v_exp_f32 v46, v46\n\t" \
    "v_exp_f32 v47, v47\n\t" \
    "s_setprio 1\n\t" \
    "v_exp_f32 v16, v16\n\t" \
    "v_exp_f32 v17, v17\n\t" \
    "s_waitcnt lgkmcnt(2)\n\t" \
    "v_smfmac_f32_32x32x32_bf16 v[0:15], v[60:63], v[40:47], v64\n\t" \
    "ds_read_b128 v[60:63], v66 offset:7488\n\t" \
    "v_exp_f32 v18, v18\n\t" \
    "v_exp_f32 v19, v19\n\t" \
    "global_load_dwordx4 a[152:155], v67, s[54:55] offset:-2048\n\t" \
    "ds_read_b64_tr_b16 a[180:181], v70 offset:0\n\t" \
    "ds_read_b64_tr_b16 a[182:183], v70 offset:1024\n\t" \
    "v_exp_f32 v20, v20\n\t" \
    "v_exp_f32 v21, v21\n\t" \
    "s_waitcnt lgkmcnt(4)\n\t" \
    "v_mfma_f32_32x32x16_bf16 v[32:47], v[52:55], v[48:51], 0\n\t" \
    "ds_read_b128 v[52:55], v67 offset:40960\n\t" \
    "v_exp_f32 v22, v22\n\t" \
    "v_exp_f32 v23, v23\n\t" \
    "v_exp_f32 v24, v24\n\t" \
    "v_exp_f32 v25, v25\n\t" \
    "s_waitcnt lgkmcnt(4)\n\t" \
    "v_smfmac_f32_32x32x32_bf16 v[0:15], v[56:59], v[16:23], v64\n\t" \
    "ds_read_b128 v[56:59], v66 offset:8064\n\t" \
    "v_exp_f32 v26, v26\n\t" \
    "v_exp_f32 v27, v27\n\t" \
    "v_exp_f32 v28, v28\n\t" \
    "v_exp_f32 v29, v29\n\t" \
    "v_exp_f32 v30, v30\n\t" \
    "v_exp_f32 v31, v31\n\t" \
    "s_setprio 0\n\t" \
    "v_exp_f32 v32, v32\n\t" \
    "v_exp_f32 v33, v33\n\t" \
    "s_waitcnt lgkmcnt(4)\n\t" \
    "v_smfmac_f32_32x32x32_bf16 v[0:15], v[60:63], v[24:31], v64\n\t" \
    "ds_read_b128 v[60:63], v66 offset:8640\n\t" \
    "v_exp_f32 v34, v34\n\t" \
    "v_exp_f32 v35, v35\n\t" \
    "global_load_dwordx4 a[156:159], v67, s[54:55] offset:-1024\n\t" \
    "s_waitcnt lgkmcnt(3)\n\t" \
    "global_store_dwordx4 v67, a[180:183], s[58:59] sc1\n\t" \
    "v_exp_f32 v36, v36\n\t" \
    "v_exp_f32 v37, v37\n\t" \
    "s_waitcnt lgkmcnt(2)\n\t" \
    "v_mfma_f32_32x32x16_bf16 v[16:31], v[52:55], v[48:51], 0\n\t" \
    "ds_read_b128 v[52:55], v67 offset:41984\n\t" \
    "v_exp_f32 v38, v38\n\t" \
    "v_exp_f32 v39, v39\n\t" \
    "v_exp_f32 v40, v40\n\t" \
    "v_exp_f32 v41, v41\n\t" \
    "s_waitcnt lgkmcnt(2)\n\t" \
    "v_smfmac_f32_32x32x32_bf16 v[0:15], v[56:59], v[32:39], v64\n\t" \
    "ds_read_b128 v[56:59], v66 offset:9216\n\t" \
    "v_exp_f32 v42, v42\n\t" \
    "v_exp_f32 v43, v43\n\t" \
    "v_exp_f32 v44, v44\n\t" \
    "v_exp_f32 v45, v45\n\t" \
    "v_exp_f32 v46, v46\n\t" \
    "v_exp_f32 v47, v47\n\t" \
    "s_setprio 3\n\t" \
    "v_exp_f32 v16, v16\n\t" \
    "v_exp_f32 v17, v17\n\t" \
    "s_waitcnt lgkmcnt(2)\n\t" \
    "v_smfmac_f32_32x32x32_bf16 v[0:15], v[60:63], v[40:47], v64\n\t" \
    "ds_read_b128 v[60:63], v66 offset:9792\n\t" \
    "v_exp_f32 v18, v18\n\t" \
    "v_exp_f32 v19, v19\n\t" \
    "global_load_dwordx4 a[160:163], v67, s[54:55] offset:0\n\t" \
    "v_exp_f32 v20, v20\n\t" \
    "v_exp_f32 v21, v21\n\t" \
    "s_waitcnt lgkmcnt(2)\n\t" \
    "v_mfma_f32_32x32x16_bf16 v[32:47], v[52:55], v[48:51], 0\n\t" \
    "ds_read_b128 v[52:55], v67 offset:43008\n\t" \
    "v_exp_f32 v22, v22\n\t" \
    "v_exp_f32 v23, v23\n\t" \
    "v_exp_f32 v24, v24\n\t" \
    "v_exp_f32 v25, v25\n\t" \
    "s_waitcnt lgkmcnt(2)\n\t" \
    "v_smfmac_f32_32x32x32_bf16 v[0:15], v[56:59], v[16:23], v64\n\t" \
    "ds_read_b128 v[56:59], v66 offset:10368\n\t" \
    "v_exp_f32 v26, v26\n\t" \
    "v_exp_f32 v27, v27\n\t" \
    "v_exp_f32 v28, v28\n\t" \
    "v_exp_f32 v29, v29\n\t" \
    "v_exp_f32 v30, v30\n\t" \
    "v_exp_f32 v31, v31\n\t" \
    "s_setprio 2\n\t" \
    "v_exp_f32 v32, v32\n\t" \
    "v_exp_f32 v33, v33\n\t" \
    "s_waitcnt lgkmcnt(2)\n\t" \
    "v_smfmac_f32_32x32x32_bf16 v[0:15], v[60:63], v[24:31], v64\n\t" \
    "ds_read_b128 v[60:63], v66 offset:10944\n\t" \
    "v_exp_f32 v34, v34\n\t" \
    "v_exp_f32 v35, v35\n\t" \
    "global_load_dwordx4 a[164:167], v67, s[54:55] offset:1024\n\t" \
    "v_exp_f32 v36, v36\n\t" \
    "v_exp_f32 v37, v37\n\t" \
    "s_waitcnt lgkmcnt(2)\n\t" \
    "v_mfma_f32_32x32x16_bf16 v[16:31], v[52:55], v[48:51], 0\n\t" \
    "ds_read_b128 v[52:55], v67 offset:44032\n\t" \
    "v_exp_f32 v38, v38\n\t" \
    "v_exp_f32 v39, v39\n\t" \
    "v_exp_f32 v40, v40\n\t" \
    "v_exp_f32 v41, v41\n\t" \
    "s_waitcnt lgkmcnt(2)\n\t" \
    "v_smfmac_f32_32x32x32_bf16 v[0:15], v[56:59], v[32:39], v64\n\t" \
    "ds_read_b128 v[56:59], v66 offset:11520\n\t" \
    "v_exp_f32 v42, v42\n\t" \
    "v_exp_f32 v43, v43\n\t" \
    "v_exp_f32 v44, v44\n\t" \
    "v_exp_f32 v45, v45\n\t" \
    "v_exp_f32 v46, v46\n\t" \
    "v_exp_f32 v47, v47\n\t" \
    "s_setprio 1\n\t" \
    "v_exp_f32 v16, v16\n\t" \
    "v_exp_f32 v17, v17\n\t" \
    "s_waitcnt lgkmcnt(2)\n\t" \
    "v_smfmac_f32_32x32x32_bf16 v[0:15], v[60:63], v[40:47], v64\n\t" \
    "ds_read_b128 v[60:63], v66 offset:12096\n\t" \
    "v_exp_f32 v18, v18\n\t" \
    "v_exp_f32 v19, v19\n\t" \
    "s_add_u32 s54, s54, 8192\n\t" \
    "s_addc_u32 s55, s55, 0\n\t" \
    "global_load_dwordx4 a[168:171], v67, s[54:55] offset:-4096\n\t" \
    "v_exp_f32 v20, v20\n\t" \
    "v_exp_f32 v21, v21\n\t" \
    "s_waitcnt lgkmcnt(2)\n\t" \
    "v_mfma_f32_32x32x16_bf16 v[32:47], v[52:55], v[48:51], 0\n\t" \
    "ds_read_b128 v[52:55], v67 offset:45056\n\t" \
    "v_exp_f32 v22, v22\n\t" \
    "v_exp_f32 v23, v23\n\t" \
    "v_exp_f32 v24, v24\n\t" \
    "v_exp_f32 v25, v25\n\t" \
    "s_waitcnt lgkmcnt(2)\n\t" \
    "v_smfmac_f32_32x32x32_bf16 v[0:15], v[56:59], v[16:23], v64\n\t" \
    "ds_read_b128 v[56:59], v66 offset:12672\n\t" \
    "v_exp_f32 v26, v26\n\t" \
    "v_exp_f32 v27, v27\n\t" \
    "v_exp_f32 v28, v28\n\t" \
    "v_exp_f32 v29, v29\n\t" \
    "v_exp_f32 v30, v30\n\t" \
    "v_exp_f32 v31, v31\n\t" \
    "s_setprio 0\n\t" \
    "v_exp_f32 v32, v32\n\t" \
    "v_exp_f32 v33, v33\n\t" \
    "s_waitcnt lgkmcnt(2)\n\t" \
    "v_smfmac_f32_32x32x32_bf16 v[0:15], v[60:63], v[24:31], v64\n\t" \
    "ds_read_b128 v[60:63], v66 offset:13248\n\t" \
    "v_exp_f32 v34, v34\n\t" \
    "v_exp_f32 v35, v35\n\t" \
    "global_load_dwordx4 a[172:175], v67, s[54:55] offset:-3072\n\t" \
    "v_exp_f32 v36, v36\n\t" \
    "v_exp_f32 v37, v37\n\t" \
    "s_waitcnt lgkmcnt(2)\n\t" \
    "v_mfma_f32_32x32x16_bf16 v[16:31], v[52:55], v[48:51], 0\n\t" \
    "ds_read_b128 v[52:55], v67 offset:46080\n\t" \
    "v_exp_f32 v38, v38\n\t" \
    "v_exp_f32 v39, v39\n\t" \
    "v_exp_f32 v40, v40\n\t" \
    "v_exp_f32 v41, v41\n\t" \
    "s_waitcnt lgkmcnt(2)\n\t" \
    "v_smfmac_f32_32x32x32_bf16 v[0:15], v[56:59], v[32:39], v64\n\t" \
    "ds_read_b128 v[56:59], v66 offset:13824\n\t" \
    "v_exp_f32 v42, v42\n\t" \
    "v_exp_f32 v43, v43\n\t" \
    "v_exp_f32 v44, v44\n\t" \
    "v_exp_f32 v45, v45\n\t" \
    "v_exp_f32 v46, v46\n\t" \
    "v_exp_f32 v47, v47\n\t" \
    "s_setprio 3\n\t" \
    "v_exp_f32 v16, v16\n\t" \
    "v_exp_f32 v17, v17\n\t" \
    "s_waitcnt lgkmcnt(2)\n\t" \
    "v_smfmac_f32_32x32x32_bf16 v[0:15], v[60:63], v[40:47], v64\n\t" \
    "ds_read_b128 v[60:63], v66 offset:14400\n\t" \
    "v_exp_f32 v18, v18\n\t" \
    "v_exp_f32 v19, v19\n\t" \
    "global_load_dwordx4 a[176:179], v67, s[54:55] offset:-2048\n\t" \
    "v_exp_f32 v20, v20\n\t" \
    "v_exp_f32 v21, v21\n\t" \
    "s_waitcnt lgkmcnt(2)\n\t" \
    "v_mfma_f32_32x32x16_bf16 v[32:47], v[52:55], v[48:51], 0\n\t" \
    "ds_read_b128 v[52:55], v67 offset:47104\n\t" \
    "v_exp_f32 v22, v22\n\t" \
    "v_exp_f32 v23, v23\n\t" \
    "v_exp_f32 v24, v24\n\t" \
    "v_exp_f32 v25, v25\n\t" \
    "s_waitcnt lgkmcnt(2)\n\t" \
    "v_smfmac_f32_32x32x32_bf16 v[0:15], v[56:59], v[16:23], v64\n\t" \
    "ds_read_b128 v[56:59], v66 offset:14976\n\t" \
    "v_exp_f32 v26, v26\n\t" \
    "v_exp_f32 v27, v27\n\t" \
    "v_exp_f32 v28, v28\n\t" \
    "v_exp_f32 v29, v29\n\t" \
    "v_exp_f32 v30, v30\n\t" \
    "v_exp_f32 v31, v31\n\t" \
    "s_setprio 2\n\t" \
    "v_exp_f32 v32, v32\n\t" \
    "v_exp_f32 v33, v33\n\t" \
    "s_waitcnt lgkmcnt(2)\n\t" \
    "v_smfmac_f32_32x32x32_bf16 v[0:15], v[60:63], v[24:31], v64\n\t" \
    "ds_read_b128 v[60:63], v66 offset:15552\n\t" \
    "v_exp_f32 v34, v34\n\t" \
    "v_exp_f32 v35, v35\n\t" \
    "global_load_dwordx4 a[180:183], v67, s[54:55] offset:-1024\n\t" \
    "v_exp_f32 v36, v36\n\t" \
    "v_exp_f32 v37, v37\n\t" \
    "s_waitcnt lgkmcnt(2)\n\t" \
    "v_mfma_f32_32x32x16_bf16 v[16:31], v[52:55], v[48:51], 0\n\t" \
    "ds_read_b128 v[52:55], v67 offset:48128\n\t" \
    "v_exp_f32 v38, v38\n\t" \
    "v_exp_f32 v39, v39\n\t" \
    "v_exp_f32 v40, v40\n\t" \
    "v_exp_f32 v41, v41\n\t" \
    "s_waitcnt lgkmcnt(2)\n\t" \
    "v_smfmac_f32_32x32x32_bf16 v[0:15], v[56:59], v[32:39], v64\n\t" \
    "ds_read_b128 v[56:59], v66 offset:16128\n\t" \
    "v_exp_f32 v42, v42\n\t" \
    "v_exp_f32 v43, v43\n\t" \
    "v_exp_f32 v44, v44\n\t" \
    "v_exp_f32 v45, v45\n\t" \
    "v_exp_f32 v46, v46\n\t" \
    "v_exp_f32 v47, v47\n\t" \
    "s_waitcnt vmcnt(6) lgkmcnt(0)\n\t" \
    "s_barrier\n\t" \
    "s_setprio 1\n\t" \
    "v_exp_f32 v16, v16\n\t" \
    "v_exp_f32 v17, v17\n\t" \
    "v_smfmac_f32_32x32x32_bf16 v[0:15], v[60:63], v[40:47], v64\n\t" \
    "ds_read_b128 v[60:63], v66 offset:16704\n\t" \
    "v_exp_f32 v18, v18\n\t" \
    "v_exp_f32 v19, v19\n\t" \
    "s_lshl_b32 s46, s43, 11\n\t" \
    "s_add_u32 m0, s46, 139264\n\t" \
    "s_mul_i32 s47, s43, 6144\n\t" \
    "global_load_lds_dwordx4 v67, s[62:63]\n\t" \
    "s_cmp_lg_u32 s43, 0\n\t" \
    "s_cbranch_scc1 .Ljf_%=\n\t" \
    "s_mov_b64 exec, 1\n\t" \
    "global_store_dword v67, v64, s[98:99] sc1\n\t" \
    "s_mov_b64 exec, -1\n\t" \
    ".Ljf_%=:\n\t" \
    "v_exp_f32 v20, v20\n\t" \
    "v_exp_f32 v21, v21\n\t" \
    "v_mfma_f32_32x32x16_bf16 v[32:47], v[52:55], v[48:51], 0\n\t" \
    "ds_read_b128 v[52:55], v67 offset:49152\n\t" \
    "v_exp_f32 v22, v22\n\t" \
    "v_exp_f32 v23, v23\n\t" \
    "v_exp_f32 v24, v24\n\t" \
    "v_exp_f32 v25, v25\n\t" \
    "v_smfmac_f32_32x32x32_bf16 v[0:15], v[56:59], v[16:23], v64\n\t" \
    "ds_read_b128 v[56:59], v66 offset:17280\n\t" \
    "v_exp_f32 v26, v26\n\t" \
    "v_exp_f32 v27, v27\n\t" \
    "v_exp_f32 v28, v28\n\t" \
    "v_exp_f32 v29, v29\n\t" \
    "v_exp_f32 v30, v30\n\t" \
    "v_exp_f32 v31, v31\n\t" \
    "s_setprio 0\n\t" \
    "v_exp_f32 v32, v32\n\t" \
    "v_exp_f32 v33, v33\n\t" \
    "s_waitcnt lgkmcnt(2)\n\t" \
    "v_smfmac_f32_32x32x32_bf16 v[0:15], v[60:63], v[24:31], v64\n\t" \
    "ds_read_b128 v[60:63], v66 offset:17856\n\t" \
    "v_exp_f32 v34, v34\n\t" \
    "v_exp_f32 v35, v35\n\t" \
    "global_load_lds_dwordx4 v67, s[62:63] offset:1024\n\t" \
    "v_exp_f32 v36, v36\n\t" \
    "v_exp_f32 v37, v37\n\t" \
    "s_waitcnt lgkmcnt(2)\n\t" \
    "v_mfma_f32_32x32x16_bf16 v[16:31], v[52:55], v[48:51], 0\n\t" \
    "ds_read_b128 v[52:55], v67 offset:50176\n\t" \
    "v_exp_f32 v38, v38\n\t" \
    "v_exp_f32 v39, v39\n\t" \
    "v_exp_f32 v40, v40\n\t" \
    "v_exp_f32 v41, v41\n\t" \
    "s_waitcnt lgkmcnt(2)\n\t" \
    "v_smfmac_f32_32x32x32_bf16 v[0:15], v[56:59], v[32:39], v64\n\t" \
    "ds_read_b128 v[56:59], v66 offset:18432\n\t" \
    "v_exp_f32 v42, v42\n\t" \
    "v_exp_f32 v43, v43\n\t" \
    "v_exp_f32 v44, v44\n\t" \
    "v_exp_f32 v45, v45\n\t" \
    "v_exp_f32 v46, v46\n\t" \
    "v_exp_f32 v47, v47\n\t" \
    "s_setprio 3\n\t" \
    "v_exp_f32 v16, v16\n\t" \
    "v_exp_f32 v17, v17\n\t" \
    "s_waitcnt lgkmcnt(2)\n\t" \
    "v_smfmac_f32_32x32x32_bf16 v[0:15], v[60:63], v[40:47], v64\n\t" \
    "ds_read_b128 v[60:63], v66 offset:19008\n\t" \
    "v_exp_f32 v18, v18\n\t" \
    "v_exp_f32 v19, v19\n\t" \
    "s_add_u32 m0, s47, 0\n\t" \
    "s_add_u32 s62, s62, 8192\n\t" \
    "s_addc_u32 s63, s63, 0\n\t" \
    "global_load_lds_dwordx4 v67, s[62:63]\n\t" \
    "v_exp_f32 v20, v20\n\t" \
    "v_exp_f32 v21, v21\n\t" \
    "s_waitcnt lgkmcnt(2)\n\t" \
    "v_mfma_f32_32x32x16_bf16 v[32:47], v[52:55], v[48:51], 0\n\t" \
    "ds_read_b128 v[52:55], v67 offset:51200\n\t" \
    "v_exp_f32 v22, v22\n\t" \
    "v_exp_f32 v23, v23\n\t" \
    "v_exp_f32 v24, v24\n\t" \
    "v_exp_f32 v25, v25\n\t" \
    "s_waitcnt lgkmcnt(2)\n\t" \
    "v_smfmac_f32_32x32x32_bf16 v[0:15], v[56:59], v[16:23], v64\n\t" \
    "ds_read_b128 v[56:59], v66 offset:19584\n\t" \
    "v_exp_f32 v26, v26\n\t" \
    "v_exp_f32 v27, v27\n\t" \
    "v_exp_f32 v28, v28\n\t" \
    "v_exp_f32 v29, v29\n\t" \
    "v_exp_f32 v30, v30\n\t" \
    "v_exp_f32 v31, v31\n\t" \
    "s_setprio 2\n\t" \
    "v_exp_f32 v32, v32\n\t" \
    "v_exp_f32 v33, v33\n\t" \
    "s_waitcnt lgkmcnt(2)\n\t" \
    "v_smfmac_f32_32x32x32_bf16 v[0:15], v[60:63], v[24:31], v64\n\t" \
    "ds_read_b128 v[60:63], v66 offset:20160\n\t" \
    "v_exp_f32 v34, v34\n\t" \
    "v_exp_f32 v35, v35\n\t" \
    "global_load_lds_dwordx4 v67, s[62:63] offset:1024\n\t" \
    "v_exp_f32 v36, v36\n\t" \
    "v_exp_f32 v37, v37\n\t" \
    "s_waitcnt lgkmcnt(2)\n\t" \
    "v_mfma_f32_32x32x16_bf16 v[16:31], v[52:55], v[48:51], 0\n\t" \
    "ds_read_b128 v[52:55], v67 offset:52224\n\t" \
    "v_exp_f32 v38, v38\n\t" \
    "v_exp_f32 v39, v39\n\t" \
    "v_exp_f32 v40, v40\n\t" \
    "v_exp_f32 v41, v41\n\t" \
    "s_waitcnt lgkmcnt(2)\n\t" \
    "v_smfmac_f32_32x32x32_bf16 v[0:15], v[56:59], v[32:39], v64\n\t" \
    "ds_read_b128 v[56:59], v66 offset:20736\n\t" \
    "v_exp_f32 v42, v42\n\t" \
    "v_exp_f32 v43, v43\n\t" \
    "v_exp_f32 v44, v44\n\t" \
    "v_exp_f32 v45, v45\n\t" \
    "v_exp_f32 v46, v46\n\t" \
    "v_exp_f32 v47, v47\n\t" \
    "s_setprio 1\n\t" \
    "v_exp_f32 v16, v16\n\t" \
    "v_exp_f32 v17, v17\n\t" \
    "s_waitcnt lgkmcnt(2)\n\t" \
    "v_smfmac_f32_32x32x32_bf16 v[0:15], v[60:63], v[40:47], v64\n\t" \
    "ds_read_b128 v[60:63], v66 offset:21312\n\t" \
    "v_exp_f32 v18, v18\n\t" \
    "v_exp_f32 v19, v19\n\t" \
    "s_add_u32 m0, s47, 2048\n\t" \
    "s_add_u32 s62, s62, 8192\n\t" \
    "s_addc_u32 s63, s63, 0\n\t" \
    "global_load_lds_dwordx4 v67, s[62:63]\n\t" \
    "v_exp_f32 v20, v20\n\t" \
    "v_exp_f32 v21, v21\n\t" \
    "s_waitcnt lgkmcnt(2)\n\t" \
    "v_mfma_f32_32x32x16_bf16 v[32:47], v[52:55], v[48:51], 0\n\t" \
    "ds_read_b128 v[52:55], v67 offset:53248\n\t" \
    "v_exp_f32 v22, v22\n\t" \
    "v_exp_f32 v23, v23\n\t" \
    "v_exp_f32 v24, v24\n\t" \
    "v_exp_f32 v25, v25\n\t" \
    "s_waitcnt lgkmcnt(2)\n\t" \
    "v_smfmac_f32_32x32x32_bf16 v[0:15], v[56:59], v[16:23], v64\n\t" \
    "ds_read_b128 v[56:59], v66 offset:21888\n\t" \
    "v_exp_f32 v26, v26\n\t" \
    "v_exp_f32 v27, v27\n\t" \
    "v_exp_f32 v28, v28\n\t" \
    "v_exp_f32 v29, v29\n\t" \
    "v_exp_f32 v30, v30\n\t" \
    "v_exp_f32 v31, v31\n\t" \
    "s_setprio 0\n\t" \
    "v_exp_f32 v32, v32\n\t" \
    "v_exp_f32 v33, v33\n\t" \
    "s_waitcnt lgkmcnt(2)\n\t" \
    "v_smfmac_f32_32x32x32_bf16 v[0:15], v[60:63], v[24:31], v64\n\t" \
    "ds_read_b128 v[60:63], v66 offset:22464\n\t" \
    "v_exp_f32 v34, v34\n\t" \
    "v_exp_f32 v35, v35\n\t" \
    "global_load_lds_dwordx4 v67, s[62:63] offset:1024\n\t" \
    "v_exp_f32 v36, v36\n\t" \
    "v_exp_f32 v37, v37\n\t" \
    "s_waitcnt lgkmcnt(2)\n\t" \
    "v_mfma_f32_32x32x16_bf16 v[16:31], v[52:55], v[48:51], 0\n\t" \
    "ds_read_b128 v[52:55], v67 offset:54272\n\t" \
    "v_exp_f32 v38, v38\n\t" \
    "v_exp_f32 v39, v39\n\t" \
    "v_exp_f32 v40, v40\n\t" \
    "v_exp_f32 v41, v41\n\t" \
    "s_waitcnt lgkmcnt(2)\n\t" \
    "v_smfmac_f32_32x32x32_bf16 v[0:15], v[56:59], v[32:39], v64\n\t" \
    "ds_read_b128 v[56:59], v66 offset:23040\n\t" \
    "v_exp_f32 v42, v42\n\t" \
    "v_exp_f32 v43, v43\n\t" \
    "v_exp_f32 v44, v44\n\t" \
    "v_exp_f32 v45, v45\n\t" \
    "v_exp_f32 v46, v46\n\t" \
    "v_exp_f32 v47, v47\n\t" \
    "s_setprio 3\n\t" \
    "v_exp_f32 v16, v16\n\t" \
    "v_exp_f32 v17, v17\n\t" \
    "s_waitcnt lgkmcnt(2)\n\t" \
    "v_smfmac_f32_32x32x32_bf16 v[0:15], v[60:63], v[40:47], v64\n\t" \
    "ds_read_b128 v[60:63], v66 offset:23616\n\t" \
    "v_exp_f32 v18, v18\n\t" \
    "v_exp_f32 v19, v19\n\t" \
    "s_add_u32 m0, s47, 4096\n\t" \
    "s_add_u32 s62, s62, 8192\n\t" \
    "s_addc_u32 s63, s63, 0\n\t" \
    "global_load_lds_dwordx4 v67, s[62:63]\n\t" \
    "v_exp_f32 v20, v20\n\t" \
    "v_exp_f32 v21, v21\n\t" \
    "s_waitcnt lgkmcnt(2)\n\t" \
    "v_mfma_f32_32x32x16_bf16 v[32:47], v[52:55], v[48:51], 0\n\t" \
    "ds_read_b128 v[52:55], v67 offset:55296\n\t" \
    "v_exp_f32 v22, v22\n\t" \
    "v_exp_f32 v23, v23\n\t" \
    "v_exp_f32 v24, v24\n\t" \
    "v_exp_f32 v25, v25\n\t" \
    "s_waitcnt lgkmcnt(2)\n\t" \
    "v_smfmac_f32_32x32x32_bf16 v[0:15], v[56:59], v[16:23], v64\n\t" \
    "ds_read_b128 v[56:59], v66 offset:24192\n\t" \
    "v_exp_f32 v26, v26\n\t" \
    "v_exp_f32 v27, v27\n\t" \
    "v_exp_f32 v28, v28\n\t" \
    "v_exp_f32 v29, v29\n\t" \
    "v_exp_f32 v30, v30\n\t" \
    "v_exp_f32 v31, v31\n\t" \
    "s_setprio 2\n\t" \
    "v_exp_f32 v32, v32\n\t" \
    "v_exp_f32 v33, v33\n\t" \
    "s_waitcnt lgkmcnt(2)\n\t" \
    "v_smfmac_f32_32x32x32_bf16 v[0:15], v[60:63], v[24:31], v64\n\t" \
    "ds_read_b128 v[60:63], v66 offset:24768\n\t" \
    "v_exp_f32 v34, v34\n\t" \
    "v_exp_f32 v35, v35\n\t" \
    "global_load_lds_dwordx4 v67, s[62:63] offset:1024\n\t" \
    "v_exp_f32 v36, v36\n\t" \
    "v_exp_f32 v37, v37\n\t" \
    "s_waitcnt lgkmcnt(2)\n\t" \
    "v_mfma_f32_32x32x16_bf16 v[16:31], v[52:55], v[48:51], 0\n\t" \
    "ds_read_b128 v[52:55], v67 offset:56320\n\t" \
    "v_exp_f32 v38, v38\n\t" \
    "v_exp_f32 v39, v39\n\t" \
    "v_exp_f32 v40, v40\n\t" \
    "v_exp_f32 v41, v41\n\t" \
    "s_waitcnt lgkmcnt(2)\n\t" \
    "v_smfmac_f32_32x32x32_bf16 v[0:15], v[56:59], v[32:39], v64\n\t" \
    "ds_read_b128 v[56:59], v66 offset:25344\n\t" \
    "v_exp_f32 v42, v42\n\t" \
    "v_exp_f32 v43, v43\n\t" \
    "v_exp_f32 v44, v44\n\t" \
    "v_exp_f32 v45, v45\n\t" \
    "v_exp_f32 v46, v46\n\t" \
    "v_exp_f32 v47, v47\n\t" \
    "s_setprio 1\n\t" \
    "v_exp_f32 v16, v16\n\t" \
    "v_exp_f32 v17, v17\n\t" \
    "s_waitcnt lgkmcnt(2)\n\t" \
    "v_smfmac_f32_32x32x32_bf16 v[0:15], v[60:63], v[40:47], v64\n\t" \
    "ds_read_b128 v[60:63], v66 offset:25920\n\t" \
    "v_exp_f32 v18, v18\n\t" \
    "v_exp_f32 v19, v19\n\t" \
    "s_add_u32 m0, s47, 65536\n\t" \
    "s_add_u32 s62, s62, 8192\n\t" \
    "s_addc_u32 s63, s63, 0\n\t" \
    "global_load_lds_dwordx4 v67, s[62:63]\n\t" \
    "v_exp_f32 v20, v20\n\t" \
    "v_exp_f32 v21, v21\n\t" \
    "s_waitcnt lgkmcnt(2)\n\t" \
    "v_mfma_f32_32x32x16_bf16 v[32:47], v[52:55], v[48:51], 0\n\t" \
    "ds_read_b128 v[52:55], v67 offset:57344\n\t" \
    "v_exp_f32 v22, v22\n\t" \
    "v_exp_f32 v23, v23\n\t" \
    "v_exp_f32 v24, v24\n\t" \
    "v_exp_f32 v25, v25\n\t" \
    "s_waitcnt lgkmcnt(2)\n\t" \
    "v_smfmac_f32_32x32x32_bf16 v[0:15], v[56:59], v[16:23], v64\n\t" \
    "ds_read_b128 v[56:59], v66 offset:26496\n\t" \
    "v_exp_f32 v26, v26\n\t" \
    "v_exp_f32 v27, v27\n\t" \
    "v_exp_f32 v28, v28\n\t" \
    "v_exp_f32 v29, v29\n\t" \
    "v_exp_f32 v30, v30\n\t" \
    "v_exp_f32 v31, v31\n\t" \
    "s_setprio 0\n\t" \
    "v_exp_f32 v32, v32\n\t" \
    "v_exp_f32 v33, v33\n\t" \
    "s_waitcnt lgkmcnt(2)\n\t" \
    "v_smfmac_f32_32x32x32_bf16 v[0:15], v[60:63], v[24:31], v64\n\t" \
    "ds_read_b128 v[60:63], v66 offset:27072\n\t" \
    "v_exp_f32 v34, v34\n\t" \
    "v_exp_f32 v35, v35\n\t" \
    "global_load_lds_dwordx4 v67, s[62:63] offset:1024\n\t" \
    "v_exp_f32 v36, v36\n\t" \
    "v_exp_f32 v37, v37\n\t" \
    "s_waitcnt lgkmcnt(2)\n\t" \
    "v_mfma_f32_32x32x16_bf16 v[16:31], v[52:55], v[48:51], 0\n\t" \
    "ds_read_b128 v[52:55], v67 offset:58368\n\t" \
    "v_exp_f32 v38, v38\n\t" \
    "v_exp_f32 v39, v39\n\t" \
    "v_exp_f32 v40, v40\n\t" \
    "v_exp_f32 v41, v41\n\t" \
    "s_waitcnt lgkmcnt(2)\n\t" \
    "v_smfmac_f32_32x32x32_bf16 v[0:15], v[56:59], v[32:39], v64\n\t" \
    "ds_read_b128 v[56:59], v66 offset:27648\n\t" \
    "v_exp_f32 v42, v42\n\t" \
    "v_exp_f32 v43, v43\n\t" \
    "v_exp_f32 v44, v44\n\t" \
    "v_exp_f32 v45, v45\n\t" \
    "v_exp_f32 v46, v46\n\t" \
    "v_exp_f32 v47, v47\n\t" \
    "s_setprio 3\n\t" \
    "v_exp_f32 v16, v16\n\t" \
    "v_exp_f32 v17, v17\n\t" \
    "s_waitcnt lgkmcnt(2)\n\t" \
    "v_smfmac_f32_32x32x32_bf16 v[0:15], v[60:63], v[40:47], v64\n\t" \
    "ds_read_b128 v[60:63], v66 offset:28224\n\t" \
    "v_exp_f32 v18, v18\n\t" \
    "v_exp_f32 v19, v19\n\t" \
    "s_add_u32 m0, s47, 67584\n\t" \
    "s_add_u32 s62, s62, 8192\n\t" \
    "s_addc_u32 s63, s63, 0\n\t" \
    "global_load_lds_dwordx4 v67, s[62:63]\n\t" \
    "v_exp_f32 v20, v20\n\t" \
    "v_exp_f32 v21, v21\n\t" \
    "s_waitcnt lgkmcnt(2)\n\t" \
    "v_mfma_f32_32x32x16_bf16 v[32:47], v[52:55], v[48:51], 0\n\t" \
    "ds_read_b128 v[52:55], v67 offset:59392\n\t" \
    "v_exp_f32 v22, v22\n\t" \
    "v_exp_f32 v23, v23\n\t" \
    "v_exp_f32 v24, v24\n\t" \
    "v_exp_f32 v25, v25\n\t" \
    "s_waitcnt lgkmcnt(2)\n\t" \
    "v_smfmac_f32_32x32x32_bf16 v[0:15], v[56:59], v[16:23], v64\n\t" \
    "ds_read_b128 v[56:59], v66 offset:28800\n\t" \
    "v_exp_f32 v26, v26\n\t" \
    "v_exp_f32 v27, v27\n\t" \
    "v_exp_f32 v28, v28\n\t" \
    "v_exp_f32 v29, v29\n\t" \
    "v_exp_f32 v30, v30\n\t" \
    "v_exp_f32 v31, v31\n\t" \
    "s_setprio 2\n\t" \
    "v_exp_f32 v32, v32\n\t" \
    "v_exp_f32 v33, v33\n\t" \
    "s_waitcnt lgkmcnt(2)\n\t" \
    "v_smfmac_f32_32x32x32_bf16 v[0:15], v[60:63], v[24:31], v64\n\t" \
    "ds_read_b128 v[60:63], v66 offset:29376\n\t" \
    "v_exp_f32 v34, v34\n\t" \
    "v_exp_f32 v35, v35\n\t" \
    "global_load_lds_dwordx4 v67, s[62:63] offset:1024\n\t" \
    "v_exp_f32 v36, v36\n\t" \
    "v_exp_f32 v37, v37\n\t" \
    "s_waitcnt lgkmcnt(2)\n\t" \
    "v_mfma_f32_32x32x16_bf16 v[16:31], v[52:55], v[48:51], 0\n\t" \
    "ds_read_b128 v[52:55], v67 offset:60416\n\t" \
    "v_exp_f32 v38, v38\n\t" \
    "v_exp_f32 v39, v39\n\t" \
    "v_exp_f32 v40, v40\n\t" \
    "v_exp_f32 v41, v41\n\t" \
    "s_waitcnt lgkmcnt(2)\n\t" \
    "v_smfmac_f32_32x32x32_bf16 v[0:15], v[56:59], v[32:39], v64\n\t" \
    "ds_read_b128 v[56:59], v66 offset:29952\n\t" \
    "v_exp_f32 v42, v42\n\t" \
    "v_exp_f32 v43, v43\n\t" \
    "v_exp_f32 v44, v44\n\t" \
    "v_exp_f32 v45, v45\n\t" \
    "v_exp_f32 v46, v46\n\t" \
    "v_exp_f32 v47, v47\n\t" \
    "s_setprio 1\n\t" \
    "v_exp_f32 v16, v16\n\t" \
    "v_exp_f32 v17, v17\n\t" \
    "s_waitcnt lgkmcnt(2)\n\t" \
    "v_smfmac_f32_32x32x32_bf16 v[0:15], v[60:63], v[40:47], v64\n\t" \
    "ds_read_b128 v[60:63], v66 offset:30528\n\t" \
    "v_exp_f32 v18, v18\n\t" \
    "v_exp_f32 v19, v19\n\t" \
    "s_add_u32 m0, s47, 69632\n\t" \
    "s_add_u32 s62, s62, 8192\n\t" \
    "s_addc_u32 s63, s63, 0\n\t" \
    "global_load_lds_dwordx4 v67, s[62:63]\n\t" \
    "s_cmp_lg_u32 s43, 7\n\t" \
    "s_cbranch_scc1 .Ljr_%=\n\t" \
    "global_load_dword v69, v71, s[96:97] sc1\n\t" \
    ".Ljr_%=:\n\t" \
    "v_exp_f32 v20, v20\n\t" \
    "v_exp_f32 v21, v21\n\t" \
    "s_waitcnt lgkmcnt(2)\n\t" \
    "v_mfma_f32_32x32x16_bf16 v[32:47], v[52:55], v[48:51], 0\n\t" \
    "ds_read_b128 v[52:55], v67 offset:61440\n\t" \
    "v_exp_f32 v22, v22\n\t" \
    "v_exp_f32 v23, v23\n\t" \
    "v_exp_f32 v24, v24\n\t" \
    "v_exp_f32 v25, v25\n\t" \
    "s_waitcnt lgkmcnt(2)\n\t" \
    "v_smfmac_f32_32x32x32_bf16 v[0:15], v[56:59], v[16:23], v64\n\t" \
    "ds_read_b128 v[56:59], v66 offset:31104\n\t" \
    "v_exp_f32 v26, v26\n\t" \
    "v_exp_f32 v27, v27\n\t" \
    "v_exp_f32 v28, v28\n\t" \
    "v_exp_f32 v29, v29\n\t" \
    "v_exp_f32 v30, v30\n\t" \
    "v_exp_f32 v31, v31\n\t" \
    "s_setprio 0\n\t" \
    "v_exp_f32 v32, v32\n\t" \
    "v_exp_f32 v33, v33\n\t" \
    "s_waitcnt lgkmcnt(2)\n\t" \
    "v_smfmac_f32_32x32x32_bf16 v[0:15], v[60:63], v[24:31], v64\n\t" \
    "ds_read_b128 v[60:63], v66 offset:31680\n\t" \
    "v_exp_f32 v34, v34\n\t" \
    "v_exp_f32 v35, v35\n\t" \
    "global_load_lds_dwordx4 v67, s[62:63] offset:1024\n\t" \
    "v_exp_f32 v36, v36\n\t" \
    "v_exp_f32 v37, v37\n\t" \
    "s_waitcnt lgkmcnt(2)\n\t" \
    "v_mfma_f32_32x32x16_bf16 v[16:31], v[52:55], v[48:51], 0\n\t" \
    "ds_read_b128 v[52:55], v67 offset:62464\n\t" \
    "v_exp_f32 v38, v38\n\t" \
    "v_exp_f32 v39, v39\n\t" \
    "v_exp_f32 v40, v40\n\t" \
    "v_exp_f32 v41, v41\n\t" \
    "s_waitcnt lgkmcnt(2)\n\t" \
    "v_smfmac_f32_32x32x32_bf16 v[0:15], v[56:59], v[32:39], v64\n\t" \
    "ds_read_b128 v[56:59], v66 offset:32256\n\t" \
    "v_exp_f32 v42, v42\n\t" \
    "v_exp_f32 v43, v43\n\t" \
    "v_exp_f32 v44, v44\n\t" \
    "v_exp_f32 v45, v45\n\t" \
    "v_exp_f32 v46, v46\n\t" \
    "v_exp_f32 v47, v47\n\t" \
    "s_setprio 3\n\t" \
    "v_exp_f32 v16, v16\n\t" \
    "v_exp_f32 v17, v17\n\t" \
    "s_waitcnt lgkmcnt(2)\n\t" \
    "v_smfmac_f32_32x32x32_bf16 v[0:15], v[60:63], v[40:47], v64\n\t" \
    "ds_read_b128 v[60:63], v66 offset:32832\n\t" \
    "v_exp_f32 v18, v18\n\t" \
    "v_exp_f32 v19, v19\n\t" \
    "v_exp_f32 v20, v20\n\t" \
    "v_exp_f32 v21, v21\n\t" \
    "s_waitcnt lgkmcnt(2)\n\t" \
    "v_mfma_f32_32x32x16_bf16 v[32:47], v[52:55], v[48:51], 0\n\t" \
    "ds_read_b128 v[52:55], v67 offset:63488\n\t" \
    "v_exp_f32 v22, v22\n\t" \
    "v_exp_f32 v23, v23\n\t" \
    "v_exp_f32 v24, v24\n\t" \
    "v_exp_f32 v25, v25\n\t" \
    "s_waitcnt lgkmcnt(2)\n\t" \
    "v_smfmac_f32_32x32x32_bf16 v[0:15], v[56:59], v[16:23], v64\n\t" \
    "ds_read_b128 v[56:59], v66 offset:33408\n\t" \
    "v_exp_f32 v26, v26\n\t" \
    "v_exp_f32 v27, v27\n\t" \
    "v_exp_f32 v28, v28\n\t" \
    "v_exp_f32 v29, v29\n\t" \
    "v_exp_f32 v30, v30\n\t" \
    "v_exp_f32 v31, v31\n\t" \
    "s_setprio 2\n\t" \
    "v_exp_f32 v32, v32\n\t" \
    "v_exp_f32 v33, v33\n\t" \
    "s_waitcnt lgkmcnt(2)\n\t" \
    "v_smfmac_f32_32x32x32_bf16 v[0:15], v[60:63], v[24:31], v64\n\t" \
    "ds_read_b128 v[60:63], v66 offset:33984\n\t" \
    "v_exp_f32 v34, v34\n\t" \
    "v_exp_f32 v35, v35\n\t" \
    "v_exp_f32 v36, v36\n\t" \
    "v_exp_f32 v37, v37\n\t" \
    "s_waitcnt lgkmcnt(2)\n\t" \
    "v_mfma_f32_32x32x16_bf16 v[16:31], v[52:55], v[48:51], 0\n\t" \
    "ds_read_b128 v[52:55], v67 offset:64512\n\t" \
    "v_exp_f32 v38, v38\n\t" \
    "v_exp_f32 v39, v39\n\t" \
    "v_exp_f32 v40, v40\n\t" \
    "v_exp_f32 v41, v41\n\t" \
    "s_waitcnt lgkmcnt(2)\n\t" \
    "v_smfmac_f32_32x32x32_bf16 v[0:15], v[56:59], v[32:39], v64\n\t" \
    "ds_read_b128 v[56:59], v66 offset:34560\n\t" \
    "v_exp_f32 v42, v42\n\t" \
    "v_exp_f32 v43, v43\n\t" \
    "v_exp_f32 v44, v44\n\t" \
    "v_exp_f32 v45, v45\n\t" \
    "v_exp_f32 v46, v46\n\t" \
    "v_exp_f32 v47, v47\n\t" \
    "s_setprio 1\n\t" \
    "v_exp_f32 v16, v16\n\t" \
    "v_exp_f32 v17, v17\n\t" \
    "s_waitcnt lgkmcnt(2)\n\t" \
    "v_smfmac_f32_32x32x32_bf16 v[0:15], v[60:63], v[40:47], v64\n\t" \
    "ds_read_b128 v[60:63], v66 offset:35136\n\t" \
    "v_exp_f32 v18, v18\n\t" \
    "v_exp_f32 v19, v19\n\t" \
    "s_cmp_lg_u32 s43, 7\n\t" \
    "s_cbranch_scc1 .Ljd_%=\n\t" \
    "s_waitcnt vmcnt(1)\n\t" \
    "s_mov_b32 s46, 200000\n\t" \
    ".Ljp_%=:\n\t" \
    "v_cmp_eq_u32 vcc, 0, v69\n\t" \
    "s_cbranch_vccz .Ljd_%=\n\t" \
    "s_sub_u32 s46, s46, 1\n\t" \
    "s_cbranch_scc1 .Ljd_%=\n\t" \
    "s_sleep 2\n\t" \
    "global_load_dword v69, v71, s[96:97] sc1\n\t" \
    "s_waitcnt vmcnt(0)\n\t" \
    "s_branch .Ljp_%=\n\t" \
    ".Ljd_%=:\n\t" \
    "global_load_dwordx4 v[68:71], v68, s[60:61]\n\t" \
    "v_exp_f32 v20, v20\n\t" \
    "v_exp_f32 v21, v21\n\t" \
    "s_waitcnt lgkmcnt(2)\n\t" \
    "v_mfma_f32_32x32x16_bf16 v[32:47], v[52:55], v[48:51], 0\n\t" \
    "global_load_dwordx4 v[48:51], v67, s[54:55]\n\t" \
    "global_load_dwordx4 v[52:55], v67, s[54:55] offset:1024\n\t" \
    "v_exp_f32 v22, v22\n\t" \
    "v_exp_f32 v23, v23\n\t" \
    "v_exp_f32 v24, v24\n\t" \
    "v_exp_f32 v25, v25\n\t" \
    "s_waitcnt lgkmcnt(1)\n\t" \
    "v_smfmac_f32_32x32x32_bf16 v[0:15], v[56:59], v[16:23], v64\n\t" \
    "ds_read_b128 v[56:59], v66 offset:35712\n\t" \
    "global_load_dwordx4 v[16:19], v67, s[54:55] offset:2048\n\t" \
    "global_load_dwordx4 v[20:23], v67, s[54:55] offset:3072\n\t" \
    "v_exp_f32 v26, v26\n\t" \
    "v_exp_f32 v27, v27\n\t" \
    "v_exp_f32 v28, v28\n\t" \
    "v_exp_f32 v29, v29\n\t" \
    "v_exp_f32 v30, v30\n\t" \
    "v_exp_f32 v31, v31\n\t" \
    "s_setprio 0\n\t" \
    "v_exp_f32 v32, v32\n\t" \
    "v_exp_f32 v33, v33\n\t" \
    "s_waitcnt lgkmcnt(1)\n\t" \
    "v_smfmac_f32_32x32x32_bf16 v[0:15], v[60:63], v[24:31], v64\n\t" \
    "ds_read_b128 v[60:63], v66 offset:36288\n\t" \
    "v_exp_f32 v34, v34\n\t" \
    "v_exp_f32 v35, v35\n\t" \
    "v_exp_f32 v36, v36\n\t" \
    "v_exp_f32 v37, v37\n\t" \
    "v_exp_f32 v38, v38\n\t" \
    "v_exp_f32 v39, v39\n\t" \
    "v_exp_f32 v40, v40\n\t" \
    "v_exp_f32 v41, v41\n\t" \
    "s_waitcnt lgkmcnt(1)\n\t" \
    "v_smfmac_f32_32x32x32_bf16 v[0:15], v[56:59], v[32:39], v64\n\t" \
    "v_exp_f32 v42, v42\n\t" \
    "v_exp_f32 v43, v43\n\t" \
    "v_exp_f32 v44, v44\n\t" \
    "v_exp_f32 v45, v45\n\t" \
    "v_exp_f32 v46, v46\n\t" \
    "v_exp_f32 v47, v47\n\t" \
    "s_setprio 0\n\t" \
    "s_waitcnt lgkmcnt(0)\n\t" \
    "s_nop 0\n\t" \
    "v_smfmac_f32_32x32x32_bf16 v[0:15], v[60:63], v[40:47], v64\n\t" \
    "v_bfe_i32 v65, v67, 7, 1\n\t" \
    "v_and_b32 v65, 0xaaaaaaaa, v65\n\t" \
    "v_or_b32 v65, 0x44444444, v65\n\t" \
    "v_and_b32 v32, 0x70, v67\n\t" \
    "v_and_b32 v33, 0x180, v67\n\t" \
    "v_lshl_or_b32 v32, v33, 1, v32\n\t" \
    "v_lshrrev_b32 v33, 9, v67\n\t" \
    "v_lshl_or_b32 v32, v33, 7, v32\n\t" \
    "s_lshl_b32 s46, s43, 10\n\t" \
    "s_add_u32 s46, s46, 155648\n\t" \
    "v_add_u32 v32, s46, v32\n\t" \
    "v_add_u32 v66, 155648, v67\n\t" \
    "s_lshl_b32 s46, s43, 11\n\t" \
    "s_add_u32 s46, s46, 139264\n\t" \
    "v_add_u32 v64, s46, v67\n\t" \
    "s_waitcnt vmcnt(5)\n\t" \
    "ds_read_b128 v[56:59], v64 offset:0\n\t" \
    "ds_read_b128 v[60:63], v64 offset:1024\n\t" \
    "v_add_u32 v64, s47, v67\n\t" \
    "ds_read_b128 v[24:27], v64 offset:0\n\t" \
    "ds_read_b128 v[28:31], v64 offset:1024\n\t" \
    "v_mov_b32 v34, v8\n\t" \
    "s_nop 1\n\t" \
    "v_permlane32_swap_b32 v34, v8\n\t" \
    "s_nop 1\n\t" \
    "v_rcp_f32 v34, v34\n\t" \
    "s_nop 0\n\t" \
    "v_mul_f32 v0, v0, v34\n\t" \
    "v_mul_f32 v1, v1, v34\n\t" \
    "v_mul_f32 v2, v2, v34\n\t" \
    "v_mul_f32 v3, v3, v34\n\t" \
    "v_mul_f32 v4, v4, v34\n\t" \
    "v_mul_f32 v5, v5, v34\n\t" \
    "v_mul_f32 v6, v6, v34\n\t" \
    "v_mul_f32 v7, v7, v34\n\t" \
    "v_cvt_pk_bf16_f32 v36, v0, v1\n\t" \
    "v_cvt_pk_bf16_f32 v37, v2, v3\n\t" \
    "v_cvt_pk_bf16_f32 v38, v4, v5\n\t" \
    "v_cvt_pk_bf16_f32 v39, v6, v7\n\t" \
    "ds_write_b128 v32, v[36:39]\n\t" \
    "v_mov_b32 v0, 0\n\t" \
    "v_mov_b32 v1, 0\n\t" \
    "v_mov_b32 v2, 0\n\t" \
    "v_mov_b32 v3, 0\n\t" \
    "v_mov_b32 v4, 0\n\t" \
    "v_mov_b32 v5, 0\n\t" \
    "v_mov_b32 v6, 0\n\t" \
    "v_mov_b32 v7, 0\n\t" \
    "v_mov_b32 v8, 0\n\t" \
    "v_mov_b32 v9, 0\n\t" \
    "v_mov_b32 v10, 0\n\t" \
    "v_mov_b32 v11, 0\n\t" \
    "v_mov_b32 v12, 0\n\t" \
    "v_mov_b32 v13, 0\n\t" \
    "v_mov_b32 v14, 0\n\t" \
    "v_mov_b32 v15, 0\n\t" \
    "s_waitcnt lgkmcnt(0)\n\t" \
    "s_barrier\n\t" \
    "ds_read_b128 v[32:35], v66 offset:0\n\t" \
    "ds_read_b128 v[36:39], v66 offset:1024\n\t" \
    "ds_read_b128 v[40:43], v66 offset:2048\n\t" \
    "ds_read_b128 v[44:47], v66 offset:3072\n\t" \
    "s_waitcnt lgkmcnt(3)\n\t" \
    "v_smfmac_f32_16x16x64_bf16 v[0:3], v[32:35], a[0:7], v65\n\t" \
    "v_smfmac_f32_16x16x64_bf16 v[4:7], v[32:35], a[8:15], v65\n\t" \
    "v_smfmac_f32_16x16x64_bf16 v[8:11], v[32:35], a[16:23], v65\n\t" \
    "v_smfmac_f32_16x16x64_bf16 v[12:15], v[32:35], v[56:63], v65\n\t" \
    "ds_read_b128 v[56:59], v64 offset:2048\n\t" \
    "ds_read_b128 v[60:63], v64 offset:3072\n\t" \
    "ds_read_b128 v[32:35], v66 offset:4096\n\t" \
    "s_waitcnt lgkmcnt(5)\n\t" \
    "v_smfmac_f32_16x16x64_bf16 v[0:3], v[36:39], a[24:31], v65\n\t" \
    "v_smfmac_f32_16x16x64_bf16 v[4:7], v[36:39], a[32:39], v65\n\t" \
    "v_smfmac_f32_16x16x64_bf16 v[8:11], v[36:39], a[40:47], v65\n\t" \
    "v_smfmac_f32_16x16x64_bf16 v[12:15], v[36:39], v[24:31], v65\n\t" \
    "ds_read_b128 v[24:27], v64 offset:4096\n\t" \
    "ds_read_b128 v[28:31], v64 offset:5120\n\t" \
    "ds_read_b128 v[36:39], v66 offset:5120\n\t" \
    "s_waitcnt lgkmcnt(7)\n\t" \
    "v_smfmac_f32_16x16x64_bf16 v[0:3], v[40:43], a[48:55], v65\n\t" \
    "v_smfmac_f32_16x16x64_bf16 v[4:7], v[40:43], a[56:63], v65\n\t" \
    "v_smfmac_f32_16x16x64_bf16 v[8:11], v[40:43], a[64:71], v65\n\t" \
    "s_waitcnt lgkmcnt(4)\n\t" \
    "v_smfmac_f32_16x16x64_bf16 v[12:15], v[40:43], v[56:63], v65\n\t" \
    "v_add_u32 v64, 0x10000, v64\n\t" \
    "ds_read_b128 v[56:59], v64 offset:0\n\t" \
    "ds_read_b128 v[60:63], v64 offset:1024\n\t" \
    "ds_read_b128 v[40:43], v66 offset:6144\n\t" \
    "v_smfmac_f32_16x16x64_bf16 v[0:3], v[44:47], a[72:79], v65\n\t" \
    "v_smfmac_f32_16x16x64_bf16 v[4:7], v[44:47], a[80:87], v65\n\t" \
    "v_smfmac_f32_16x16x64_bf16 v[8:11], v[44:47], a[88:95], v65\n\t" \
    "s_waitcnt lgkmcnt(4)\n\t" \
    "v_smfmac_f32_16x16x64_bf16 v[12:15], v[44:47], v[24:31], v65\n\t" \
    "ds_read_b128 v[24:27], v64 offset:2048\n\t" \
    "ds_read_b128 v[28:31], v64 offset:3072\n\t" \
    "ds_read_b128 v[44:47], v66 offset:7168\n\t" \
    "v_smfmac_f32_16x16x64_bf16 v[0:3], v[32:35], a[96:103], v65\n\t" \
    "v_smfmac_f32_16x16x64_bf16 v[4:7], v[32:35], a[104:111], v65\n\t" \
    "v_smfmac_f32_16x16x64_bf16 v[8:11], v[32:35], a[112:119], v65\n\t" \
    "s_waitcnt lgkmcnt(4)\n\t" \
    "v_smfmac_f32_16x16x64_bf16 v[12:15], v[32:35], v[56:63], v65\n\t" \
    "ds_read_b128 v[56:59], v64 offset:4096\n\t" \
    "ds_read_b128 v[60:63], v64 offset:5120\n\t" \
    "v_smfmac_f32_16x16x64_bf16 v[0:3], v[36:39], a[120:127], v65\n\t" \
    "v_smfmac_f32_16x16x64_bf16 v[4:7], v[36:39], a[128:135], v65\n\t" \
    "v_smfmac_f32_16x16x64_bf16 v[8:11], v[36:39], a[136:143], v65\n\t" \
    "s_waitcnt lgkmcnt(3)\n\t" \
    "v_smfmac_f32_16x16x64_bf16 v[12:15], v[36:39], v[24:31], v65\n\t" \
    "v_smfmac_f32_16x16x64_bf16 v[0:3], v[40:43], a[144:151], v65\n\t" \
    "v_smfmac_f32_16x16x64_bf16 v[4:7], v[40:43], a[152:159], v65\n\t" \
    "v_smfmac_f32_16x16x64_bf16 v[8:11], v[40:43], a[160:167], v65\n\t" \
    "s_waitcnt lgkmcnt(0)\n\t" \
    "v_smfmac_f32_16x16x64_bf16 v[12:15], v[40:43], v[56:63], v65\n\t" \
    "v_smfmac_f32_16x16x64_bf16 v[0:3], v[44:47], a[168:175], v65\n\t" \
    "v_smfmac_f32_16x16x64_bf16 v[4:7], v[44:47], a[176:183], v65\n\t" \
    "s_waitcnt vmcnt(0)\n\t" \
    "v_smfmac_f32_16x16x64_bf16 v[8:11], v[44:47], v[48:55], v65\n\t" \
    "v_smfmac_f32_16x16x64_bf16 v[12:15], v[44:47], v[16:23], v65\n\t" \
    "s_waitcnt lgkmcnt(0)\n\t" \
    "s_nop 7\n\t" \
    ""
#define K2_W0_ASM \
    "s_cmp_eq_u32 s43, 7\n\t" \
    "s_cbranch_scc1 .Lnw0_%=\n\t" \
    "global_load_dwordx4 a[0:3], v67, s[100:101] offset:-4096 sc1\n\t" \
    "global_load_dwordx4 a[4:7], v67, s[100:101] offset:-3072 sc1\n\t" \
    "global_load_dwordx4 a[8:11], v67, s[100:101] offset:-2048 sc1\n\t" \
    "global_load_dwordx4 a[12:15], v67, s[100:101] offset:-1024 sc1\n\t" \
    "global_load_dwordx4 a[16:19], v67, s[100:101] offset:0 sc1\n\t" \
    "global_load_dwordx4 a[20:23], v67, s[100:101] offset:1024 sc1\n\t" \
    "global_load_dwordx4 a[24:27], v67, s[100:101] offset:2048 sc1\n\t" \
    "global_load_dwordx4 a[28:31], v67, s[100:101] offset:3072 sc1\n\t" \
    ".Lnw0_%=:\n\t" \
    ""

#define KF_ASM \
    "v_bfe_u32 v65, v67, 4, 4\n\t" \
    "v_lshlrev_b32 v65, 7, v65\n\t" \
    "v_mov_b32 v64, 1\n\t" \
    "s_and_b32 s44, s43, 3\n\t" \
    "v_bfe_u32 v60, v67, 4, 3\n\t" \
    "v_min_u32 v60, 6, v60\n\t" \
    "v_bfe_u32 v61, v67, 7, 2\n\t" \
    "v_lshlrev_b32 v61, 10, v61\n\t" \
    "v_lshl_or_b32 v61, v60, 7, v61\n\t" \
    "s_lshl_b32 s44, s44, 12\n\t" \
    "v_add_u32 v61, s44, v61\n\t" \
    "s_and_b32 s44, s43, 1\n\t" \
    "s_lshl_b32 s44, s44, 8\n\t" \
    "v_xor_b32 v62, s44, v67\n\t" \
    "s_cmp_eq_u32 s43, 7\n\t" \
    "s_cbranch_scc1 .Lsvc_%=\n\t" \
    "s_add_u32 s40, s40, 12288\n\t" \
    "s_addc_u32 s41, s41, 0\n\t" \
    "global_load_dwordx4 a[32:35], v67, s[40:41] offset:-4096 sc1\n\t" \
    "global_load_dwordx4 a[36:39], v67, s[40:41] offset:-3072 sc1\n\t" \
    "global_load_dwordx4 a[40:43], v67, s[40:41] offset:-2048 sc1\n\t" \
    "global_load_dwordx4 a[44:47], v67, s[40:41] offset:-1024 sc1\n\t" \
    "global_load_dwordx4 a[48:51], v67, s[40:41] sc1\n\t" \
    "global_load_dwordx4 a[52:55], v67, s[40:41] offset:1024 sc1\n\t" \
    "global_load_dwordx4 a[56:59], v67, s[40:41] offset:2048 sc1\n\t" \
    "global_load_dwordx4 a[60:63], v67, s[40:41] offset:3072 sc1\n\t" \
    "s_add_u32 s40, s40, 8192\n\t" \
    "s_addc_u32 s41, s41, 0\n\t" \
    "global_load_dwordx4 a[64:67], v67, s[40:41] offset:-4096 sc1\n\t" \
    "global_load_dwordx4 a[68:71], v67, s[40:41] offset:-3072 sc1\n\t" \
    "s_waitcnt vmcnt(10)\n\t" \
    "s_barrier\n\t" \
    "s_sleep 1\n\t" \
    "global_load_dwordx4 a[72:75], v67, s[40:41] offset:-2048 sc1\n\t" \
    "global_load_dwordx4 a[76:79], v67, s[40:41] offset:-1024 sc1\n\t" \
    "global_load_dwordx4 a[80:83], v67, s[40:41] sc1\n\t" \
    "global_load_dwordx4 a[84:87], v67, s[40:41] offset:1024 sc1\n\t" \
    "global_load_dwordx4 a[88:91], v67, s[40:41] offset:2048 sc1\n\t" \
    "global_load_dwordx4 a[92:95], v67, s[40:41] offset:3072 sc1\n\t" \
    "s_add_u32 s40, s40, 8192\n\t" \
    "s_addc_u32 s41, s41, 0\n\t" \
    "global_load_dwordx4 a[96:99], v67, s[40:41] offset:-4096 sc1\n\t" \
    "global_load_dwordx4 a[100:103], v67, s[40:41] offset:-3072 sc1\n\t" \
    "global_load_dwordx4 a[104:107], v67, s[40:41] offset:-2048 sc1\n\t" \
    "global_load_dwordx4 a[108:111], v67, s[40:41] offset:-1024 sc1\n\t" \
    "global_load_dwordx4 a[112:115], v67, s[40:41] sc1\n\t" \
    "global_load_dwordx4 a[116:119], v67, s[40:41] offset:1024 sc1\n\t" \
    "s_barrier\n\t" \
    "global_load_dwordx4 a[120:123], v67, s[40:41] offset:2048 sc1\n\t" \
    "global_load_dwordx4 a[124:127], v67, s[40:41] offset:3072 sc1\n\t" \
    "v_mov_b32 v63, 0xbdd2d3e8\n\t" \
    "v_add_u32 v66, 0x10000, v67\n\t" \
    "s_barrier\n\t" \
    "ds_read_b128 v[32:35], v67 offset:0\n\t" \
    "ds_read_b128 v[36:39], v67 offset:1024\n\t" \
    "ds_read_b128 v[40:43], v67 offset:2048\n\t" \
    "ds_read_b128 v[44:47], v67 offset:3072\n\t" \
    "s_waitcnt lgkmcnt(3)\n\t" \
    "v_mfma_f32_16x16x32_bf16 v[0:3], a[0:3], v[32:35], 0\n\t" \
    "v_mfma_f32_16x16x32_bf16 v[8:11], a[4:7], v[32:35], 0\n\t" \
    "ds_read_b128 v[32:35], v67 offset:4096\n\t" \
    "s_waitcnt lgkmcnt(3)\n\t" \
    "v_mfma_f32_16x16x32_bf16 v[4:7], a[0:3], v[36:39], 0\n\t" \
    "v_mfma_f32_16x16x32_bf16 v[12:15], a[4:7], v[36:39], 0\n\t" \
    "ds_read_b128 v[36:39], v67 offset:5120\n\t" \
    "s_waitcnt lgkmcnt(3)\n\t" \
    "v_mfma_f32_16x16x32_bf16 v[0:3], a[8:11], v[40:43], v[0:3]\n\t" \
    "v_mfma_f32_16x16x32_bf16 v[8:11], a[12:15], v[40:43], v[8:11]\n\t" \
    "ds_read_b128 v[40:43], v67 offset:6144\n\t" \
    "s_waitcnt lgkmcnt(3)\n\t" \
    "v_mfma_f32_16x16x32_bf16 v[4:7], a[8:11], v[44:47], v[4:7]\n\t" \
    "v_mfma_f32_16x16x32_bf16 v[12:15], a[12:15], v[44:47], v[12:15]\n\t" \
    "ds_read_b128 v[44:47], v67 offset:7168\n\t" \
    "s_waitcnt lgkmcnt(3)\n\t" \
    "v_mfma_f32_16x16x32_bf16 v[0:3], a[16:19], v[32:35], v[0:3]\n\t" \
    "v_mfma_f32_16x16x32_bf16 v[8:11], a[20:23], v[32:35], v[8:11]\n\t" \
    "ds_read_b128 v[32:35], v67 offset:8192\n\t" \
    "s_waitcnt lgkmcnt(3)\n\t" \
    "v_mfma_f32_16x16x32_bf16 v[4:7], a[16:19], v[36:39], v[4:7]\n\t" \
    "v_mfma_f32_16x16x32_bf16 v[12:15], a[20:23], v[36:39], v[12:15]\n\t" \
    "ds_read_b128 v[36:39], v67 offset:9216\n\t" \
    "s_waitcnt lgkmcnt(3)\n\t" \
    "v_mfma_f32_16x16x32_bf16 v[0:3], a[24:27], v[40:43], v[0:3]\n\t" \
    "v_mfma_f32_16x16x32_bf16 v[8:11], a[28:31], v[40:43], v[8:11]\n\t" \
    "ds_read_b128 v[40:43], v67 offset:10240\n\t" \
    "s_waitcnt lgkmcnt(3)\n\t" \
    "v_mfma_f32_16x16x32_bf16 v[4:7], a[24:27], v[44:47], v[4:7]\n\t" \
    "v_mfma_f32_16x16x32_bf16 v[12:15], a[28:31], v[44:47], v[12:15]\n\t" \
    "ds_read_b128 v[44:47], v67 offset:11264\n\t" \
    "s_waitcnt vmcnt(22) lgkmcnt(3)\n\t" \
    "v_mfma_f32_16x16x32_bf16 v[0:3], a[32:35], v[32:35], v[0:3]\n\t" \
    "v_mfma_f32_16x16x32_bf16 v[8:11], a[36:39], v[32:35], v[8:11]\n\t" \
    "ds_read_b128 v[32:35], v67 offset:12288\n\t" \
    "s_waitcnt lgkmcnt(3)\n\t" \
    "v_mfma_f32_16x16x32_bf16 v[4:7], a[32:35], v[36:39], v[4:7]\n\t" \
    "v_mfma_f32_16x16x32_bf16 v[12:15], a[36:39], v[36:39], v[12:15]\n\t" \
    "ds_read_b128 v[36:39], v67 offset:13312\n\t" \
    "s_waitcnt vmcnt(20) lgkmcnt(3)\n\t" \
    "v_mfma_f32_16x16x32_bf16 v[0:3], a[40:43], v[40:43], v[0:3]\n\t" \
    "v_mfma_f32_16x16x32_bf16 v[8:11], a[44:47], v[40:43], v[8:11]\n\t" \
    "ds_read_b128 v[40:43], v67 offset:14336\n\t" \
    "s_waitcnt lgkmcnt(3)\n\t" \
    "v_mfma_f32_16x16x32_bf16 v[4:7], a[40:43], v[44:47], v[4:7]\n\t" \
    "v_mfma_f32_16x16x32_bf16 v[12:15], a[44:47], v[44:47], v[12:15]\n\t" \
    "ds_read_b128 v[44:47], v67 offset:15360\n\t" \
    "s_waitcnt vmcnt(18) lgkmcnt(3)\n\t" \
    "v_mfma_f32_16x16x32_bf16 v[0:3], a[48:51], v[32:35], v[0:3]\n\t" \
    "v_mfma_f32_16x16x32_bf16 v[8:11], a[52:55], v[32:35], v[8:11]\n\t" \
    "ds_read_b128 v[32:35], v67 offset:16384\n\t" \
    "s_waitcnt lgkmcnt(3)\n\t" \
    "v_mfma_f32_16x16x32_bf16 v[4:7], a[48:51], v[36:39], v[4:7]\n\t" \
    "v_mfma_f32_16x16x32_bf16 v[12:15], a[52:55], v[36:39], v[12:15]\n\t" \
    "ds_read_b128 v[36:39], v67 offset:17408\n\t" \
    "s_waitcnt vmcnt(16) lgkmcnt(3)\n\t" \
    "v_mfma_f32_16x16x32_bf16 v[0:3], a[56:59], v[40:43], v[0:3]\n\t" \
    "v_mfma_f32_16x16x32_bf16 v[8:11], a[60:63], v[40:43], v[8:11]\n\t" \
    "ds_read_b128 v[40:43], v67 offset:18432\n\t" \
    "s_waitcnt lgkmcnt(3)\n\t" \
    "v_mfma_f32_16x16x32_bf16 v[4:7], a[56:59], v[44:47], v[4:7]\n\t" \
    "v_mfma_f32_16x16x32_bf16 v[12:15], a[60:63], v[44:47], v[12:15]\n\t" \
    "ds_read_b128 v[44:47], v67 offset:19456\n\t" \
    "s_waitcnt vmcnt(14) lgkmcnt(3)\n\t" \
    "v_mfma_f32_16x16x32_bf16 v[0:3], a[64:67], v[32:35], v[0:3]\n\t" \
    "v_mfma_f32_16x16x32_bf16 v[8:11], a[68:71], v[32:35], v[8:11]\n\t" \
    "ds_read_b128 v[32:35], v67 offset:20480\n\t" \
    "s_waitcnt lgkmcnt(3)\n\t" \
    "v_mfma_f32_16x16x32_bf16 v[4:7], a[64:67], v[36:39], v[4:7]\n\t" \
    "v_mfma_f32_16x16x32_bf16 v[12:15], a[68:71], v[36:39], v[12:15]\n\t" \
    "ds_read_b128 v[36:39], v67 offset:21504\n\t" \
    "s_waitcnt vmcnt(12) lgkmcnt(3)\n\t" \
    "v_mfma_f32_16x16x32_bf16 v[0:3], a[72:75], v[40:43], v[0:3]\n\t" \
    "v_mfma_f32_16x16x32_bf16 v[8:11], a[76:79], v[40:43], v[8:11]\n\t" \
    "ds_read_b128 v[40:43], v67 offset:22528\n\t" \
    "s_waitcnt lgkmcnt(3)\n\t" \
    "v_mfma_f32_16x16x32_bf16 v[4:7], a[72:75], v[44:47], v[4:7]\n\t" \
    "v_mfma_f32_16x16x32_bf16 v[12:15], a[76:79], v[44:47], v[12:15]\n\t" \
    "ds_read_b128 v[44:47], v67 offset:23552\n\t" \
    "s_waitcnt vmcnt(10) lgkmcnt(3)\n\t" \
    "v_mfma_f32_16x16x32_bf16 v[0:3], a[80:83], v[32:35], v[0:3]\n\t" \
    "v_mfma_f32_16x16x32_bf16 v[8:11], a[84:87], v[32:35], v[8:11]\n\t" \
    "ds_read_b128 v[32:35], v67 offset:24576\n\t" \
    "s_waitcnt lgkmcnt(3)\n\t" \
    "v_mfma_f32_16x16x32_bf16 v[4:7], a[80:83], v[36:39], v[4:7]\n\t" \
    "v_mfma_f32_16x16x32_bf16 v[12:15], a[84:87], v[36:39], v[12:15]\n\t" \
    "ds_read_b128 v[36:39], v67 offset:25600\n\t" \
    "s_waitcnt vmcnt(8) lgkmcnt(3)\n\t" \
    "v_mfma_f32_16x16x32_bf16 v[0:3], a[88:91], v[40:43], v[0:3]\n\t" \
    "v_mfma_f32_16x16x32_bf16 v[8:11], a[92:95], v[40:43], v[8:11]\n\t" \
    "ds_read_b128 v[40:43], v67 offset:26624\n\t" \
    "s_waitcnt lgkmcnt(3)\n\t" \
    "v_mfma_f32_16x16x32_bf16 v[4:7], a[88:91], v[44:47], v[4:7]\n\t" \
    "v_mfma_f32_16x16x32_bf16 v[12:15], a[92:95], v[44:47], v[12:15]\n\t" \
    "ds_read_b128 v[44:47], v67 offset:27648\n\t" \
    "s_waitcnt vmcnt(6) lgkmcnt(3)\n\t" \
    "v_mfma_f32_16x16x32_bf16 v[0:3], a[96:99], v[32:35], v[0:3]\n\t" \
    "v_mfma_f32_16x16x32_bf16 v[8:11], a[100:103], v[32:35], v[8:11]\n\t" \
    "ds_read_b128 v[32:35], v67 offset:28672\n\t" \
    "s_waitcnt lgkmcnt(3)\n\t" \
    "v_mfma_f32_16x16x32_bf16 v[4:7], a[96:99], v[36:39], v[4:7]\n\t" \
    "v_mfma_f32_16x16x32_bf16 v[12:15], a[100:103], v[36:39], v[12:15]\n\t" \
    "ds_read_b128 v[36:39], v67 offset:29696\n\t" \
    "s_waitcnt vmcnt(4) lgkmcnt(3)\n\t" \
    "v_mfma_f32_16x16x32_bf16 v[0:3], a[104:107], v[40:43], v[0:3]\n\t" \
    "v_mfma_f32_16x16x32_bf16 v[8:11], a[108:111], v[40:43], v[8:11]\n\t" \
    "ds_read_b128 v[40:43], v67 offset:30720\n\t" \
    "s_waitcnt lgkmcnt(3)\n\t" \
    "v_mfma_f32_16x16x32_bf16 v[4:7], a[104:107], v[44:47], v[4:7]\n\t" \
    "v_mfma_f32_16x16x32_bf16 v[12:15], a[108:111], v[44:47], v[12:15]\n\t" \
    "ds_read_b128 v[44:47], v67 offset:31744\n\t" \
    "s_waitcnt vmcnt(2) lgkmcnt(3)\n\t" \
    "v_mfma_f32_16x16x32_bf16 v[0:3], a[112:115], v[32:35], v[0:3]\n\t" \
    "v_mfma_f32_16x16x32_bf16 v[8:11], a[116:119], v[32:35], v[8:11]\n\t" \
    "s_barrier\n\t" \
    "ds_read_b128 v[32:35], v67 offset:32768\n\t" \
    "s_waitcnt lgkmcnt(3)\n\t" \
    "v_mfma_f32_16x16x32_bf16 v[4:7], a[112:115], v[36:39], v[4:7]\n\t" \
    "v_mfma_f32_16x16x32_bf16 v[12:15], a[116:119], v[36:39], v[12:15]\n\t" \
    "ds_read_b128 v[36:39], v67 offset:33792\n\t" \
    "s_waitcnt vmcnt(0) lgkmcnt(3)\n\t" \
    "v_mfma_f32_16x16x32_bf16 v[0:3], a[120:123], v[40:43], v[0:3]\n\t" \
    "v_mfma_f32_16x16x32_bf16 v[8:11], a[124:127], v[40:43], v[8:11]\n\t" \
    "ds_read_b128 v[40:43], v67 offset:34816\n\t" \
    "s_waitcnt lgkmcnt(3)\n\t" \
    "v_mfma_f32_16x16x32_bf16 v[4:7], a[120:123], v[44:47], v[4:7]\n\t" \
    "v_mfma_f32_16x16x32_bf16 v[12:15], a[124:127], v[44:47], v[12:15]\n\t" \
    "ds_read_b128 v[44:47], v67 offset:35840\n\t" \
    "s_waitcnt lgkmcnt(3)\n\t" \
    "v_mfma_f32_16x16x32_bf16 v[16:19], a[0:3], v[32:35], 0\n\t" \
    "v_mfma_f32_16x16x32_bf16 v[24:27], a[4:7], v[32:35], 0\n\t" \
    "ds_read_b128 v[32:35], v67 offset:36864\n\t" \
    "s_waitcnt lgkmcnt(3)\n\t" \
    "v_mfma_f32_16x16x32_bf16 v[20:23], a[0:3], v[36:39], 0\n\t" \
    "v_mfma_f32_16x16x32_bf16 v[28:31], a[4:7], v[36:39], 0\n\t" \
    "ds_read_b128 v[36:39], v67 offset:37888\n\t" \
    "s_waitcnt lgkmcnt(3)\n\t" \
    "v_mfma_f32_16x16x32_bf16 v[16:19], a[8:11], v[40:43], v[16:19]\n\t" \
    "v_mfma_f32_16x16x32_bf16 v[24:27], a[12:15], v[40:43], v[24:27]\n\t" \
    "ds_read_b128 v[40:43], v67 offset:38912\n\t" \
    "s_waitcnt lgkmcnt(3)\n\t" \
    "v_mfma_f32_16x16x32_bf16 v[20:23], a[8:11], v[44:47], v[20:23]\n\t" \
    "v_mul_f32 v48, v0, v0\n\t" \
    "v_mul_f32 v8, v0, v8\n\t" \
    "v_mfma_f32_16x16x32_bf16 v[28:31], a[12:15], v[44:47], v[28:31]\n\t" \
    "ds_read_b128 v[44:47], v67 offset:39936\n\t" \
    "v_fmaak_f32 v48, v63, v48, 0xc0135761\n\t" \
    "s_waitcnt lgkmcnt(3)\n\t" \
    "v_mfma_f32_16x16x32_bf16 v[16:19], a[16:19], v[32:35], v[16:19]\n\t" \
    "v_mul_f32 v0, v0, v48\n\t" \
    "v_mfma_f32_16x16x32_bf16 v[24:27], a[20:23], v[32:35], v[24:27]\n\t" \
    "ds_read_b128 v[32:35], v67 offset:40960\n\t" \
    "v_exp_f32 v0, v0\n\t" \
    "v_mul_f32 v49, v1, v1\n\t" \
    "global_load_dwordx4 a[128:131], v62, s[62:63] sc1\n\t" \
    "s_waitcnt lgkmcnt(3)\n\t" \
    "v_mfma_f32_16x16x32_bf16 v[20:23], a[16:19], v[36:39], v[20:23]\n\t" \
    "v_fmaak_f32 v49, v63, v49, 0xc0135761\n\t" \
    "v_mfma_f32_16x16x32_bf16 v[28:31], a[20:23], v[36:39], v[28:31]\n\t" \
    "ds_read_b128 v[36:39], v67 offset:41984\n\t" \
    "v_add_f32 v0, 1.0, v0\n\t" \
    "v_mul_f32 v9, v1, v9\n\t" \
    "s_waitcnt lgkmcnt(3)\n\t" \
    "v_mfma_f32_16x16x32_bf16 v[16:19], a[24:27], v[40:43], v[16:19]\n\t" \
    "v_rcp_f32 v0, v0\n\t" \
    "v_mfma_f32_16x16x32_bf16 v[24:27], a[28:31], v[40:43], v[24:27]\n\t" \
    "ds_read_b128 v[40:43], v67 offset:43008\n\t" \
    "v_mul_f32 v1, v1, v49\n\t" \
    "s_waitcnt lgkmcnt(3)\n\t" \
    "v_mfma_f32_16x16x32_bf16 v[20:23], a[24:27], v[44:47], v[20:23]\n\t" \
    "v_mul_f32 v8, v8, v0\n\t" \
    "v_mfma_f32_16x16x32_bf16 v[28:31], a[28:31], v[44:47], v[28:31]\n\t" \
    "ds_read_b128 v[44:47], v67 offset:44032\n\t" \
    "v_exp_f32 v1, v1\n\t" \
    "v_mul_f32 v48, v2, v2\n\t" \
    "s_waitcnt lgkmcnt(3)\n\t" \
    "v_mfma_f32_16x16x32_bf16 v[16:19], a[32:35], v[32:35], v[16:19]\n\t" \
    "v_fmaak_f32 v48, v63, v48, 0xc0135761\n\t" \
    "v_mfma_f32_16x16x32_bf16 v[24:27], a[36:39], v[32:35], v[24:27]\n\t" \
    "ds_read_b128 v[32:35], v67 offset:45056\n\t" \
    "v_add_f32 v1, 1.0, v1\n\t" \
    "v_mul_f32 v10, v2, v10\n\t" \
    "global_load_dwordx4 a[132:135], v62, s[64:65] sc1\n\t" \
    "s_waitcnt lgkmcnt(3)\n\t" \
    "v_mfma_f32_16x16x32_bf16 v[20:23], a[32:35], v[36:39], v[20:23]\n\t" \
    "v_rcp_f32 v1, v1\n\t" \
    "v_mfma_f32_16x16x32_bf16 v[28:31], a[36:39], v[36:39], v[28:31]\n\t" \
    "ds_read_b128 v[36:39], v67 offset:46080\n\t" \
    "v_mul_f32 v2, v2, v48\n\t" \
    "s_waitcnt lgkmcnt(3)\n\t" \
    "v_mfma_f32_16x16x32_bf16 v[16:19], a[40:43], v[40:43], v[16:19]\n\t" \
    "v_mul_f32 v9, v9, v1\n\t" \
    "v_cvt_pk_bf16_f32 v52, v8, v9\n\t" \
    "v_mfma_f32_16x16x32_bf16 v[24:27], a[44:47], v[40:43], v[24:27]\n\t" \
    "ds_read_b128 v[40:43], v67 offset:47104\n\t" \
    "v_exp_f32 v2, v2\n\t" \
    "s_waitcnt lgkmcnt(3)\n\t" \
    "v_mfma_f32_16x16x32_bf16 v[20:23], a[40:43], v[44:47], v[20:23]\n\t" \
    "v_mul_f32 v49, v3, v3\n\t" \
    "v_mfma_f32_16x16x32_bf16 v[28:31], a[44:47], v[44:47], v[28:31]\n\t" \
    "ds_read_b128 v[44:47], v67 offset:48128\n\t" \
    "v_fmaak_f32 v49, v63, v49, 0xc0135761\n\t" \
    "v_add_f32 v2, 1.0, v2\n\t" \
    "s_waitcnt lgkmcnt(3)\n\t" \
    "v_mfma_f32_16x16x32_bf16 v[16:19], a[48:51], v[32:35], v[16:19]\n\t" \
    "v_mul_f32 v11, v3, v11\n\t" \
    "v_mfma_f32_16x16x32_bf16 v[24:27], a[52:55], v[32:35], v[24:27]\n\t" \
    "ds_read_b128 v[32:35], v67 offset:49152\n\t" \
    "v_rcp_f32 v2, v2\n\t" \
    "global_load_dwordx4 a[136:139], v62, s[62:63] offset:1024 sc1\n\t" \
    "s_waitcnt lgkmcnt(3)\n\t" \
    "v_mfma_f32_16x16x32_bf16 v[20:23], a[48:51], v[36:39], v[20:23]\n\t" \
    "v_mul_f32 v3, v3, v49\n\t" \
    "v_mul_f32 v10, v10, v2\n\t" \
    "v_mfma_f32_16x16x32_bf16 v[28:31], a[52:55], v[36:39], v[28:31]\n\t" \
    "ds_read_b128 v[36:39], v67 offset:50176\n\t" \
    "v_exp_f32 v3, v3\n\t" \
    "s_waitcnt lgkmcnt(3)\n\t" \
    "v_mfma_f32_16x16x32_bf16 v[16:19], a[56:59], v[40:43], v[16:19]\n\t" \
    "v_mul_f32 v48, v4, v4\n\t" \
    "v_mfma_f32_16x16x32_bf16 v[24:27], a[60:63], v[40:43], v[24:27]\n\t" \
    "ds_read_b128 v[40:43], v67 offset:51200\n\t" \
    "v_fmaak_f32 v48, v63, v48, 0xc0135761\n\t" \
    "v_add_f32 v3, 1.0, v3\n\t" \
    "s_waitcnt lgkmcnt(3)\n\t" \
    "v_mfma_f32_16x16x32_bf16 v[20:23], a[56:59], v[44:47], v[20:23]\n\t" \
    "v_mul_f32 v12, v4, v12\n\t" \
    "v_mfma_f32_16x16x32_bf16 v[28:31], a[60:63], v[44:47], v[28:31]\n\t" \
    "ds_read_b128 v[44:47], v67 offset:52224\n\t" \
    "v_rcp_f32 v3, v3\n\t" \
    "s_waitcnt lgkmcnt(3)\n\t" \
    "v_mfma_f32_16x16x32_bf16 v[16:19], a[64:67], v[32:35], v[16:19]\n\t" \
    "v_mul_f32 v4, v4, v48\n\t" \
    "v_mul_f32 v11, v11, v3\n\t" \
    "v_mfma_f32_16x16x32_bf16 v[24:27], a[68:71], v[32:35], v[24:27]\n\t" \
    "ds_read_b128 v[32:35], v67 offset:53248\n\t" \
    "v_cvt_pk_bf16_f32 v53, v10, v11\n\t" \
    "global_load_dwordx4 a[140:143], v62, s[64:65] offset:1024 sc1\n\t" \
    "s_waitcnt lgkmcnt(3)\n\t" \
    "v_mfma_f32_16x16x32_bf16 v[20:23], a[64:67], v[36:39], v[20:23]\n\t" \
    "v_exp_f32 v4, v4\n\t" \
    "v_mfma_f32_16x16x32_bf16 v[28:31], a[68:71], v[36:39], v[28:31]\n\t" \
    "ds_read_b128 v[36:39], v67 offset:54272\n\t" \
    "v_mul_f32 v49, v5, v5\n\t" \
    "v_fmaak_f32 v49, v63, v49, 0xc0135761\n\t" \
    "s_waitcnt lgkmcnt(3)\n\t" \
    "v_mfma_f32_16x16x32_bf16 v[16:19], a[72:75], v[40:43], v[16:19]\n\t" \
    "v_add_f32 v4, 1.0, v4\n\t" \
    "v_mfma_f32_16x16x32_bf16 v[24:27], a[76:79], v[40:43], v[24:27]\n\t" \
    "ds_read_b128 v[40:43], v67 offset:55296\n\t" \
    "v_mul_f32 v13, v5, v13\n\t" \
    "s_waitcnt lgkmcnt(3)\n\t" \
    "v_mfma_f32_16x16x32_bf16 v[20:23], a[72:75], v[44:47], v[20:23]\n\t" \
    "v_rcp_f32 v4, v4\n\t" \
    "v_mul_f32 v5, v5, v49\n\t" \
    "v_mfma_f32_16x16x32_bf16 v[28:31], a[76:79], v[44:47], v[28:31]\n\t" \
    "ds_read_b128 v[44:47], v67 offset:56320\n\t" \
    "v_mul_f32 v12, v12, v4\n\t" \
    "s_waitcnt lgkmcnt(3)\n\t" \
    "v_mfma_f32_16x16x32_bf16 v[16:19], a[80:83], v[32:35], v[16:19]\n\t" \
    "v_exp_f32 v5, v5\n\t" \
    "v_mfma_f32_16x16x32_bf16 v[24:27], a[84:87], v[32:35], v[24:27]\n\t" \
    "ds_read_b128 v[32:35], v67 offset:57344\n\t" \
    "v_mul_f32 v48, v6, v6\n\t" \
    "v_fmaak_f32 v48, v63, v48, 0xc0135761\n\t" \
    "global_load_dwordx4 a[144:147], v62, s[62:63] offset:2048 sc1\n\t" \
    "s_waitcnt lgkmcnt(3)\n\t" \
    "v_mfma_f32_16x16x32_bf16 v[20:23], a[80:83], v[36:39], v[20:23]\n\t" \
    "v_add_f32 v5, 1.0, v5\n\t" \
    "v_mfma_f32_16x16x32_bf16 v[28:31], a[84:87], v[36:39], v[28:31]\n\t" \
    "ds_read_b128 v[36:39], v67 offset:58368\n\t" \
    "v_mul_f32 v14, v6, v14\n\t" \
    "s_waitcnt lgkmcnt(3)\n\t" \
    "v_mfma_f32_16x16x32_bf16 v[16:19], a[88:91], v[40:43], v[16:19]\n\t" \
    "v_rcp_f32 v5, v5\n\t" \
    "v_mul_f32 v6, v6, v48\n\t" \
    "v_mfma_f32_16x16x32_bf16 v[24:27], a[92:95], v[40:43], v[24:27]\n\t" \
    "ds_read_b128 v[40:43], v67 offset:59392\n\t" \
    "v_mul_f32 v13, v13, v5\n\t" \
    "s_waitcnt lgkmcnt(3)\n\t" \
    "v_mfma_f32_16x16x32_bf16 v[20:23], a[88:91], v[44:47], v[20:23]\n\t" \
    "v_exp_f32 v6, v6\n\t" \
    "v_mfma_f32_16x16x32_bf16 v[28:31], a[92:95], v[44:47], v[28:31]\n\t" \
    "ds_read_b128 v[44:47], v67 offset:60416\n\t" \
    "v_mul_f32 v49, v7, v7\n\t" \
    "s_waitcnt lgkmcnt(3)\n\t" \
    "v_mfma_f32_16x16x32_bf16 v[16:19], a[96:99], v[32:35], v[16:19]\n\t" \
    "v_fmaak_f32 v49, v63, v49, 0xc0135761\n\t" \
    "v_add_f32 v6, 1.0, v6\n\t" \
    "v_mfma_f32_16x16x32_bf16 v[24:27], a[100:103], v[32:35], v[24:27]\n\t" \
    "ds_read_b128 v[32:35], v67 offset:61440\n\t" \
    "v_mul_f32 v15, v7, v15\n\t" \
    "global_load_dwordx4 a[148:151], v62, s[64:65] offset:2048 sc1\n\t" \
    "s_waitcnt lgkmcnt(3)\n\t" \
    "v_mfma_f32_16x16x32_bf16 v[20:23], a[96:99], v[36:39], v[20:23]\n\t" \
    "v_rcp_f32 v6, v6\n\t" \
    "v_mfma_f32_16x16x32_bf16 v[28:31], a[100:103], v[36:39], v[28:31]\n\t" \
    "ds_read_b128 v[36:39], v67 offset:62464\n\t" \
    "v_mul_f32 v7, v7, v49\n\t" \
    "v_mul_f32 v14, v14, v6\n\t" \
    "s_waitcnt lgkmcnt(3)\n\t" \
    "v_mfma_f32_16x16x32_bf16 v[16:19], a[104:107], v[40:43], v[16:19]\n\t" \
    "v_exp_f32 v7, v7\n\t" \
    "v_mfma_f32_16x16x32_bf16 v[24:27], a[108:111], v[40:43], v[24:27]\n\t" \
    "ds_read_b128 v[40:43], v67 offset:63488\n\t" \
    "v_cvt_pk_bf16_f32 v54, v12, v13\n\t" \
    "s_waitcnt lgkmcnt(3)\n\t" \
    "v_mfma_f32_16x16x32_bf16 v[20:23], a[104:107], v[44:47], v[20:23]\n\t" \
    "v_add_f32 v7, 1.0, v7\n\t" \
    "v_rcp_f32 v7, v7\n\t" \
    "v_mfma_f32_16x16x32_bf16 v[28:31], a[108:111], v[44:47], v[28:31]\n\t" \
    "ds_read_b128 v[44:47], v67 offset:64512\n\t" \
    "s_nop 0\n\t" \
    "v_mul_f32 v15, v15, v7\n\t" \
    "s_waitcnt lgkmcnt(3)\n\t" \
    "v_mfma_f32_16x16x32_bf16 v[16:19], a[112:115], v[32:35], v[16:19]\n\t" \
    "v_cvt_pk_bf16_f32 v55, v14, v15\n\t" \
    "s_nop 1\n\t" \
    "v_mfma_f32_16x16x32_bf16 v[24:27], a[116:119], v[32:35], v[24:27]\n\t" \
    "s_barrier\n\t" \
    "ds_read_b128 v[32:35], v66 offset:0\n\t" \
    "v_permlane16_swap_b32 v52, v54\n\t" \
    "v_permlane16_swap_b32 v53, v55\n\t" \
    "s_nop 0\n\t" \
    "global_store_dwordx4 v67, v[52:55], s[50:51] sc1\n\t" \
    "s_add_u32 s50, s50, 114688\n\t" \
    "s_addc_u32 s51, s51, 0\n\t" \
    "global_load_dwordx4 a[152:155], v62, s[62:63] offset:3072 sc1\n\t" \
    "s_waitcnt lgkmcnt(3)\n\t" \
    "v_mfma_f32_16x16x32_bf16 v[20:23], a[112:115], v[36:39], v[20:23]\n\t" \
    "v_mfma_f32_16x16x32_bf16 v[28:31], a[116:119], v[36:39], v[28:31]\n\t" \
    "ds_read_b128 v[36:39], v66 offset:1024\n\t" \
    "s_waitcnt lgkmcnt(3)\n\t" \
    "v_mfma_f32_16x16x32_bf16 v[16:19], a[120:123], v[40:43], v[16:19]\n\t" \
    "v_mfma_f32_16x16x32_bf16 v[24:27], a[124:127], v[40:43], v[24:27]\n\t" \
    "ds_read_b128 v[40:43], v66 offset:2048\n\t" \
    "s_waitcnt lgkmcnt(3)\n\t" \
    "v_mfma_f32_16x16x32_bf16 v[20:23], a[120:123], v[44:47], v[20:23]\n\t" \
    "v_mfma_f32_16x16x32_bf16 v[28:31], a[124:127], v[44:47], v[28:31]\n\t" \
    "ds_read_b128 v[44:47], v66 offset:3072\n\t" \
    "s_waitcnt lgkmcnt(3)\n\t" \
    "v_mfma_f32_16x16x32_bf16 v[0:3], a[0:3], v[32:35], 0\n\t" \
    "v_mfma_f32_16x16x32_bf16 v[8:11], a[4:7], v[32:35], 0\n\t" \
    "ds_read_b128 v[32:35], v66 offset:4096\n\t" \
    "s_waitcnt lgkmcnt(3)\n\t" \
    "v_mfma_f32_16x16x32_bf16 v[4:7], a[0:3], v[36:39], 0\n\t" \
    "v_mfma_f32_16x16x32_bf16 v[12:15], a[4:7], v[36:39], 0\n\t" \
    "ds_read_b128 v[36:39], v66 offset:5120\n\t" \
    "s_waitcnt lgkmcnt(3)\n\t" \
    "v_mfma_f32_16x16x32_bf16 v[0:3], a[8:11], v[40:43], v[0:3]\n\t" \
    "v_mfma_f32_16x16x32_bf16 v[8:11], a[12:15], v[40:43], v[8:11]\n\t" \
    "ds_read_b128 v[40:43], v66 offset:6144\n\t" \
    "s_waitcnt lgkmcnt(3)\n\t" \
    "v_mfma_f32_16x16x32_bf16 v[4:7], a[8:11], v[44:47], v[4:7]\n\t" \
    "v_mul_f32 v48, v16, v16\n\t" \
    "v_mul_f32 v24, v16, v24\n\t" \
    "v_mfma_f32_16x16x32_bf16 v[12:15], a[12:15], v[44:47], v[12:15]\n\t" \
    "ds_read_b128 v[44:47], v66 offset:7168\n\t" \
    "v_fmaak_f32 v48, v63, v48, 0xc0135761\n\t" \
    "v_mul_f32 v16, v16, v48\n\t" \
    "s_waitcnt lgkmcnt(3)\n\t" \
    "v_mfma_f32_16x16x32_bf16 v[0:3], a[16:19], v[32:35], v[0:3]\n\t" \
    "v_exp_f32 v16, v16\n\t" \
    "v_mul_f32 v49, v17, v17\n\t" \
    "v_mfma_f32_16x16x32_bf16 v[8:11], a[20:23], v[32:35], v[8:11]\n\t" \
    "ds_read_b128 v[32:35], v66 offset:8192\n\t" \
    "v_fmaak_f32 v49, v63, v49, 0xc0135761\n\t" \
    "v_add_f32 v16, 1.0, v16\n\t" \
    "v_mul_f32 v25, v17, v25\n\t" \
    "s_waitcnt lgkmcnt(3)\n\t" \
    "v_mfma_f32_16x16x32_bf16 v[4:7], a[16:19], v[36:39], v[4:7]\n\t" \
    "v_rcp_f32 v16, v16\n\t" \
    "v_mfma_f32_16x16x32_bf16 v[12:15], a[20:23], v[36:39], v[12:15]\n\t" \
    "ds_read_b128 v[36:39], v66 offset:9216\n\t" \
    "v_mul_f32 v17, v17, v49\n\t" \
    "v_mul_f32 v24, v24, v16\n\t" \
    "s_waitcnt lgkmcnt(3)\n\t" \
    "v_mfma_f32_16x16x32_bf16 v[0:3], a[24:27], v[40:43], v[0:3]\n\t" \
    "v_exp_f32 v17, v17\n\t" \
    "v_mul_f32 v48, v18, v18\n\t" \
    "v_mfma_f32_16x16x32_bf16 v[8:11], a[28:31], v[40:43], v[8:11]\n\t" \
    "ds_read_b128 v[40:43], v66 offset:10240\n\t" \
    "v_fmaak_f32 v48, v63, v48, 0xc0135761\n\t" \
    "v_add_f32 v17, 1.0, v17\n\t" \
    "s_waitcnt lgkmcnt(3)\n\t" \
    "v_mfma_f32_16x16x32_bf16 v[4:7], a[24:27], v[44:47], v[4:7]\n\t" \
    "v_mul_f32 v26, v18, v26\n\t" \
    "v_rcp_f32 v17, v17\n\t" \
    "v_mfma_f32_16x16x32_bf16 v[12:15], a[28:31], v[44:47], v[12:15]\n\t" \
    "ds_read_b128 v[44:47], v66 offset:11264\n\t" \
    "v_mul_f32 v18, v18, v48\n\t" \
    "v_mul_f32 v25, v25, v17\n\t" \
    "s_waitcnt lgkmcnt(3)\n\t" \
    "v_mfma_f32_16x16x32_bf16 v[0:3], a[32:35], v[32:35], v[0:3]\n\t" \
    "v_cvt_pk_bf16_f32 v52, v24, v25\n\t" \
    "v_exp_f32 v18, v18\n\t" \
    "v_mfma_f32_16x16x32_bf16 v[8:11], a[36:39], v[32:35], v[8:11]\n\t" \
    "ds_read_b128 v[32:35], v66 offset:12288\n\t" \
    "v_mul_f32 v49, v19, v19\n\t" \
    "v_fmaak_f32 v49, v63, v49, 0xc0135761\n\t" \
    "s_waitcnt lgkmcnt(3)\n\t" \
    "v_mfma_f32_16x16x32_bf16 v[4:7], a[32:35], v[36:39], v[4:7]\n\t" \
    "v_add_f32 v18, 1.0, v18\n\t" \
    "v_mul_f32 v27, v19, v27\n\t" \
    "v_mfma_f32_16x16x32_bf16 v[12:15], a[36:39], v[36:39], v[12:15]\n\t" \
    "ds_read_b128 v[36:39], v66 offset:13312\n\t" \
    "v_rcp_f32 v18, v18\n\t" \
    "s_waitcnt lgkmcnt(3)\n\t" \
    "v_mfma_f32_16x16x32_bf16 v[0:3], a[40:43], v[40:43], v[0:3]\n\t" \
    "v_mul_f32 v19, v19, v49\n\t" \
    "v_mul_f32 v26, v26, v18\n\t" \
    "v_mfma_f32_16x16x32_bf16 v[8:11], a[44:47], v[40:43], v[8:11]\n\t" \
    "ds_read_b128 v[40:43], v66 offset:14336\n\t" \
    "v_exp_f32 v19, v19\n\t" \
    "v_mul_f32 v48, v20, v20\n\t" \
    "s_waitcnt lgkmcnt(3)\n\t" \
    "v_mfma_f32_16x16x32_bf16 v[4:7], a[40:43], v[44:47], v[4:7]\n\t" \
    "v_fmaak_f32 v48, v63, v48, 0xc0135761\n\t" \
    "v_add_f32 v19, 1.0, v19\n\t" \
    "v_mul_f32 v28, v20, v28\n\t" \
    "v_mfma_f32_16x16x32_bf16 v[12:15], a[44:47], v[44:47], v[12:15]\n\t" \
    "ds_read_b128 v[44:47], v66 offset:15360\n\t" \
    "v_rcp_f32 v19, v19\n\t" \
    "s_waitcnt lgkmcnt(3)\n\t" \
    "v_mfma_f32_16x16x32_bf16 v[0:3], a[48:51], v[32:35], v[0:3]\n\t" \
    "v_mul_f32 v20, v20, v48\n\t" \
    "v_mul_f32 v27, v27, v19\n\t" \
    "v_mfma_f32_16x16x32_bf16 v[8:11], a[52:55], v[32:35], v[8:11]\n\t" \
    "ds_read_b128 v[32:35], v66 offset:16384\n\t" \
    "v_cvt_pk_bf16_f32 v53, v26, v27\n\t" \
    "v_exp_f32 v20, v20\n\t" \
    "s_waitcnt lgkmcnt(3)\n\t" \
    "v_mfma_f32_16x16x32_bf16 v[4:7], a[48:51], v[36:39], v[4:7]\n\t" \
    "v_mul_f32 v49, v21, v21\n\t" \
    "v_fmaak_f32 v49, v63, v49, 0xc0135761\n\t" \
    "v_mfma_f32_16x16x32_bf16 v[12:15], a[52:55], v[36:39], v[12:15]\n\t" \
    "ds_read_b128 v[36:39], v66 offset:17408\n\t" \
    "v_add_f32 v20, 1.0, v20\n\t" \
    "v_mul_f32 v29, v21, v29\n\t" \
    "s_waitcnt lgkmcnt(3)\n\t" \
    "v_mfma_f32_16x16x32_bf16 v[0:3], a[56:59], v[40:43], v[0:3]\n\t" \
    "v_rcp_f32 v20, v20\n\t" \
    "v_mul_f32 v21, v21, v49\n\t" \
    "v_mfma_f32_16x16x32_bf16 v[8:11], a[60:63], v[40:43], v[8:11]\n\t" \
    "ds_read_b128 v[40:43], v66 offset:18432\n\t" \
    "v_mul_f32 v28, v28, v20\n\t" \
    "v_exp_f32 v21, v21\n\t" \
    "s_waitcnt lgkmcnt(3)\n\t" \
    "v_mfma_f32_16x16x32_bf16 v[4:7], a[56:59], v[44:47], v[4:7]\n\t" \
    "v_mul_f32 v48, v22, v22\n\t" \
    "v_fmaak_f32 v48, v63, v48, 0xc0135761\n\t" \
    "v_mfma_f32_16x16x32_bf16 v[12:15], a[60:63], v[44:47], v[12:15]\n\t" \
    "ds_read_b128 v[44:47], v66 offset:19456\n\t" \
    "v_add_f32 v21, 1.0, v21\n\t" \
    "v_mul_f32 v30, v22, v30\n\t" \
    "s_waitcnt lgkmcnt(3)\n\t" \
    "v_mfma_f32_16x16x32_bf16 v[0:3], a[64:67], v[32:35], v[0:3]\n\t" \
    "v_rcp_f32 v21, v21\n\t" \
    "v_mul_f32 v22, v22, v48\n\t" \
    "v_mfma_f32_16x16x32_bf16 v[8:11], a[68:71], v[32:35], v[8:11]\n\t" \
    "ds_read_b128 v[32:35], v66 offset:20480\n\t" \
    "v_mul_f32 v29, v29, v21\n\t" \
    "s_waitcnt lgkmcnt(3)\n\t" \
    "v_mfma_f32_16x16x32_bf16 v[4:7], a[64:67], v[36:39], v[4:7]\n\t" \
    "v_exp_f32 v22, v22\n\t" \
    "v_mul_f32 v49, v23, v23\n\t" \
    "v_mfma_f32_16x16x32_bf16 v[12:15], a[68:71], v[36:39], v[12:15]\n\t" \
    "ds_read_b128 v[36:39], v66 offset:21504\n\t" \
    "v_fmaak_f32 v49, v63, v49, 0xc0135761\n\t" \
    "v_add_f32 v22, 1.0, v22\n\t" \
    "v_mul_f32 v31, v23, v31\n\t" \
    "s_waitcnt lgkmcnt(3)\n\t" \
    "v_mfma_f32_16x16x32_bf16 v[0:3], a[72:75], v[40:43], v[0:3]\n\t" \
    "v_rcp_f32 v22, v22\n\t" \
    "v_mfma_f32_16x16x32_bf16 v[8:11], a[76:79], v[40:43], v[8:11]\n\t" \
    "ds_read_b128 v[40:43], v66 offset:22528\n\t" \
    "v_mul_f32 v23, v23, v49\n\t" \
    "v_mul_f32 v30, v30, v22\n\t" \
    "s_waitcnt lgkmcnt(3)\n\t" \
    "v_mfma_f32_16x16x32_bf16 v[4:7], a[72:75], v[44:47], v[4:7]\n\t" \
    "v_exp_f32 v23, v23\n\t" \
    "v_cvt_pk_bf16_f32 v54, v28, v29\n\t" \
    "v_mfma_f32_16x16x32_bf16 v[12:15], a[76:79], v[44:47], v[12:15]\n\t" \
    "ds_read_b128 v[44:47], v66 offset:23552\n\t" \
    "v_add_f32 v23, 1.0, v23\n\t" \
    "v_rcp_f32 v23, v23\n\t" \
    "s_waitcnt lgkmcnt(3)\n\t" \
    "v_mfma_f32_16x16x32_bf16 v[0:3], a[80:83], v[32:35], v[0:3]\n\t" \
    "s_nop 0\n\t" \
    "v_mul_f32 v31, v31, v23\n\t" \
    "v_cvt_pk_bf16_f32 v55, v30, v31\n\t" \
    "v_mfma_f32_16x16x32_bf16 v[8:11], a[84:87], v[32:35], v[8:11]\n\t" \
    "ds_read_b128 v[32:35], v66 offset:24576\n\t" \
    "s_nop 1\n\t" \
    "v_permlane16_swap_b32 v52, v54\n\t" \
    "v_permlane16_swap_b32 v53, v55\n\t" \
    "s_nop 0\n\t" \
    "global_store_dwordx4 v67, v[52:55], s[50:51] sc1\n\t" \
    "s_add_u32 s50, s50, 114688\n\t" \
    "s_addc_u32 s51, s51, 0\n\t" \
    "s_waitcnt lgkmcnt(3)\n\t" \
    "v_mfma_f32_16x16x32_bf16 v[4:7], a[80:83], v[36:39], v[4:7]\n\t" \
    "v_mfma_f32_16x16x32_bf16 v[12:15], a[84:87], v[36:39], v[12:15]\n\t" \
    "ds_read_b128 v[36:39], v66 offset:25600\n\t" \
    "global_load_dwordx4 a[156:159], v62, s[64:65] offset:3072 sc1\n\t" \
    "s_waitcnt lgkmcnt(3)\n\t" \
    "v_mfma_f32_16x16x32_bf16 v[0:3], a[88:91], v[40:43], v[0:3]\n\t" \
    "v_mfma_f32_16x16x32_bf16 v[8:11], a[92:95], v[40:43], v[8:11]\n\t" \
    "ds_read_b128 v[40:43], v66 offset:26624\n\t" \
    "s_add_u32 s62, s62, 8192\n\t" \
    "s_addc_u32 s63, s63, 0\n\t" \
    "global_load_dwordx4 a[160:163], v62, s[62:63] offset:-4096 sc1\n\t" \
    "s_waitcnt lgkmcnt(3)\n\t" \
    "v_mfma_f32_16x16x32_bf16 v[4:7], a[88:91], v[44:47], v[4:7]\n\t" \
    "v_mfma_f32_16x16x32_bf16 v[12:15], a[92:95], v[44:47], v[12:15]\n\t" \
    "ds_read_b128 v[44:47], v66 offset:27648\n\t" \
    "s_waitcnt lgkmcnt(3)\n\t" \
    "v_mfma_f32_16x16x32_bf16 v[0:3], a[96:99], v[32:35], v[0:3]\n\t" \
    "v_mfma_f32_16x16x32_bf16 v[8:11], a[100:103], v[32:35], v[8:11]\n\t" \
    "ds_read_b128 v[32:35], v66 offset:28672\n\t" \
    "s_add_u32 s64, s64, 8192\n\t" \
    "s_addc_u32 s65, s65, 0\n\t" \
    "global_load_dwordx4 a[164:167], v62, s[64:65] offset:-4096 sc1\n\t" \
    "s_waitcnt lgkmcnt(3)\n\t" \
    "v_mfma_f32_16x16x32_bf16 v[4:7], a[96:99], v[36:39], v[4:7]\n\t" \
    "v_mfma_f32_16x16x32_bf16 v[12:15], a[100:103], v[36:39], v[12:15]\n\t" \
    "ds_read_b128 v[36:39], v66 offset:29696\n\t" \
    "global_load_dwordx4 a[168:171], v62, s[62:63] offset:-3072 sc1\n\t" \
    "s_waitcnt lgkmcnt(3)\n\t" \
    "v_mfma_f32_16x16x32_bf16 v[0:3], a[104:107], v[40:43], v[0:3]\n\t" \
    "v_mfma_f32_16x16x32_bf16 v[8:11], a[108:111], v[40:43], v[8:11]\n\t" \
    "ds_read_b128 v[40:43], v66 offset:30720\n\t" \
    "s_waitcnt lgkmcnt(3)\n\t" \
    "v_mfma_f32_16x16x32_bf16 v[4:7], a[104:107], v[44:47], v[4:7]\n\t" \
    "v_mfma_f32_16x16x32_bf16 v[12:15], a[108:111], v[44:47], v[12:15]\n\t" \
    "ds_read_b128 v[44:47], v66 offset:31744\n\t" \
    "global_load_dwordx4 a[172:175], v62, s[64:65] offset:-3072 sc1\n\t" \
    "s_waitcnt lgkmcnt(3)\n\t" \
    "v_mfma_f32_16x16x32_bf16 v[0:3], a[112:115], v[32:35], v[0:3]\n\t" \
    "v_mfma_f32_16x16x32_bf16 v[8:11], a[116:119], v[32:35], v[8:11]\n\t" \
    "s_barrier\n\t" \
    "ds_read_b128 v[32:35], v66 offset:32768\n\t" \
    "global_load_dwordx4 a[176:179], v62, s[62:63] offset:-2048 sc1\n\t" \
    "s_waitcnt lgkmcnt(3)\n\t" \
    "v_mfma_f32_16x16x32_bf16 v[4:7], a[112:115], v[36:39], v[4:7]\n\t" \
    "v_mfma_f32_16x16x32_bf16 v[12:15], a[116:119], v[36:39], v[12:15]\n\t" \
    "ds_read_b128 v[36:39], v66 offset:33792\n\t" \
    "s_waitcnt lgkmcnt(3)\n\t" \
    "v_mfma_f32_16x16x32_bf16 v[0:3], a[120:123], v[40:43], v[0:3]\n\t" \
    "v_mfma_f32_16x16x32_bf16 v[8:11], a[124:127], v[40:43], v[8:11]\n\t" \
    "ds_read_b128 v[40:43], v66 offset:34816\n\t" \
    "global_load_dwordx4 a[180:183], v62, s[64:65] offset:-2048 sc1\n\t" \
    "s_waitcnt lgkmcnt(3)\n\t" \
    "v_mfma_f32_16x16x32_bf16 v[4:7], a[120:123], v[44:47], v[4:7]\n\t" \
    "v_mfma_f32_16x16x32_bf16 v[12:15], a[124:127], v[44:47], v[12:15]\n\t" \
    "ds_read_b128 v[44:47], v66 offset:35840\n\t" \
    "s_waitcnt lgkmcnt(3)\n\t" \
    "v_mfma_f32_16x16x32_bf16 v[16:19], a[0:3], v[32:35], 0\n\t" \
    "v_mfma_f32_16x16x32_bf16 v[24:27], a[4:7], v[32:35], 0\n\t" \
    "ds_read_b128 v[32:35], v66 offset:36864\n\t" \
    "s_waitcnt lgkmcnt(3)\n\t" \
    "v_mfma_f32_16x16x32_bf16 v[20:23], a[0:3], v[36:39], 0\n\t" \
    "v_mfma_f32_16x16x32_bf16 v[28:31], a[4:7], v[36:39], 0\n\t" \
    "ds_read_b128 v[36:39], v66 offset:37888\n\t" \
    "s_waitcnt lgkmcnt(3)\n\t" \
    "v_mfma_f32_16x16x32_bf16 v[16:19], a[8:11], v[40:43], v[16:19]\n\t" \
    "v_mfma_f32_16x16x32_bf16 v[24:27], a[12:15], v[40:43], v[24:27]\n\t" \
    "s_barrier\n\t" \
    "ds_read_b128 v[40:43], v66 offset:38912\n\t" \
    "s_waitcnt lgkmcnt(3)\n\t" \
    "v_mfma_f32_16x16x32_bf16 v[20:23], a[8:11], v[44:47], v[20:23]\n\t" \
    "v_mul_f32 v48, v0, v0\n\t" \
    "v_mul_f32 v8, v0, v8\n\t" \
    "v_mfma_f32_16x16x32_bf16 v[28:31], a[12:15], v[44:47], v[28:31]\n\t" \
    "ds_read_b128 v[44:47], v66 offset:39936\n\t" \
    "v_fmaak_f32 v48, v63, v48, 0xc0135761\n\t" \
    "s_waitcnt vmcnt(7)\n\t" \
    "s_mov_b64 exec, 1\n\t" \
    "global_store_dword v67, v64, s[56:57] sc1\n\t" \
    "s_mov_b64 exec, -1\n\t" \
    "s_waitcnt lgkmcnt(3)\n\t" \
    "v_mfma_f32_16x16x32_bf16 v[16:19], a[16:19], v[32:35], v[16:19]\n\t" \
    "v_mul_f32 v0, v0, v48\n\t" \
    "v_mfma_f32_16x16x32_bf16 v[24:27], a[20:23], v[32:35], v[24:27]\n\t" \
    "ds_read_b128 v[32:35], v66 offset:40960\n\t" \
    "v_exp_f32 v0, v0\n\t" \
    "v_mul_f32 v49, v1, v1\n\t" \
    "s_waitcnt lgkmcnt(3)\n\t" \
    "v_mfma_f32_16x16x32_bf16 v[20:23], a[16:19], v[36:39], v[20:23]\n\t" \
    "v_fmaak_f32 v49, v63, v49, 0xc0135761\n\t" \
    "v_mfma_f32_16x16x32_bf16 v[28:31], a[20:23], v[36:39], v[28:31]\n\t" \
    "ds_read_b128 v[36:39], v66 offset:41984\n\t" \
    "v_add_f32 v0, 1.0, v0\n\t" \
    "v_mul_f32 v9, v1, v9\n\t" \
    "s_waitcnt lgkmcnt(3)\n\t" \
    "v_mfma_f32_16x16x32_bf16 v[16:19], a[24:27], v[40:43], v[16:19]\n\t" \
    "v_rcp_f32 v0, v0\n\t" \
    "v_mfma_f32_16x16x32_bf16 v[24:27], a[28:31], v[40:43], v[24:27]\n\t" \
    "ds_read_b128 v[40:43], v66 offset:43008\n\t" \
    "v_mul_f32 v1, v1, v49\n\t" \
    "s_waitcnt lgkmcnt(3)\n\t" \
    "v_mfma_f32_16x16x32_bf16 v[20:23], a[24:27], v[44:47], v[20:23]\n\t" \
    "v_mul_f32 v8, v8, v0\n\t" \
    "v_mfma_f32_16x16x32_bf16 v[28:31], a[28:31], v[44:47], v[28:31]\n\t" \
    "ds_read_b128 v[44:47], v66 offset:44032\n\t" \
    "v_exp_f32 v1, v1\n\t" \
    "v_mul_f32 v48, v2, v2\n\t" \
    "s_waitcnt lgkmcnt(3)\n\t" \
    "v_mfma_f32_16x16x32_bf16 v[16:19], a[32:35], v[32:35], v[16:19]\n\t" \
    "v_fmaak_f32 v48, v63, v48, 0xc0135761\n\t" \
    "v_mfma_f32_16x16x32_bf16 v[24:27], a[36:39], v[32:35], v[24:27]\n\t" \
    "ds_read_b128 v[32:35], v66 offset:45056\n\t" \
    "v_add_f32 v1, 1.0, v1\n\t" \
    "v_mul_f32 v10, v2, v10\n\t" \
    "s_waitcnt lgkmcnt(3)\n\t" \
    "v_mfma_f32_16x16x32_bf16 v[20:23], a[32:35], v[36:39], v[20:23]\n\t" \
    "v_rcp_f32 v1, v1\n\t" \
    "v_mfma_f32_16x16x32_bf16 v[28:31], a[36:39], v[36:39], v[28:31]\n\t" \
    "ds_read_b128 v[36:39], v66 offset:46080\n\t" \
    "v_mul_f32 v2, v2, v48\n\t" \
    "s_waitcnt lgkmcnt(3)\n\t" \
    "v_mfma_f32_16x16x32_bf16 v[16:19], a[40:43], v[40:43], v[16:19]\n\t" \
    "v_mul_f32 v9, v9, v1\n\t" \
    "v_cvt_pk_bf16_f32 v52, v8, v9\n\t" \
    "v_mfma_f32_16x16x32_bf16 v[24:27], a[44:47], v[40:43], v[24:27]\n\t" \
    "ds_read_b128 v[40:43], v66 offset:47104\n\t" \
    "v_exp_f32 v2, v2\n\t" \
    "s_waitcnt lgkmcnt(3)\n\t" \
    "v_mfma_f32_16x16x32_bf16 v[20:23], a[40:43], v[44:47], v[20:23]\n\t" \
    "v_mul_f32 v49, v3, v3\n\t" \
    "v_mfma_f32_16x16x32_bf16 v[28:31], a[44:47], v[44:47], v[28:31]\n\t" \
    "ds_read_b128 v[44:47], v66 offset:48128\n\t" \
    "v_fmaak_f32 v49, v63, v49, 0xc0135761\n\t" \
    "v_add_f32 v2, 1.0, v2\n\t" \
    "s_waitcnt lgkmcnt(3)\n\t" \
    "v_mfma_f32_16x16x32_bf16 v[16:19], a[48:51], v[32:35], v[16:19]\n\t" \
    "v_mul_f32 v11, v3, v11\n\t" \
    "v_mfma_f32_16x16x32_bf16 v[24:27], a[52:55], v[32:35], v[24:27]\n\t" \
    "ds_read_b128 v[32:35], v66 offset:49152\n\t" \
    "v_rcp_f32 v2, v2\n\t" \
    "s_waitcnt lgkmcnt(3)\n\t" \
    "v_mfma_f32_16x16x32_bf16 v[20:23], a[48:51], v[36:39], v[20:23]\n\t" \
    "v_mul_f32 v3, v3, v49\n\t" \
    "v_mul_f32 v10, v10, v2\n\t" \
    "v_mfma_f32_16x16x32_bf16 v[28:31], a[52:55], v[36:39], v[28:31]\n\t" \
    "ds_read_b128 v[36:39], v66 offset:50176\n\t" \
    "v_exp_f32 v3, v3\n\t" \
    "s_waitcnt lgkmcnt(3)\n\t" \
    "v_mfma_f32_16x16x32_bf16 v[16:19], a[56:59], v[40:43], v[16:19]\n\t" \
    "v_mul_f32 v48, v4, v4\n\t" \
    "v_mfma_f32_16x16x32_bf16 v[24:27], a[60:63], v[40:43], v[24:27]\n\t" \
    "ds_read_b128 v[40:43], v66 offset:51200\n\t" \
    "v_fmaak_f32 v48, v63, v48, 0xc0135761\n\t" \
    "v_add_f32 v3, 1.0, v3\n\t" \
    "s_waitcnt lgkmcnt(3)\n\t" \
    "v_mfma_f32_16x16x32_bf16 v[20:23], a[56:59], v[44:47], v[20:23]\n\t" \
    "v_mul_f32 v12, v4, v12\n\t" \
    "v_mfma_f32_16x16x32_bf16 v[28:31], a[60:63], v[44:47], v[28:31]\n\t" \
    "ds_read_b128 v[44:47], v66 offset:52224\n\t" \
    "v_rcp_f32 v3, v3\n\t" \
    "s_waitcnt lgkmcnt(3)\n\t" \
    "v_mfma_f32_16x16x32_bf16 v[16:19], a[64:67], v[32:35], v[16:19]\n\t" \
    "v_mul_f32 v4, v4, v48\n\t" \
    "v_mul_f32 v11, v11, v3\n\t" \
    "v_mfma_f32_16x16x32_bf16 v[24:27], a[68:71], v[32:35], v[24:27]\n\t" \
    "ds_read_b128 v[32:35], v66 offset:53248\n\t" \
    "v_cvt_pk_bf16_f32 v53, v10, v11\n\t" \
    "s_waitcnt lgkmcnt(3)\n\t" \
    "v_mfma_f32_16x16x32_bf16 v[20:23], a[64:67], v[36:39], v[20:23]\n\t" \
    "v_exp_f32 v4, v4\n\t" \
    "v_mfma_f32_16x16x32_bf16 v[28:31], a[68:71], v[36:39], v[28:31]\n\t" \
    "ds_read_b128 v[36:39], v66 offset:54272\n\t" \
    "v_mul_f32 v49, v5, v5\n\t" \
    "v_fmaak_f32 v49, v63, v49, 0xc0135761\n\t" \
    "s_waitcnt lgkmcnt(3)\n\t" \
    "v_mfma_f32_16x16x32_bf16 v[16:19], a[72:75], v[40:43], v[16:19]\n\t" \
    "v_add_f32 v4, 1.0, v4\n\t" \
    "v_mfma_f32_16x16x32_bf16 v[24:27], a[76:79], v[40:43], v[24:27]\n\t" \
    "ds_read_b128 v[40:43], v66 offset:55296\n\t" \
    "v_mul_f32 v13, v5, v13\n\t" \
    "s_waitcnt lgkmcnt(3)\n\t" \
    "v_mfma_f32_16x16x32_bf16 v[20:23], a[72:75], v[44:47], v[20:23]\n\t" \
    "v_rcp_f32 v4, v4\n\t" \
    "v_mul_f32 v5, v5, v49\n\t" \
    "v_mfma_f32_16x16x32_bf16 v[28:31], a[76:79], v[44:47], v[28:31]\n\t" \
    "ds_read_b128 v[44:47], v66 offset:56320\n\t" \
    "v_mul_f32 v12, v12, v4\n\t" \
    "s_waitcnt lgkmcnt(3)\n\t" \
    "v_mfma_f32_16x16x32_bf16 v[16:19], a[80:83], v[32:35], v[16:19]\n\t" \
    "v_exp_f32 v5, v5\n\t" \
    "v_mfma_f32_16x16x32_bf16 v[24:27], a[84:87], v[32:35], v[24:27]\n\t" \
    "ds_read_b128 v[32:35], v66 offset:57344\n\t" \
    "v_mul_f32 v48, v6, v6\n\t" \
    "v_fmaak_f32 v48, v63, v48, 0xc0135761\n\t" \
    "s_waitcnt lgkmcnt(3)\n\t" \
    "v_mfma_f32_16x16x32_bf16 v[20:23], a[80:83], v[36:39], v[20:23]\n\t" \
    "v_add_f32 v5, 1.0, v5\n\t" \
    "v_mfma_f32_16x16x32_bf16 v[28:31], a[84:87], v[36:39], v[28:31]\n\t" \
    "ds_read_b128 v[36:39], v66 offset:58368\n\t" \
    "v_mul_f32 v14, v6, v14\n\t" \
    "s_waitcnt lgkmcnt(3)\n\t" \
    "v_mfma_f32_16x16x32_bf16 v[16:19], a[88:91], v[40:43], v[16:19]\n\t" \
    "v_rcp_f32 v5, v5\n\t" \
    "v_mul_f32 v6, v6, v48\n\t" \
    "v_mfma_f32_16x16x32_bf16 v[24:27], a[92:95], v[40:43], v[24:27]\n\t" \
    "ds_read_b128 v[40:43], v66 offset:59392\n\t" \
    "v_mul_f32 v13, v13, v5\n\t" \
    "s_waitcnt lgkmcnt(3)\n\t" \
    "v_mfma_f32_16x16x32_bf16 v[20:23], a[88:91], v[44:47], v[20:23]\n\t" \
    "v_exp_f32 v6, v6\n\t" \
    "v_mfma_f32_16x16x32_bf16 v[28:31], a[92:95], v[44:47], v[28:31]\n\t" \
    "ds_read_b128 v[44:47], v66 offset:60416\n\t" \
    "v_mul_f32 v49, v7, v7\n\t" \
    "s_waitcnt lgkmcnt(3)\n\t" \
    "v_mfma_f32_16x16x32_bf16 v[16:19], a[96:99], v[32:35], v[16:19]\n\t" \
    "v_fmaak_f32 v49, v63, v49, 0xc0135761\n\t" \
    "v_add_f32 v6, 1.0, v6\n\t" \
    "v_mfma_f32_16x16x32_bf16 v[24:27], a[100:103], v[32:35], v[24:27]\n\t" \
    "ds_read_b128 v[32:35], v66 offset:61440\n\t" \
    "v_mul_f32 v15, v7, v15\n\t" \
    "s_waitcnt lgkmcnt(3)\n\t" \
    "v_mfma_f32_16x16x32_bf16 v[20:23], a[96:99], v[36:39], v[20:23]\n\t" \
    "v_rcp_f32 v6, v6\n\t" \
    "v_mfma_f32_16x16x32_bf16 v[28:31], a[100:103], v[36:39], v[28:31]\n\t" \
    "ds_read_b128 v[36:39], v66 offset:62464\n\t" \
    "v_mul_f32 v7, v7, v49\n\t" \
    "v_mul_f32 v14, v14, v6\n\t" \
    "s_waitcnt lgkmcnt(3)\n\t" \
    "v_mfma_f32_16x16x32_bf16 v[16:19], a[104:107], v[40:43], v[16:19]\n\t" \
    "v_exp_f32 v7, v7\n\t" \
    "v_mfma_f32_16x16x32_bf16 v[24:27], a[108:111], v[40:43], v[24:27]\n\t" \
    "ds_read_b128 v[40:43], v66 offset:63488\n\t" \
    "v_cvt_pk_bf16_f32 v54, v12, v13\n\t" \
    "s_waitcnt lgkmcnt(3)\n\t" \
    "v_mfma_f32_16x16x32_bf16 v[20:23], a[104:107], v[44:47], v[20:23]\n\t" \
    "v_add_f32 v7, 1.0, v7\n\t" \
    "v_rcp_f32 v7, v7\n\t" \
    "v_mfma_f32_16x16x32_bf16 v[28:31], a[108:111], v[44:47], v[28:31]\n\t" \
    "ds_read_b128 v[44:47], v66 offset:64512\n\t" \
    "s_nop 0\n\t" \
    "v_mul_f32 v15, v15, v7\n\t" \
    "s_waitcnt lgkmcnt(3)\n\t" \
    "v_mfma_f32_16x16x32_bf16 v[16:19], a[112:115], v[32:35], v[16:19]\n\t" \
    "v_cvt_pk_bf16_f32 v55, v14, v15\n\t" \
    "s_nop 1\n\t" \
    "v_mfma_f32_16x16x32_bf16 v[24:27], a[116:119], v[32:35], v[24:27]\n\t" \
    "v_permlane16_swap_b32 v52, v54\n\t" \
    "v_permlane16_swap_b32 v53, v55\n\t" \
    "s_nop 0\n\t" \
    "global_store_dwordx4 v67, v[52:55], s[50:51] sc1\n\t" \
    "s_add_u32 s50, s50, 114688\n\t" \
    "s_addc_u32 s51, s51, 0\n\t" \
    "s_waitcnt lgkmcnt(2)\n\t" \
    "v_mfma_f32_16x16x32_bf16 v[20:23], a[112:115], v[36:39], v[20:23]\n\t" \
    "v_mfma_f32_16x16x32_bf16 v[28:31], a[116:119], v[36:39], v[28:31]\n\t" \
    "s_cmp_gt_u32 s43, 3\n\t" \
    "s_cbranch_scc1 .Lnep_%=\n\t" \
    "global_load_dword v60, v61, s[54:55] sc1\n\t" \
    ".Lnep_%=:\n\t" \
    "s_waitcnt lgkmcnt(1)\n\t" \
    "v_mfma_f32_16x16x32_bf16 v[16:19], a[120:123], v[40:43], v[16:19]\n\t" \
    "v_mfma_f32_16x16x32_bf16 v[24:27], a[124:127], v[40:43], v[24:27]\n\t" \
    "s_waitcnt lgkmcnt(0)\n\t" \
    "v_mfma_f32_16x16x32_bf16 v[20:23], a[120:123], v[44:47], v[20:23]\n\t" \
    "v_mfma_f32_16x16x32_bf16 v[28:31], a[124:127], v[44:47], v[28:31]\n\t" \
    "s_cmp_gt_u32 s43, 3\n\t" \
    "s_cbranch_scc1 .Llate_%=\n\t" \
    "s_nop 10\n\t" \
    "v_mul_f32 v48, v16, v16\n\t" \
    "v_mul_f32 v24, v16, v24\n\t" \
    "v_fmaak_f32 v48, v63, v48, 0xc0135761\n\t" \
    "v_mul_f32 v16, v16, v48\n\t" \
    "v_exp_f32 v16, v16\n\t" \
    "v_mul_f32 v49, v17, v17\n\t" \
    "v_fmaak_f32 v49, v63, v49, 0xc0135761\n\t" \
    "v_add_f32 v16, 1.0, v16\n\t" \
    "v_mul_f32 v25, v17, v25\n\t" \
    "v_rcp_f32 v16, v16\n\t" \
    "v_mul_f32 v17, v17, v49\n\t" \
    "v_mul_f32 v24, v24, v16\n\t" \
    "v_exp_f32 v17, v17\n\t" \
    "v_mul_f32 v48, v18, v18\n\t" \
    "v_fmaak_f32 v48, v63, v48, 0xc0135761\n\t" \
    "v_add_f32 v17, 1.0, v17\n\t" \
    "v_mul_f32 v26, v18, v26\n\t" \
    "v_rcp_f32 v17, v17\n\t" \
    "v_mul_f32 v18, v18, v48\n\t" \
    "v_mul_f32 v25, v25, v17\n\t" \
    "v_cvt_pk_bf16_f32 v52, v24, v25\n\t" \
    "v_exp_f32 v18, v18\n\t" \
    "v_mul_f32 v49, v19, v19\n\t" \
    "v_fmaak_f32 v49, v63, v49, 0xc0135761\n\t" \
    "v_add_f32 v18, 1.0, v18\n\t" \
    "v_mul_f32 v27, v19, v27\n\t" \
    "v_rcp_f32 v18, v18\n\t" \
    "v_mul_f32 v19, v19, v49\n\t" \
    "v_mul_f32 v26, v26, v18\n\t" \
    "v_exp_f32 v19, v19\n\t" \
    "v_mul_f32 v48, v20, v20\n\t" \
    "v_fmaak_f32 v48, v63, v48, 0xc0135761\n\t" \
    "v_add_f32 v19, 1.0, v19\n\t" \
    "v_mul_f32 v28, v20, v28\n\t" \
    "v_rcp_f32 v19, v19\n\t" \
    "v_mul_f32 v20, v20, v48\n\t" \
    "v_mul_f32 v27, v27, v19\n\t" \
    "v_cvt_pk_bf16_f32 v53, v26, v27\n\t" \
    "v_exp_f32 v20, v20\n\t" \
    "v_mul_f32 v49, v21, v21\n\t" \
    "v_fmaak_f32 v49, v63, v49, 0xc0135761\n\t" \
    "v_add_f32 v20, 1.0, v20\n\t" \
    "v_mul_f32 v29, v21, v29\n\t" \
    "v_rcp_f32 v20, v20\n\t" \
    "v_mul_f32 v21, v21, v49\n\t" \
    "v_mul_f32 v28, v28, v20\n\t" \
    "v_exp_f32 v21, v21\n\t" \
    "v_mul_f32 v48, v22, v22\n\t" \
    "v_fmaak_f32 v48, v63, v48, 0xc0135761\n\t" \
    "v_add_f32 v21, 1.0, v21\n\t" \
    "v_mul_f32 v30, v22, v30\n\t" \
    "v_rcp_f32 v21, v21\n\t" \
    "v_mul_f32 v22, v22, v48\n\t" \
    "v_mul_f32 v29, v29, v21\n\t" \
    "v_exp_f32 v22, v22\n\t" \
    "v_mul_f32 v49, v23, v23\n\t" \
    "v_fmaak_f32 v49, v63, v49, 0xc0135761\n\t" \
    "v_add_f32 v22, 1.0, v22\n\t" \
    "v_mul_f32 v31, v23, v31\n\t" \
    "v_rcp_f32 v22, v22\n\t" \
    "v_mul_f32 v23, v23, v49\n\t" \
    "v_mul_f32 v30, v30, v22\n\t" \
    "v_exp_f32 v23, v23\n\t" \
    "v_cvt_pk_bf16_f32 v54, v28, v29\n\t" \
    "v_add_f32 v23, 1.0, v23\n\t" \
    "v_rcp_f32 v23, v23\n\t" \
    "s_nop 0\n\t" \
    "v_mul_f32 v31, v31, v23\n\t" \
    "v_cvt_pk_bf16_f32 v55, v30, v31\n\t" \
    "s_nop 1\n\t" \
    "v_permlane16_swap_b32 v52, v54\n\t" \
    "v_permlane16_swap_b32 v53, v55\n\t" \
    "s_nop 0\n\t" \
    "global_store_dwordx4 v67, v[52:55], s[50:51] sc1\n\t" \
    "s_waitcnt vmcnt(1)\n\t" \
    "s_mov_b32 s46, 200000\n\t" \
    ".Lpoll1_%=:\n\t" \
    "v_cmp_eq_u32 vcc, 0, v60\n\t" \
    "s_cbranch_vccz .Ldone1_%=\n\t" \
    "s_sub_u32 s46, s46, 1\n\t" \
    "s_cbranch_scc1 .Ldone1_%=\n\t" \
    "s_sleep 1\n\t" \
    "global_load_dword v60, v61, s[54:55] sc1\n\t" \
    "s_waitcnt vmcnt(0)\n\t" \
    "s_branch .Lpoll1_%=\n\t" \
    ".Ldone1_%=:\n\t" \
    "global_load_dwordx4 a[120:123], v62, s[62:63] offset:-1024 sc1\n\t" \
    "global_load_dwordx4 a[124:127], v62, s[64:65] offset:-1024 sc1\n\t" \
    "global_load_dwordx4 a[0:3], v67, s[66:67] sc1\n\t" \
    "global_load_dwordx4 a[4:7], v67, s[66:67] offset:1024 sc1\n\t" \
    "global_load_dwordx4 a[8:11], v67, s[66:67] offset:2048 sc1\n\t" \
    "global_load_dwordx4 a[12:15], v67, s[66:67] offset:3072 sc1\n\t" \
    "s_add_u32 s66, s66, 8192\n\t" \
    "s_addc_u32 s67, s67, 0\n\t" \
    "global_load_dwordx4 a[16:19], v67, s[66:67] offset:-4096 sc1\n\t" \
    "global_load_dwordx4 a[20:23], v67, s[66:67] offset:-3072 sc1\n\t" \
    "global_load_dwordx4 a[24:27], v67, s[66:67] offset:-2048 sc1\n\t" \
    "global_load_dwordx4 a[28:31], v67, s[66:67] offset:-1024 sc1\n\t" \
    "global_load_dwordx4 a[32:35], v67, s[66:67] sc1\n\t" \
    "global_load_dwordx4 a[36:39], v67, s[66:67] offset:1024 sc1\n\t" \
    "v_mov_b32 v64, 2\n\t" \
    "s_waitcnt vmcnt(12)\n\t" \
    "s_mov_b64 exec, 1\n\t" \
    "global_store_dword v67, v64, s[56:57] sc1\n\t" \
    "s_mov_b64 exec, -1\n\t" \
    "s_barrier\n\t" \
    "s_branch .Ldown_e_%=\n\t" \
    ".Llate_%=:\n\t" \
    "s_setprio 3\n\t" \
    "s_nop 10\n\t" \
    "v_mul_f32 v48, v16, v16\n\t" \
    "v_mul_f32 v24, v16, v24\n\t" \
    "v_fmaak_f32 v48, v63, v48, 0xc0135761\n\t" \
    "v_mul_f32 v16, v16, v48\n\t" \
    "v_exp_f32 v16, v16\n\t" \
    "v_mul_f32 v49, v17, v17\n\t" \
    "v_fmaak_f32 v49, v63, v49, 0xc0135761\n\t" \
    "v_add_f32 v16, 1.0, v16\n\t" \
    "v_mul_f32 v25, v17, v25\n\t" \
    "v_rcp_f32 v16, v16\n\t" \
    "v_mul_f32 v17, v17, v49\n\t" \
    "v_mul_f32 v24, v24, v16\n\t" \
    "v_exp_f32 v17, v17\n\t" \
    "v_mul_f32 v48, v18, v18\n\t" \
    "v_fmaak_f32 v48, v63, v48, 0xc0135761\n\t" \
    "v_add_f32 v17, 1.0, v17\n\t" \
    "v_mul_f32 v26, v18, v26\n\t" \
    "v_rcp_f32 v17, v17\n\t" \
    "v_mul_f32 v18, v18, v48\n\t" \
    "v_mul_f32 v25, v25, v17\n\t" \
    "v_cvt_pk_bf16_f32 v52, v24, v25\n\t" \
    "v_exp_f32 v18, v18\n\t" \
    "v_mul_f32 v49, v19, v19\n\t" \
    "v_fmaak_f32 v49, v63, v49, 0xc0135761\n\t" \
    "v_add_f32 v18, 1.0, v18\n\t" \
    "v_mul_f32 v27, v19, v27\n\t" \
    "v_rcp_f32 v18, v18\n\t" \
    "v_mul_f32 v19, v19, v49\n\t" \
    "v_mul_f32 v26, v26, v18\n\t" \
    "v_exp_f32 v19, v19\n\t" \
    "v_mul_f32 v48, v20, v20\n\t" \
    "v_fmaak_f32 v48, v63, v48, 0xc0135761\n\t" \
    "v_add_f32 v19, 1.0, v19\n\t" \
    "v_mul_f32 v28, v20, v28\n\t" \
    "v_rcp_f32 v19, v19\n\t" \
    "v_mul_f32 v20, v20, v48\n\t" \
    "v_mul_f32 v27, v27, v19\n\t" \
    "v_cvt_pk_bf16_f32 v53, v26, v27\n\t" \
    "v_exp_f32 v20, v20\n\t" \
    "v_mul_f32 v49, v21, v21\n\t" \
    "v_fmaak_f32 v49, v63, v49, 0xc0135761\n\t" \
    "v_add_f32 v20, 1.0, v20\n\t" \
    "v_mul_f32 v29, v21, v29\n\t" \
    "v_rcp_f32 v20, v20\n\t" \
    "v_mul_f32 v21, v21, v49\n\t" \
    "v_mul_f32 v28, v28, v20\n\t" \
    "v_exp_f32 v21, v21\n\t" \
    "v_mul_f32 v48, v22, v22\n\t" \
    "v_fmaak_f32 v48, v63, v48, 0xc0135761\n\t" \
    "v_add_f32 v21, 1.0, v21\n\t" \
    "v_mul_f32 v30, v22, v30\n\t" \
    "v_rcp_f32 v21, v21\n\t" \
    "v_mul_f32 v22, v22, v48\n\t" \
    "v_mul_f32 v29, v29, v21\n\t" \
    "v_exp_f32 v22, v22\n\t" \
    "v_mul_f32 v49, v23, v23\n\t" \
    "v_fmaak_f32 v49, v63, v49, 0xc0135761\n\t" \
    "v_add_f32 v22, 1.0, v22\n\t" \
    "v_mul_f32 v31, v23, v31\n\t" \
    "v_rcp_f32 v22, v22\n\t" \
    "v_mul_f32 v23, v23, v49\n\t" \
    "v_mul_f32 v30, v30, v22\n\t" \
    "v_exp_f32 v23, v23\n\t" \
    "v_cvt_pk_bf16_f32 v54, v28, v29\n\t" \
    "v_add_f32 v23, 1.0, v23\n\t" \
    "v_rcp_f32 v23, v23\n\t" \
    "s_nop 0\n\t" \
    "v_mul_f32 v31, v31, v23\n\t" \
    "v_cvt_pk_bf16_f32 v55, v30, v31\n\t" \
    "s_nop 1\n\t" \
    "v_permlane16_swap_b32 v52, v54\n\t" \
    "v_permlane16_swap_b32 v53, v55\n\t" \
    "s_nop 0\n\t" \
    "global_store_dwordx4 v67, v[52:55], s[50:51] sc1\n\t" \
    "s_barrier\n\t" \
    "global_load_dwordx4 a[120:123], v62, s[62:63] offset:-1024 sc1\n\t" \
    "global_load_dwordx4 a[124:127], v62, s[64:65] offset:-1024 sc1\n\t" \
    "v_mov_b32 v64, 2\n\t" \
    "s_waitcnt vmcnt(2)\n\t" \
    "s_mov_b64 exec, 1\n\t" \
    "global_store_dword v67, v64, s[56:57] sc1\n\t" \
    "s_mov_b64 exec, -1\n\t" \
    "s_setprio 0\n\t" \
    "s_barrier\n\t" \
    ".Llate2_%=:\n\t" \
    "s_mov_b32 s46, 200000\n\t" \
    ".Lpoll2_%=:\n\t" \
    "global_load_dword v60, v61, s[54:55] sc1\n\t" \
    "s_waitcnt vmcnt(0)\n\t" \
    "v_cmp_gt_u32 vcc, 2, v60\n\t" \
    "s_cbranch_vccz .Ldone2_%=\n\t" \
    "s_sub_u32 s46, s46, 1\n\t" \
    "s_cbranch_scc1 .Ldone2_%=\n\t" \
    "s_sleep 1\n\t" \
    "s_branch .Lpoll2_%=\n\t" \
    ".Ldone2_%=:\n\t" \
    "global_load_dwordx4 a[0:3], v67, s[66:67] sc1\n\t" \
    "global_load_dwordx4 a[4:7], v67, s[66:67] offset:1024 sc1\n\t" \
    "global_load_dwordx4 a[8:11], v67, s[66:67] offset:2048 sc1\n\t" \
    "global_load_dwordx4 a[12:15], v67, s[66:67] offset:3072 sc1\n\t" \
    "s_add_u32 s66, s66, 8192\n\t" \
    "s_addc_u32 s67, s67, 0\n\t" \
    "global_load_dwordx4 a[16:19], v67, s[66:67] offset:-4096 sc1\n\t" \
    "global_load_dwordx4 a[20:23], v67, s[66:67] offset:-3072 sc1\n\t" \
    "global_load_dwordx4 a[24:27], v67, s[66:67] offset:-2048 sc1\n\t" \
    "global_load_dwordx4 a[28:31], v67, s[66:67] offset:-1024 sc1\n\t" \
    "global_load_dwordx4 a[32:35], v67, s[66:67] sc1\n\t" \
    "global_load_dwordx4 a[36:39], v67, s[66:67] offset:1024 sc1\n\t" \
    "global_load_dwordx4 a[40:43], v67, s[66:67] offset:2048 sc1\n\t" \
    "global_load_dwordx4 a[44:47], v67, s[66:67] offset:3072 sc1\n\t" \
    "s_add_u32 s66, s66, 8192\n\t" \
    "s_addc_u32 s67, s67, 0\n\t" \
    "global_load_dwordx4 a[48:51], v67, s[66:67] offset:-4096 sc1\n\t" \
    "global_load_dwordx4 a[52:55], v67, s[66:67] offset:-3072 sc1\n\t" \
    "s_branch .Ldown_l_%=\n\t" \
    ".Lsvc_%=:\n\t" \
    "s_setprio 3\n\t" \
    "s_waitcnt vmcnt(0)\n\t" \
    "s_barrier\n\t" \
    "s_mov_b64 exec, 1\n\t" \
    "global_store_dword v67, v64, s[56:57] sc1\n\t" \
    "s_mov_b64 exec, -1\n\t" \
    "s_mov_b32 s46, 200000\n\t" \
    ".Lpoll3_%=:\n\t" \
    "global_load_dword v48, v65, s[52:53] sc1\n\t" \
    "s_waitcnt vmcnt(0)\n\t" \
    "v_cmp_gt_u32 vcc, 1, v48\n\t" \
    "s_cbranch_vccz .Ldone3_%=\n\t" \
    "s_sub_u32 s46, s46, 1\n\t" \
    "s_cbranch_scc1 .Ldone3_%=\n\t" \
    "s_sleep 1\n\t" \
    "s_branch .Lpoll3_%=\n\t" \
    ".Ldone3_%=:\n\t" \
    "s_mov_b32 m0, 0\n\t" \
    "s_nop 0\n\t" \
    "global_load_lds_dwordx4 v67, s[48:49] sc1\n\t" \
    "global_load_lds_dwordx4 v67, s[48:49] offset:1024 sc1\n\t" \
    "global_load_lds_dwordx4 v67, s[48:49] offset:2048 sc1\n\t" \
    "global_load_lds_dwordx4 v67, s[48:49] offset:3072 sc1\n\t" \
    "s_add_u32 s48, s48, 4096\n\t" \
    "s_addc_u32 s49, s49, 0\n\t" \
    "s_mov_b32 m0, 4096\n\t" \
    "s_nop 0\n\t" \
    "global_load_lds_dwordx4 v67, s[48:49] sc1\n\t" \
    "global_load_lds_dwordx4 v67, s[48:49] offset:1024 sc1\n\t" \
    "global_load_lds_dwordx4 v67, s[48:49] offset:2048 sc1\n\t" \
    "global_load_lds_dwordx4 v67, s[48:49] offset:3072 sc1\n\t" \
    "s_add_u32 s48, s48, 4096\n\t" \
    "s_addc_u32 s49, s49, 0\n\t" \
    "s_mov_b32 m0, 8192\n\t" \
    "s_nop 0\n\t" \
    "global_load_lds_dwordx4 v67, s[48:49] sc1\n\t" \
    "global_load_lds_dwordx4 v67, s[48:49] offset:1024 sc1\n\t" \
    "global_load_lds_dwordx4 v67, s[48:49] offset:2048 sc1\n\t" \
    "global_load_lds_dwordx4 v67, s[48:49] offset:3072 sc1\n\t" \
    "s_add_u32 s48, s48, 4096\n\t" \
    "s_addc_u32 s49, s49, 0\n\t" \
    "s_mov_b32 m0, 12288\n\t" \
    "s_nop 0\n\t" \
    "global_load_lds_dwordx4 v67, s[48:49] sc1\n\t" \
    "global_load_lds_dwordx4 v67, s[48:49] offset:1024 sc1\n\t" \
    "global_load_lds_dwordx4 v67, s[48:49] offset:2048 sc1\n\t" \
    "global_load_lds_dwordx4 v67, s[48:49] offset:3072 sc1\n\t" \
    "s_add_u32 s48, s48, 4096\n\t" \
    "s_addc_u32 s49, s49, 0\n\t" \
    "s_mov_b32 m0, 16384\n\t" \
    "s_nop 0\n\t" \
    "global_load_lds_dwordx4 v67, s[48:49] sc1\n\t" \
    "global_load_lds_dwordx4 v67, s[48:49] offset:1024 sc1\n\t" \
    "global_load_lds_dwordx4 v67, s[48:49] offset:2048 sc1\n\t" \
    "global_load_lds_dwordx4 v67, s[48:49] offset:3072 sc1\n\t" \
    "s_add_u32 s48, s48, 4096\n\t" \
    "s_addc_u32 s49, s49, 0\n\t" \
    "s_mov_b32 m0, 20480\n\t" \
    "s_nop 0\n\t" \
    "global_load_lds_dwordx4 v67, s[48:49] sc1\n\t" \
    "global_load_lds_dwordx4 v67, s[48:49] offset:1024 sc1\n\t" \
    "global_load_lds_dwordx4 v67, s[48:49] offset:2048 sc1\n\t" \
    "global_load_lds_dwordx4 v67, s[48:49] offset:3072 sc1\n\t" \
    "s_add_u32 s48, s48, 4096\n\t" \
    "s_addc_u32 s49, s49, 0\n\t" \
    "s_mov_b32 m0, 24576\n\t" \
    "s_nop 0\n\t" \
    "global_load_lds_dwordx4 v67, s[48:49] sc1\n\t" \
    "global_load_lds_dwordx4 v67, s[48:49] offset:1024 sc1\n\t" \
    "global_load_lds_dwordx4 v67, s[48:49] offset:2048 sc1\n\t" \
    "global_load_lds_dwordx4 v67, s[48:49] offset:3072 sc1\n\t" \
    "s_add_u32 s48, s48, 4096\n\t" \
    "s_addc_u32 s49, s49, 0\n\t" \
    "s_mov_b32 m0, 28672\n\t" \
    "s_nop 0\n\t" \
    "global_load_lds_dwordx4 v67, s[48:49] sc1\n\t" \
    "global_load_lds_dwordx4 v67, s[48:49] offset:1024 sc1\n\t" \
    "global_load_lds_dwordx4 v67, s[48:49] offset:2048 sc1\n\t" \
    "global_load_lds_dwordx4 v67, s[48:49] offset:3072 sc1\n\t" \
    "s_barrier\n\t" \
    "s_add_u32 s48, s48, 4096\n\t" \
    "s_addc_u32 s49, s49, 0\n\t" \
    "s_mov_b32 m0, 32768\n\t" \
    "s_nop 0\n\t" \
    "global_load_lds_dwordx4 v67, s[48:49] sc1\n\t" \
    "global_load_lds_dwordx4 v67, s[48:49] offset:1024 sc1\n\t" \
    "global_load_lds_dwordx4 v67, s[48:49] offset:2048 sc1\n\t" \
    "global_load_lds_dwordx4 v67, s[48:49] offset:3072 sc1\n\t" \
    "s_add_u32 s48, s48, 4096\n\t" \
    "s_addc_u32 s49, s49, 0\n\t" \
    "s_mov_b32 m0, 36864\n\t" \
    "s_nop 0\n\t" \
    "global_load_lds_dwordx4 v67, s[48:49] sc1\n\t" \
    "global_load_lds_dwordx4 v67, s[48:49] offset:1024 sc1\n\t" \
    "global_load_lds_dwordx4 v67, s[48:49] offset:2048 sc1\n\t" \
    "global_load_lds_dwordx4 v67, s[48:49] offset:3072 sc1\n\t" \
    "s_add_u32 s48, s48, 4096\n\t" \
    "s_addc_u32 s49, s49, 0\n\t" \
    "s_mov_b32 m0, 40960\n\t" \
    "s_nop 0\n\t" \
    "global_load_lds_dwordx4 v67, s[48:49] sc1\n\t" \
    "global_load_lds_dwordx4 v67, s[48:49] offset:1024 sc1\n\t" \
    "global_load_lds_dwordx4 v67, s[48:49] offset:2048 sc1\n\t" \
    "global_load_lds_dwordx4 v67, s[48:49] offset:3072 sc1\n\t" \
    "s_add_u32 s48, s48, 4096\n\t" \
    "s_addc_u32 s49, s49, 0\n\t" \
    "s_mov_b32 m0, 45056\n\t" \
    "s_nop 0\n\t" \
    "global_load_lds_dwordx4 v67, s[48:49] sc1\n\t" \
    "global_load_lds_dwordx4 v67, s[48:49] offset:1024 sc1\n\t" \
    "global_load_lds_dwordx4 v67, s[48:49] offset:2048 sc1\n\t" \
    "global_load_lds_dwordx4 v67, s[48:49] offset:3072 sc1\n\t" \
    "s_add_u32 s48, s48, 4096\n\t" \
    "s_addc_u32 s49, s49, 0\n\t" \
    "s_mov_b32 m0, 49152\n\t" \
    "s_nop 0\n\t" \
    "global_load_lds_dwordx4 v67, s[48:49] sc1\n\t" \
    "global_load_lds_dwordx4 v67, s[48:49] offset:1024 sc1\n\t" \
    "global_load_lds_dwordx4 v67, s[48:49] offset:2048 sc1\n\t" \
    "global_load_lds_dwordx4 v67, s[48:49] offset:3072 sc1\n\t" \
    "s_add_u32 s48, s48, 4096\n\t" \
    "s_addc_u32 s49, s49, 0\n\t" \
    "s_mov_b32 m0, 53248\n\t" \
    "s_nop 0\n\t" \
    "global_load_lds_dwordx4 v67, s[48:49] sc1\n\t" \
    "global_load_lds_dwordx4 v67, s[48:49] offset:1024 sc1\n\t" \
    "global_load_lds_dwordx4 v67, s[48:49] offset:2048 sc1\n\t" \
    "global_load_lds_dwordx4 v67, s[48:49] offset:3072 sc1\n\t" \
    "s_add_u32 s48, s48, 4096\n\t" \
    "s_addc_u32 s49, s49, 0\n\t" \
    "s_mov_b32 m0, 57344\n\t" \
    "s_nop 0\n\t" \
    "global_load_lds_dwordx4 v67, s[48:49] sc1\n\t" \
    "global_load_lds_dwordx4 v67, s[48:49] offset:1024 sc1\n\t" \
    "global_load_lds_dwordx4 v67, s[48:49] offset:2048 sc1\n\t" \
    "global_load_lds_dwordx4 v67, s[48:49] offset:3072 sc1\n\t" \
    "s_add_u32 s48, s48, 4096\n\t" \
    "s_addc_u32 s49, s49, 0\n\t" \
    "s_mov_b32 m0, 61440\n\t" \
    "s_nop 0\n\t" \
    "global_load_lds_dwordx4 v67, s[48:49] sc1\n\t" \
    "global_load_lds_dwordx4 v67, s[48:49] offset:1024 sc1\n\t" \
    "s_waitcnt vmcnt(30)\n\t" \
    "s_barrier\n\t" \
    "global_load_lds_dwordx4 v67, s[48:49] offset:2048 sc1\n\t" \
    "global_load_lds_dwordx4 v67, s[48:49] offset:3072 sc1\n\t" \
    "s_add_u32 s48, s48, 4096\n\t" \
    "s_addc_u32 s49, s49, 0\n\t" \
    "s_mov_b32 m0, 65536\n\t" \
    "s_nop 0\n\t" \
    "global_load_lds_dwordx4 v67, s[48:49] sc1\n\t" \
    "global_load_lds_dwordx4 v67, s[48:49] offset:1024 sc1\n\t" \
    "global_load_lds_dwordx4 v67, s[48:49] offset:2048 sc1\n\t" \
    "global_load_lds_dwordx4 v67, s[48:49] offset:3072 sc1\n\t" \
    "s_add_u32 s48, s48, 4096\n\t" \
    "s_addc_u32 s49, s49, 0\n\t" \
    "s_mov_b32 m0, 69632\n\t" \
    "s_nop 0\n\t" \
    "global_load_lds_dwordx4 v67, s[48:49] sc1\n\t" \
    "global_load_lds_dwordx4 v67, s[48:49] offset:1024 sc1\n\t" \
    "global_load_lds_dwordx4 v67, s[48:49] offset:2048 sc1\n\t" \
    "global_load_lds_dwordx4 v67, s[48:49] offset:3072 sc1\n\t" \
    "s_add_u32 s48, s48, 4096\n\t" \
    "s_addc_u32 s49, s49, 0\n\t" \
    "s_mov_b32 m0, 73728\n\t" \
    "s_nop 0\n\t" \
    "global_load_lds_dwordx4 v67, s[48:49] sc1\n\t" \
    "global_load_lds_dwordx4 v67, s[48:49] offset:1024 sc1\n\t" \
    "global_load_lds_dwordx4 v67, s[48:49] offset:2048 sc1\n\t" \
    "global_load_lds_dwordx4 v67, s[48:49] offset:3072 sc1\n\t" \
    "s_add_u32 s48, s48, 4096\n\t" \
    "s_addc_u32 s49, s49, 0\n\t" \
    "s_mov_b32 m0, 77824\n\t" \
    "s_nop 0\n\t" \
    "global_load_lds_dwordx4 v67, s[48:49] sc1\n\t" \
    "global_load_lds_dwordx4 v67, s[48:49] offset:1024 sc1\n\t" \
    "global_load_lds_dwordx4 v67, s[48:49] offset:2048 sc1\n\t" \
    "global_load_lds_dwordx4 v67, s[48:49] offset:3072 sc1\n\t" \
    "s_add_u32 s48, s48, 4096\n\t" \
    "s_addc_u32 s49, s49, 0\n\t" \
    "s_mov_b32 m0, 81920\n\t" \
    "s_nop 0\n\t" \
    "global_load_lds_dwordx4 v67, s[48:49] sc1\n\t" \
    "global_load_lds_dwordx4 v67, s[48:49] offset:1024 sc1\n\t" \
    "global_load_lds_dwordx4 v67, s[48:49] offset:2048 sc1\n\t" \
    "global_load_lds_dwordx4 v67, s[48:49] offset:3072 sc1\n\t" \
    "s_add_u32 s48, s48, 4096\n\t" \
    "s_addc_u32 s49, s49, 0\n\t" \
    "s_mov_b32 m0, 86016\n\t" \
    "s_nop 0\n\t" \
    "global_load_lds_dwordx4 v67, s[48:49] sc1\n\t" \
    "global_load_lds_dwordx4 v67, s[48:49] offset:1024 sc1\n\t" \
    "global_load_lds_dwordx4 v67, s[48:49] offset:2048 sc1\n\t" \
    "global_load_lds_dwordx4 v67, s[48:49] offset:3072 sc1\n\t" \
    "s_add_u32 s48, s48, 4096\n\t" \
    "s_addc_u32 s49, s49, 0\n\t" \
    "s_mov_b32 m0, 90112\n\t" \
    "s_nop 0\n\t" \
    "global_load_lds_dwordx4 v67, s[48:49] sc1\n\t" \
    "global_load_lds_dwordx4 v67, s[48:49] offset:1024 sc1\n\t" \
    "global_load_lds_dwordx4 v67, s[48:49] offset:2048 sc1\n\t" \
    "global_load_lds_dwordx4 v67, s[48:49] offset:3072 sc1\n\t" \
    "s_add_u32 s48, s48, 4096\n\t" \
    "s_addc_u32 s49, s49, 0\n\t" \
    "s_mov_b32 m0, 94208\n\t" \
    "s_nop 0\n\t" \
    "global_load_lds_dwordx4 v67, s[48:49] sc1\n\t" \
    "global_load_lds_dwordx4 v67, s[48:49] offset:1024 sc1\n\t" \
    "s_waitcnt vmcnt(30)\n\t" \
    "global_load_lds_dwordx4 v67, s[48:49] offset:2048 sc1\n\t" \
    "global_load_lds_dwordx4 v67, s[48:49] offset:3072 sc1\n\t" \
    "s_add_u32 s48, s48, 4096\n\t" \
    "s_addc_u32 s49, s49, 0\n\t" \
    "s_mov_b32 m0, 98304\n\t" \
    "s_nop 0\n\t" \
    "global_load_lds_dwordx4 v67, s[48:49] sc1\n\t" \
    "global_load_lds_dwordx4 v67, s[48:49] offset:1024 sc1\n\t" \
    "global_load_lds_dwordx4 v67, s[48:49] offset:2048 sc1\n\t" \
    "global_load_lds_dwordx4 v67, s[48:49] offset:3072 sc1\n\t" \
    "s_add_u32 s48, s48, 4096\n\t" \
    "s_addc_u32 s49, s49, 0\n\t" \
    "s_mov_b32 m0, 102400\n\t" \
    "s_nop 0\n\t" \
    "global_load_lds_dwordx4 v67, s[48:49] sc1\n\t" \
    "global_load_lds_dwordx4 v67, s[48:49] offset:1024 sc1\n\t" \
    "global_load_lds_dwordx4 v67, s[48:49] offset:2048 sc1\n\t" \
    "global_load_lds_dwordx4 v67, s[48:49] offset:3072 sc1\n\t" \
    "s_add_u32 s48, s48, 4096\n\t" \
    "s_addc_u32 s49, s49, 0\n\t" \
    "s_mov_b32 m0, 106496\n\t" \
    "s_nop 0\n\t" \
    "global_load_lds_dwordx4 v67, s[48:49] sc1\n\t" \
    "global_load_lds_dwordx4 v67, s[48:49] offset:1024 sc1\n\t" \
    "global_load_lds_dwordx4 v67, s[48:49] offset:2048 sc1\n\t" \
    "global_load_lds_dwordx4 v67, s[48:49] offset:3072 sc1\n\t" \
    "s_add_u32 s48, s48, 4096\n\t" \
    "s_addc_u32 s49, s49, 0\n\t" \
    "s_mov_b32 m0, 110592\n\t" \
    "s_nop 0\n\t" \
    "global_load_lds_dwordx4 v67, s[48:49] sc1\n\t" \
    "global_load_lds_dwordx4 v67, s[48:49] offset:1024 sc1\n\t" \
    "global_load_lds_dwordx4 v67, s[48:49] offset:2048 sc1\n\t" \
    "global_load_lds_dwordx4 v67, s[48:49] offset:3072 sc1\n\t" \
    "s_add_u32 s48, s48, 4096\n\t" \
    "s_addc_u32 s49, s49, 0\n\t" \
    "s_mov_b32 m0, 114688\n\t" \
    "s_nop 0\n\t" \
    "global_load_lds_dwordx4 v67, s[48:49] sc1\n\t" \
    "global_load_lds_dwordx4 v67, s[48:49] offset:1024 sc1\n\t" \
    "global_load_lds_dwordx4 v67, s[48:49] offset:2048 sc1\n\t" \
    "global_load_lds_dwordx4 v67, s[48:49] offset:3072 sc1\n\t" \
    "s_add_u32 s48, s48, 4096\n\t" \
    "s_addc_u32 s49, s49, 0\n\t" \
    "s_mov_b32 m0, 118784\n\t" \
    "s_nop 0\n\t" \
    "global_load_lds_dwordx4 v67, s[48:49] sc1\n\t" \
    "global_load_lds_dwordx4 v67, s[48:49] offset:1024 sc1\n\t" \
    "global_load_lds_dwordx4 v67, s[48:49] offset:2048 sc1\n\t" \
    "global_load_lds_dwordx4 v67, s[48:49] offset:3072 sc1\n\t" \
    "s_add_u32 s48, s48, 4096\n\t" \
    "s_addc_u32 s49, s49, 0\n\t" \
    "s_mov_b32 m0, 122880\n\t" \
    "s_nop 0\n\t" \
    "global_load_lds_dwordx4 v67, s[48:49] sc1\n\t" \
    "global_load_lds_dwordx4 v67, s[48:49] offset:1024 sc1\n\t" \
    "global_load_lds_dwordx4 v67, s[48:49] offset:2048 sc1\n\t" \
    "global_load_lds_dwordx4 v67, s[48:49] offset:3072 sc1\n\t" \
    "s_barrier\n\t" \
    "s_add_u32 s48, s48, 4096\n\t" \
    "s_addc_u32 s49, s49, 0\n\t" \
    "s_mov_b32 m0, 126976\n\t" \
    "s_nop 0\n\t" \
    "global_load_lds_dwordx4 v67, s[48:49] sc1\n\t" \
    "global_load_lds_dwordx4 v67, s[48:49] offset:1024 sc1\n\t" \
    "s_waitcnt vmcnt(54)\n\t" \
    "global_load_lds_dwordx4 v67, s[48:49] offset:2048 sc1\n\t" \
    "global_load_lds_dwordx4 v67, s[48:49] offset:3072 sc1\n\t" \
    "s_waitcnt vmcnt(32)\n\t" \
    "s_add_u32 s60, s60, 86016\n\t" \
    "s_addc_u32 s61, s61, 0\n\t" \
    "s_mov_b32 m0, 131072\n\t" \
    "s_nop 0\n\t" \
    "global_load_lds_dwordx4 v67, s[60:61] sc1\n\t" \
    "global_load_lds_dwordx4 v67, s[60:61] offset:1024 sc1\n\t" \
    "global_load_lds_dwordx4 v67, s[60:61] offset:2048 sc1\n\t" \
    "global_load_lds_dwordx4 v67, s[60:61] offset:3072 sc1\n\t" \
    "s_add_u32 s60, s60, 4096\n\t" \
    "s_addc_u32 s61, s61, 0\n\t" \
    "s_mov_b32 m0, 135168\n\t" \
    "s_nop 0\n\t" \
    "global_load_lds_dwordx4 v67, s[60:61] sc1\n\t" \
    "global_load_lds_dwordx4 v67, s[60:61] offset:1024 sc1\n\t" \
    "global_load_lds_dwordx4 v67, s[60:61] offset:2048 sc1\n\t" \
    "global_load_lds_dwordx4 v67, s[60:61] offset:3072 sc1\n\t" \
    "s_add_u32 s60, s60, 4096\n\t" \
    "s_addc_u32 s61, s61, 0\n\t" \
    "s_mov_b32 m0, 139264\n\t" \
    "s_nop 0\n\t" \
    "global_load_lds_dwordx4 v67, s[60:61] sc1\n\t" \
    "global_load_lds_dwordx4 v67, s[60:61] offset:1024 sc1\n\t" \
    "global_load_lds_dwordx4 v67, s[60:61] offset:2048 sc1\n\t" \
    "global_load_lds_dwordx4 v67, s[60:61] offset:3072 sc1\n\t" \
    "s_add_u32 s60, s60, 4096\n\t" \
    "s_addc_u32 s61, s61, 0\n\t" \
    "s_mov_b32 m0, 143360\n\t" \
    "s_nop 0\n\t" \
    "global_load_lds_dwordx4 v67, s[60:61] sc1\n\t" \
    "global_load_lds_dwordx4 v67, s[60:61] offset:1024 sc1\n\t" \
    "global_load_lds_dwordx4 v67, s[60:61] offset:2048 sc1\n\t" \
    "global_load_lds_dwordx4 v67, s[60:61] offset:3072 sc1\n\t" \
    "s_add_u32 s60, s60, 102400\n\t" \
    "s_addc_u32 s61, s61, 0\n\t" \
    "s_mov_b32 m0, 147456\n\t" \
    "s_nop 0\n\t" \
    "global_load_lds_dwordx4 v67, s[60:61] sc1\n\t" \
    "global_load_lds_dwordx4 v67, s[60:61] offset:1024 sc1\n\t" \
    "global_load_lds_dwordx4 v67, s[60:61] offset:2048 sc1\n\t" \
    "global_load_lds_dwordx4 v67, s[60:61] offset:3072 sc1\n\t" \
    "s_add_u32 s60, s60, 4096\n\t" \
    "s_addc_u32 s61, s61, 0\n\t" \
    "s_mov_b32 m0, 151552\n\t" \
    "s_nop 0\n\t" \
    "global_load_lds_dwordx4 v67, s[60:61] sc1\n\t" \
    "global_load_lds_dwordx4 v67, s[60:61] offset:1024 sc1\n\t" \
    "global_load_lds_dwordx4 v67, s[60:61] offset:2048 sc1\n\t" \
    "global_load_lds_dwordx4 v67, s[60:61] offset:3072 sc1\n\t" \
    "s_add_u32 s60, s60, 4096\n\t" \
    "s_addc_u32 s61, s61, 0\n\t" \
    "s_mov_b32 m0, 155648\n\t" \
    "s_nop 0\n\t" \
    "global_load_lds_dwordx4 v67, s[60:61] sc1\n\t" \
    "global_load_lds_dwordx4 v67, s[60:61] offset:1024 sc1\n\t" \
    "global_load_lds_dwordx4 v67, s[60:61] offset:2048 sc1\n\t" \
    "global_load_lds_dwordx4 v67, s[60:61] offset:3072 sc1\n\t" \
    "s_add_u32 s60, s60, 4096\n\t" \
    "s_addc_u32 s61, s61, 0\n\t" \
    "s_mov_b32 m0, 159744\n\t" \
    "s_nop 0\n\t" \
    "global_load_lds_dwordx4 v67, s[60:61] sc1\n\t" \
    "global_load_lds_dwordx4 v67, s[60:61] offset:1024 sc1\n\t" \
    "s_waitcnt vmcnt(54)\n\t" \
    "global_load_lds_dwordx4 v67, s[60:61] offset:2048 sc1\n\t" \
    "global_load_lds_dwordx4 v67, s[60:61] offset:3072 sc1\n\t" \
    "s_barrier\n\t" \
    "global_load_dwordx4 a[128:131], v62, s[62:63] sc1\n\t" \
    "global_load_dwordx4 a[132:135], v62, s[64:65] sc1\n\t" \
    "global_load_dwordx4 a[136:139], v62, s[62:63] offset:1024 sc1\n\t" \
    "global_load_dwordx4 a[140:143], v62, s[64:65] offset:1024 sc1\n\t" \
    "global_load_dwordx4 a[144:147], v62, s[62:63] offset:2048 sc1\n\t" \
    "global_load_dwordx4 a[148:151], v62, s[64:65] offset:2048 sc1\n\t" \
    "s_waitcnt vmcnt(54)\n\t" \
    "global_load_dwordx4 a[152:155], v62, s[62:63] offset:3072 sc1\n\t" \
    "global_load_dwordx4 a[156:159], v62, s[64:65] offset:3072 sc1\n\t" \
    "s_add_u32 s62, s62, 8192\n\t" \
    "s_addc_u32 s63, s63, 0\n\t" \
    "global_load_dwordx4 a[160:163], v62, s[62:63] offset:-4096 sc1\n\t" \
    "s_add_u32 s64, s64, 8192\n\t" \
    "s_addc_u32 s65, s65, 0\n\t" \
    "global_load_dwordx4 a[164:167], v62, s[64:65] offset:-4096 sc1\n\t" \
    "global_load_dwordx4 a[168:171], v62, s[62:63] offset:-3072 sc1\n\t" \
    "global_load_dwordx4 a[172:175], v62, s[64:65] offset:-3072 sc1\n\t" \
    "global_load_dwordx4 a[176:179], v62, s[62:63] offset:-2048 sc1\n\t" \
    "global_load_dwordx4 a[180:183], v62, s[64:65] offset:-2048 sc1\n\t" \
    "s_waitcnt vmcnt(54)\n\t" \
    "global_load_dwordx4 a[120:123], v62, s[62:63] offset:-1024 sc1\n\t" \
    "global_load_dwordx4 a[124:127], v62, s[64:65] offset:-1024 sc1\n\t" \
    "s_waitcnt vmcnt(48)\n\t" \
    "s_barrier\n\t" \
    "s_sub_u32 s60, s60, 212992\n\t" \
    "s_subb_u32 s61, s61, 0\n\t" \
    "s_mov_b32 m0, 0\n\t" \
    "s_nop 0\n\t" \
    "global_load_lds_dwordx4 v67, s[60:61] sc1\n\t" \
    "global_load_lds_dwordx4 v67, s[60:61] offset:1024 sc1\n\t" \
    "global_load_lds_dwordx4 v67, s[60:61] offset:2048 sc1\n\t" \
    "global_load_lds_dwordx4 v67, s[60:61] offset:3072 sc1\n\t" \
    "s_add_u32 s60, s60, 4096\n\t" \
    "s_addc_u32 s61, s61, 0\n\t" \
    "s_mov_b32 m0, 4096\n\t" \
    "s_nop 0\n\t" \
    "global_load_lds_dwordx4 v67, s[60:61] sc1\n\t" \
    "global_load_lds_dwordx4 v67, s[60:61] offset:1024 sc1\n\t" \
    "global_load_lds_dwordx4 v67, s[60:61] offset:2048 sc1\n\t" \
    "global_load_lds_dwordx4 v67, s[60:61] offset:3072 sc1\n\t" \
    "s_add_u32 s60, s60, 4096\n\t" \
    "s_addc_u32 s61, s61, 0\n\t" \
    "s_mov_b32 m0, 8192\n\t" \
    "s_nop 0\n\t" \
    "global_load_lds_dwordx4 v67, s[60:61] sc1\n\t" \
    "global_load_lds_dwordx4 v67, s[60:61] offset:1024 sc1\n\t" \
    "global_load_lds_dwordx4 v67, s[60:61] offset:2048 sc1\n\t" \
    "global_load_lds_dwordx4 v67, s[60:61] offset:3072 sc1\n\t" \
    "s_add_u32 s60, s60, 4096\n\t" \
    "s_addc_u32 s61, s61, 0\n\t" \
    "s_mov_b32 m0, 12288\n\t" \
    "s_nop 0\n\t" \
    "global_load_lds_dwordx4 v67, s[60:61] sc1\n\t" \
    "global_load_lds_dwordx4 v67, s[60:61] offset:1024 sc1\n\t" \
    "s_waitcnt vmcnt(54)\n\t" \
    "global_load_lds_dwordx4 v67, s[60:61] offset:2048 sc1\n\t" \
    "global_load_lds_dwordx4 v67, s[60:61] offset:3072 sc1\n\t" \
    "s_barrier\n\t" \
    "s_add_u32 s60, s60, 102400\n\t" \
    "s_addc_u32 s61, s61, 0\n\t" \
    "s_mov_b32 m0, 16384\n\t" \
    "s_nop 0\n\t" \
    "global_load_lds_dwordx4 v67, s[60:61] sc1\n\t" \
    "global_load_lds_dwordx4 v67, s[60:61] offset:1024 sc1\n\t" \
    "global_load_lds_dwordx4 v67, s[60:61] offset:2048 sc1\n\t" \
    "global_load_lds_dwordx4 v67, s[60:61] offset:3072 sc1\n\t" \
    "s_add_u32 s60, s60, 4096\n\t" \
    "s_addc_u32 s61, s61, 0\n\t" \
    "s_mov_b32 m0, 20480\n\t" \
    "s_nop 0\n\t" \
    "global_load_lds_dwordx4 v67, s[60:61] sc1\n\t" \
    "global_load_lds_dwordx4 v67, s[60:61] offset:1024 sc1\n\t" \
    "s_waitcnt vmcnt(54)\n\t" \
    "global_load_lds_dwordx4 v67, s[60:61] offset:2048 sc1\n\t" \
    "global_load_lds_dwordx4 v67, s[60:61] offset:3072 sc1\n\t" \
    "s_add_u32 s60, s60, 4096\n\t" \
    "s_addc_u32 s61, s61, 0\n\t" \
    "s_mov_b32 m0, 24576\n\t" \
    "s_nop 0\n\t" \
    "global_load_lds_dwordx4 v67, s[60:61] sc1\n\t" \
    "global_load_lds_dwordx4 v67, s[60:61] offset:1024 sc1\n\t" \
    "global_load_lds_dwordx4 v67, s[60:61] offset:2048 sc1\n\t" \
    "global_load_lds_dwordx4 v67, s[60:61] offset:3072 sc1\n\t" \
    "s_add_u32 s60, s60, 4096\n\t" \
    "s_addc_u32 s61, s61, 0\n\t" \
    "s_mov_b32 m0, 28672\n\t" \
    "s_nop 0\n\t" \
    "global_load_lds_dwordx4 v67, s[60:61] sc1\n\t" \
    "global_load_lds_dwordx4 v67, s[60:61] offset:1024 sc1\n\t" \
    "s_waitcnt vmcnt(54)\n\t" \
    "global_load_lds_dwordx4 v67, s[60:61] offset:2048 sc1\n\t" \
    "global_load_lds_dwordx4 v67, s[60:61] offset:3072 sc1\n\t" \
    "s_sub_u32 s60, s60, 98304\n\t" \
    "s_subb_u32 s61, s61, 0\n\t" \
    "s_mov_b32 m0, 32768\n\t" \
    "s_nop 0\n\t" \
    "global_load_lds_dwordx4 v67, s[60:61] sc1\n\t" \
    "global_load_lds_dwordx4 v67, s[60:61] offset:1024 sc1\n\t" \
    "global_load_lds_dwordx4 v67, s[60:61] offset:2048 sc1\n\t" \
    "global_load_lds_dwordx4 v67, s[60:61] offset:3072 sc1\n\t" \
    "s_add_u32 s60, s60, 4096\n\t" \
    "s_addc_u32 s61, s61, 0\n\t" \
    "s_mov_b32 m0, 36864\n\t" \
    "s_nop 0\n\t" \
    "global_load_lds_dwordx4 v67, s[60:61] sc1\n\t" \
    "global_load_lds_dwordx4 v67, s[60:61] offset:1024 sc1\n\t" \
    "s_waitcnt vmcnt(54)\n\t" \
    "global_load_lds_dwordx4 v67, s[60:61] offset:2048 sc1\n\t" \
    "global_load_lds_dwordx4 v67, s[60:61] offset:3072 sc1\n\t" \
    "s_add_u32 s60, s60, 4096\n\t" \
    "s_addc_u32 s61, s61, 0\n\t" \
    "s_mov_b32 m0, 40960\n\t" \
    "s_nop 0\n\t" \
    "global_load_lds_dwordx4 v67, s[60:61] sc1\n\t" \
    "global_load_lds_dwordx4 v67, s[60:61] offset:1024 sc1\n\t" \
    "global_load_lds_dwordx4 v67, s[60:61] offset:2048 sc1\n\t" \
    "global_load_lds_dwordx4 v67, s[60:61] offset:3072 sc1\n\t" \
    "s_add_u32 s60, s60, 4096\n\t" \
    "s_addc_u32 s61, s61, 0\n\t" \
    "s_mov_b32 m0, 45056\n\t" \
    "s_nop 0\n\t" \
    "global_load_lds_dwordx4 v67, s[60:61] sc1\n\t" \
    "global_load_lds_dwordx4 v67, s[60:61] offset:1024 sc1\n\t" \
    "s_waitcnt vmcnt(54)\n\t" \
    "global_load_lds_dwordx4 v67, s[60:61] offset:2048 sc1\n\t" \
    "global_load_lds_dwordx4 v67, s[60:61] offset:3072 sc1\n\t" \
    "s_add_u32 s60, s60, 102400\n\t" \
    "s_addc_u32 s61, s61, 0\n\t" \
    "s_mov_b32 m0, 49152\n\t" \
    "s_nop 0\n\t" \
    "global_load_lds_dwordx4 v67, s[60:61] sc1\n\t" \
    "global_load_lds_dwordx4 v67, s[60:61] offset:1024 sc1\n\t" \
    "global_load_lds_dwordx4 v67, s[60:61] offset:2048 sc1\n\t" \
    "global_load_lds_dwordx4 v67, s[60:61] offset:3072 sc1\n\t" \
    "s_add_u32 s60, s60, 4096\n\t" \
    "s_addc_u32 s61, s61, 0\n\t" \
    "s_mov_b32 m0, 53248\n\t" \
    "s_nop 0\n\t" \
    "global_load_lds_dwordx4 v67, s[60:61] sc1\n\t" \
    "global_load_lds_dwordx4 v67, s[60:61] offset:1024 sc1\n\t" \
    "s_waitcnt vmcnt(54)\n\t" \
    "global_load_lds_dwordx4 v67, s[60:61] offset:2048 sc1\n\t" \
    "global_load_lds_dwordx4 v67, s[60:61] offset:3072 sc1\n\t" \
    "s_add_u32 s60, s60, 4096\n\t" \
    "s_addc_u32 s61, s61, 0\n\t" \
    "s_mov_b32 m0, 57344\n\t" \
    "s_nop 0\n\t" \
    "global_load_lds_dwordx4 v67, s[60:61] sc1\n\t" \
    "global_load_lds_dwordx4 v67, s[60:61] offset:1024 sc1\n\t" \
    "global_load_lds_dwordx4 v67, s[60:61] offset:2048 sc1\n\t" \
    "global_load_lds_dwordx4 v67, s[60:61] offset:3072 sc1\n\t" \
    "s_add_u32 s60, s60, 4096\n\t" \
    "s_addc_u32 s61, s61, 0\n\t" \
    "s_mov_b32 m0, 61440\n\t" \
    "s_nop 0\n\t" \
    "global_load_lds_dwordx4 v67, s[60:61] sc1\n\t" \
    "global_load_lds_dwordx4 v67, s[60:61] offset:1024 sc1\n\t" \
    "s_waitcnt vmcnt(54)\n\t" \
    "global_load_lds_dwordx4 v67, s[60:61] offset:2048 sc1\n\t" \
    "global_load_lds_dwordx4 v67, s[60:61] offset:3072 sc1\n\t" \
    "s_sub_u32 s60, s60, 98304\n\t" \
    "s_subb_u32 s61, s61, 0\n\t" \
    "s_mov_b32 m0, 65536\n\t" \
    "s_nop 0\n\t" \
    "global_load_lds_dwordx4 v67, s[60:61] sc1\n\t" \
    "global_load_lds_dwordx4 v67, s[60:61] offset:1024 sc1\n\t" \
    "global_load_lds_dwordx4 v67, s[60:61] offset:2048 sc1\n\t" \
    "global_load_lds_dwordx4 v67, s[60:61] offset:3072 sc1\n\t" \
    "s_add_u32 s60, s60, 4096\n\t" \
    "s_addc_u32 s61, s61, 0\n\t" \
    "s_mov_b32 m0, 69632\n\t" \
    "s_nop 0\n\t" \
    "global_load_lds_dwordx4 v67, s[60:61] sc1\n\t" \
    "global_load_lds_dwordx4 v67, s[60:61] offset:1024 sc1\n\t" \
    "s_waitcnt vmcnt(54)\n\t" \
    "global_load_lds_dwordx4 v67, s[60:61] offset:2048 sc1\n\t" \
    "global_load_lds_dwordx4 v67, s[60:61] offset:3072 sc1\n\t" \
    "s_add_u32 s60, s60, 4096\n\t" \
    "s_addc_u32 s61, s61, 0\n\t" \
    "s_mov_b32 m0, 73728\n\t" \
    "s_nop 0\n\t" \
    "global_load_lds_dwordx4 v67, s[60:61] sc1\n\t" \
    "global_load_lds_dwordx4 v67, s[60:61] offset:1024 sc1\n\t" \
    "global_load_lds_dwordx4 v67, s[60:61] offset:2048 sc1\n\t" \
    "global_load_lds_dwordx4 v67, s[60:61] offset:3072 sc1\n\t" \
    "s_add_u32 s60, s60, 4096\n\t" \
    "s_addc_u32 s61, s61, 0\n\t" \
    "s_mov_b32 m0, 77824\n\t" \
    "s_nop 0\n\t" \
    "global_load_lds_dwordx4 v67, s[60:61] sc1\n\t" \
    "global_load_lds_dwordx4 v67, s[60:61] offset:1024 sc1\n\t" \
    "s_waitcnt vmcnt(54)\n\t" \
    "global_load_lds_dwordx4 v67, s[60:61] offset:2048 sc1\n\t" \
    "global_load_lds_dwordx4 v67, s[60:61] offset:3072 sc1\n\t" \
    "s_add_u32 s60, s60, 102400\n\t" \
    "s_addc_u32 s61, s61, 0\n\t" \
    "s_mov_b32 m0, 81920\n\t" \
    "s_nop 0\n\t" \
    "global_load_lds_dwordx4 v67, s[60:61] sc1\n\t" \
    "global_load_lds_dwordx4 v67, s[60:61] offset:1024 sc1\n\t" \
    "global_load_lds_dwordx4 v67, s[60:61] offset:2048 sc1\n\t" \
    "global_load_lds_dwordx4 v67, s[60:61] offset:3072 sc1\n\t" \
    "s_add_u32 s60, s60, 4096\n\t" \
    "s_addc_u32 s61, s61, 0\n\t" \
    "s_mov_b32 m0, 86016\n\t" \
    "s_nop 0\n\t" \
    "global_load_lds_dwordx4 v67, s[60:61] sc1\n\t" \
    "global_load_lds_dwordx4 v67, s[60:61] offset:1024 sc1\n\t" \
    "s_waitcnt vmcnt(54)\n\t" \
    "global_load_lds_dwordx4 v67, s[60:61] offset:2048 sc1\n\t" \
    "global_load_lds_dwordx4 v67, s[60:61] offset:3072 sc1\n\t" \
    "s_add_u32 s60, s60, 4096\n\t" \
    "s_addc_u32 s61, s61, 0\n\t" \
    "s_mov_b32 m0, 90112\n\t" \
    "s_nop 0\n\t" \
    "global_load_lds_dwordx4 v67, s[60:61] sc1\n\t" \
    "global_load_lds_dwordx4 v67, s[60:61] offset:1024 sc1\n\t" \
    "global_load_lds_dwordx4 v67, s[60:61] offset:2048 sc1\n\t" \
    "global_load_lds_dwordx4 v67, s[60:61] offset:3072 sc1\n\t" \
    "s_add_u32 s60, s60, 4096\n\t" \
    "s_addc_u32 s61, s61, 0\n\t" \
    "s_mov_b32 m0, 94208\n\t" \
    "s_nop 0\n\t" \
    "global_load_lds_dwordx4 v67, s[60:61] sc1\n\t" \
    "global_load_lds_dwordx4 v67, s[60:61] offset:1024 sc1\n\t" \
    "s_waitcnt vmcnt(54)\n\t" \
    "global_load_lds_dwordx4 v67, s[60:61] offset:2048 sc1\n\t" \
    "global_load_lds_dwordx4 v67, s[60:61] offset:3072 sc1\n\t" \
    "s_waitcnt vmcnt(0)\n\t" \
    "s_barrier\n\t" \
    "s_sub_u32 s60, s60, 167936\n\t" \
    "s_subb_u32 s61, s61, 0\n\t" \
    "s_mov_b32 m0, 98304\n\t" \
    "s_nop 0\n\t" \
    "global_load_lds_dwordx4 v67, s[60:61] sc1\n\t" \
    "global_load_lds_dwordx4 v67, s[60:61] offset:1024 sc1\n\t" \
    "global_load_lds_dwordx4 v67, s[60:61] offset:2048 sc1\n\t" \
    "global_load_lds_dwordx4 v67, s[60:61] offset:3072 sc1\n\t" \
    "s_add_u32 s60, s60, 114688\n\t" \
    "s_addc_u32 s61, s61, 0\n\t" \
    "s_mov_b32 m0, 102400\n\t" \
    "s_nop 0\n\t" \
    "global_load_lds_dwordx4 v67, s[60:61] sc1\n\t" \
    "global_load_lds_dwordx4 v67, s[60:61] offset:1024 sc1\n\t" \
    "global_load_lds_dwordx4 v67, s[60:61] offset:2048 sc1\n\t" \
    "global_load_lds_dwordx4 v67, s[60:61] offset:3072 sc1\n\t" \
    "s_sub_u32 s60, s60, 86016\n\t" \
    "s_subb_u32 s61, s61, 0\n\t" \
    "s_mov_b32 m0, 106496\n\t" \
    "s_nop 0\n\t" \
    "global_load_lds_dwordx4 v67, s[60:61] sc1\n\t" \
    "global_load_lds_dwordx4 v67, s[60:61] offset:1024 sc1\n\t" \
    "global_load_lds_dwordx4 v67, s[60:61] offset:2048 sc1\n\t" \
    "global_load_lds_dwordx4 v67, s[60:61] offset:3072 sc1\n\t" \
    "s_add_u32 s60, s60, 114688\n\t" \
    "s_addc_u32 s61, s61, 0\n\t" \
    "s_mov_b32 m0, 110592\n\t" \
    "s_nop 0\n\t" \
    "global_load_lds_dwordx4 v67, s[60:61] sc1\n\t" \
    "global_load_lds_dwordx4 v67, s[60:61] offset:1024 sc1\n\t" \
    "global_load_lds_dwordx4 v67, s[60:61] offset:2048 sc1\n\t" \
    "global_load_lds_dwordx4 v67, s[60:61] offset:3072 sc1\n\t" \
    "s_sub_u32 s60, s60, 86016\n\t" \
    "s_subb_u32 s61, s61, 0\n\t" \
    "s_mov_b32 m0, 114688\n\t" \
    "s_nop 0\n\t" \
    "global_load_lds_dwordx4 v67, s[60:61] sc1\n\t" \
    "global_load_lds_dwordx4 v67, s[60:61] offset:1024 sc1\n\t" \
    "global_load_lds_dwordx4 v67, s[60:61] offset:2048 sc1\n\t" \
    "global_load_lds_dwordx4 v67, s[60:61] offset:3072 sc1\n\t" \
    "s_add_u32 s60, s60, 114688\n\t" \
    "s_addc_u32 s61, s61, 0\n\t" \
    "s_mov_b32 m0, 118784\n\t" \
    "s_nop 0\n\t" \
    "global_load_lds_dwordx4 v67, s[60:61] sc1\n\t" \
    "global_load_lds_dwordx4 v67, s[60:61] offset:1024 sc1\n\t" \
    "global_load_lds_dwordx4 v67, s[60:61] offset:2048 sc1\n\t" \
    "global_load_lds_dwordx4 v67, s[60:61] offset:3072 sc1\n\t" \
    "s_sub_u32 s60, s60, 86016\n\t" \
    "s_subb_u32 s61, s61, 0\n\t" \
    "s_mov_b32 m0, 122880\n\t" \
    "s_nop 0\n\t" \
    "global_load_lds_dwordx4 v67, s[60:61] sc1\n\t" \
    "global_load_lds_dwordx4 v67, s[60:61] offset:1024 sc1\n\t" \
    "global_load_lds_dwordx4 v67, s[60:61] offset:2048 sc1\n\t" \
    "global_load_lds_dwordx4 v67, s[60:61] offset:3072 sc1\n\t" \
    "s_add_u32 s60, s60, 114688\n\t" \
    "s_addc_u32 s61, s61, 0\n\t" \
    "s_mov_b32 m0, 126976\n\t" \
    "s_nop 0\n\t" \
    "global_load_lds_dwordx4 v67, s[60:61] sc1\n\t" \
    "global_load_lds_dwordx4 v67, s[60:61] offset:1024 sc1\n\t" \
    "global_load_lds_dwordx4 v67, s[60:61] offset:2048 sc1\n\t" \
    "global_load_lds_dwordx4 v67, s[60:61] offset:3072 sc1\n\t" \
    "s_waitcnt vmcnt(0)\n\t" \
    "s_setprio 0\n\t" \
    "s_barrier\n\t" \
    "s_branch .Llate2_%=\n\t" \
    ".Ldown_e_%=:\n\t" \
    "s_and_b32 s44, s43, 3\n\t" \
    "s_lshr_b32 s45, s44, 1\n\t" \
    "s_lshl_b32 s46, s44, 1\n\t" \
    "s_add_u32 s46, s46, s45\n\t" \
    "s_cmp_eq_u32 s44, 3\n\t" \
    "s_cselect_b32 s44, 32768, 0\n\t" \
    "s_lshl_b32 s45, s46, 14\n\t" \
    "s_add_u32 s45, s45, s44\n\t" \
    "v_add_u32 v64, s45, v62\n\t" \
    "v_xor_b32 v65, 0x4000, v64\n\t" \
    "s_lshl_b32 s45, s46, 12\n\t" \
    "s_add_u32 s45, s45, 98304\n\t" \
    "v_add_u32 v66, s45, v62\n\t" \
    "ds_read_b128 v[32:35], v64 offset:0\n\t" \
    "ds_read_b128 v[36:39], v65 offset:0\n\t" \
    "ds_read_b128 v[40:43], v64 offset:1024\n\t" \
    "ds_read_b128 v[44:47], v65 offset:1024\n\t" \
    "ds_read_b128 v[48:51], v64 offset:2048\n\t" \
    "ds_read_b128 v[52:55], v65 offset:2048\n\t" \
    "ds_read_b128 v[56:59], v64 offset:3072\n\t" \
    "ds_read_b128 v[60:63], v65 offset:3072\n\t" \
    "s_waitcnt vmcnt(10) lgkmcnt(7)\n\t" \
    "v_mfma_f32_32x32x16_bf16 v[0:15], v[32:35], a[0:3], 0\n\t" \
    "ds_read_b128 v[32:35], v64 offset:4096\n\t" \
    "global_load_dwordx4 a[40:43], v67, s[66:67] offset:2048 sc1\n\t" \
    "s_waitcnt lgkmcnt(7)\n\t" \
    "v_mfma_f32_32x32x16_bf16 v[16:31], v[36:39], a[0:3], 0\n\t" \
    "ds_read_b128 v[36:39], v65 offset:4096\n\t" \
    "global_load_dwordx4 a[44:47], v67, s[66:67] offset:3072 sc1\n\t" \
    "s_waitcnt vmcnt(11) lgkmcnt(7)\n\t" \
    "v_mfma_f32_32x32x16_bf16 v[0:15], v[40:43], a[4:7], v[0:15]\n\t" \
    "ds_read_b128 v[40:43], v64 offset:5120\n\t" \
    "s_add_u32 s66, s66, 8192\n\t" \
    "s_addc_u32 s67, s67, 0\n\t" \
    "global_load_dwordx4 a[48:51], v67, s[66:67] offset:-4096 sc1\n\t" \
    "s_waitcnt lgkmcnt(7)\n\t" \
    "v_mfma_f32_32x32x16_bf16 v[16:31], v[44:47], a[4:7], v[16:31]\n\t" \
    "ds_read_b128 v[44:47], v65 offset:5120\n\t" \
    "global_load_dwordx4 a[52:55], v67, s[66:67] offset:-3072 sc1\n\t" \
    "s_waitcnt vmcnt(12) lgkmcnt(7)\n\t" \
    "v_mfma_f32_32x32x16_bf16 v[0:15], v[48:51], a[8:11], v[0:15]\n\t" \
    "ds_read_b128 v[48:51], v64 offset:6144\n\t" \
    "global_load_dwordx4 a[56:59], v67, s[66:67] offset:-2048 sc1\n\t" \
    "s_waitcnt lgkmcnt(7)\n\t" \
    "v_mfma_f32_32x32x16_bf16 v[16:31], v[52:55], a[8:11], v[16:31]\n\t" \
    "ds_read_b128 v[52:55], v65 offset:6144\n\t" \
    "global_load_dwordx4 a[60:63], v67, s[66:67] offset:-1024 sc1\n\t" \
    "s_waitcnt vmcnt(13) lgkmcnt(7)\n\t" \
    "v_mfma_f32_32x32x16_bf16 v[0:15], v[56:59], a[12:15], v[0:15]\n\t" \
    "ds_read_b128 v[56:59], v64 offset:7168\n\t" \
    "global_load_dwordx4 a[64:67], v67, s[66:67] sc1\n\t" \
    "s_waitcnt lgkmcnt(7)\n\t" \
    "v_mfma_f32_32x32x16_bf16 v[16:31], v[60:63], a[12:15], v[16:31]\n\t" \
    "ds_read_b128 v[60:63], v65 offset:7168\n\t" \
    "global_load_dwordx4 a[68:71], v67, s[66:67] offset:1024 sc1\n\t" \
    "s_waitcnt vmcnt(14) lgkmcnt(7)\n\t" \
    "v_mfma_f32_32x32x16_bf16 v[0:15], v[32:35], a[16:19], v[0:15]\n\t" \
    "ds_read_b128 v[32:35], v64 offset:8192\n\t" \
    "global_load_dwordx4 a[72:75], v67, s[66:67] offset:2048 sc1\n\t" \
    "s_waitcnt lgkmcnt(7)\n\t" \
    "v_mfma_f32_32x32x16_bf16 v[16:31], v[36:39], a[16:19], v[16:31]\n\t" \
    "ds_read_b128 v[36:39], v65 offset:8192\n\t" \
    "global_load_dwordx4 a[76:79], v67, s[66:67] offset:3072 sc1\n\t" \
    "s_waitcnt vmcnt(15) lgkmcnt(7)\n\t" \
    "v_mfma_f32_32x32x16_bf16 v[0:15], v[40:43], a[20:23], v[0:15]\n\t" \
    "ds_read_b128 v[40:43], v64 offset:9216\n\t" \
    "s_add_u32 s66, s66, 8192\n\t" \
    "s_addc_u32 s67, s67, 0\n\t" \
    "global_load_dwordx4 a[80:83], v67, s[66:67] offset:-4096 sc1\n\t" \
    "s_waitcnt lgkmcnt(7)\n\t" \
    "v_mfma_f32_32x32x16_bf16 v[16:31], v[44:47], a[20:23], v[16:31]\n\t" \
    "ds_read_b128 v[44:47], v65 offset:9216\n\t" \
    "global_load_dwordx4 a[84:87], v67, s[66:67] offset:-3072 sc1\n\t" \
    "s_waitcnt vmcnt(16) lgkmcnt(7)\n\t" \
    "v_mfma_f32_32x32x16_bf16 v[0:15], v[48:51], a[24:27], v[0:15]\n\t" \
    "ds_read_b128 v[48:51], v64 offset:10240\n\t" \
    "global_load_dwordx4 a[88:91], v67, s[66:67] offset:-2048 sc1\n\t" \
    "s_waitcnt lgkmcnt(7)\n\t" \
    "v_mfma_f32_32x32x16_bf16 v[16:31], v[52:55], a[24:27], v[16:31]\n\t" \
    "ds_read_b128 v[52:55], v65 offset:10240\n\t" \
    "s_waitcnt vmcnt(16) lgkmcnt(7)\n\t" \
    "v_mfma_f32_32x32x16_bf16 v[0:15], v[56:59], a[28:31], v[0:15]\n\t" \
    "ds_read_b128 v[56:59], v64 offset:11264\n\t" \
    "global_load_dwordx4 a[92:95], v67, s[66:67] offset:-1024 sc1\n\t" \
    "s_waitcnt lgkmcnt(7)\n\t" \
    "v_mfma_f32_32x32x16_bf16 v[16:31], v[60:63], a[28:31], v[16:31]\n\t" \
    "ds_read_b128 v[60:63], v65 offset:11264\n\t" \
    "s_waitcnt vmcnt(16) lgkmcnt(7)\n\t" \
    "v_mfma_f32_32x32x16_bf16 v[0:15], v[32:35], a[32:35], v[0:15]\n\t" \
    "ds_read_b128 v[32:35], v64 offset:12288\n\t" \
    "global_load_dwordx4 a[96:99], v67, s[66:67] sc1\n\t" \
    "s_waitcnt lgkmcnt(7)\n\t" \
    "v_mfma_f32_32x32x16_bf16 v[16:31], v[36:39], a[32:35], v[16:31]\n\t" \
    "ds_read_b128 v[36:39], v65 offset:12288\n\t" \
    "s_waitcnt vmcnt(16) lgkmcnt(7)\n\t" \
    "v_mfma_f32_32x32x16_bf16 v[0:15], v[40:43], a[36:39], v[0:15]\n\t" \
    "ds_read_b128 v[40:43], v64 offset:13312\n\t" \
    "global_load_dwordx4 a[100:103], v67, s[66:67] offset:1024 sc1\n\t" \
    "s_waitcnt lgkmcnt(7)\n\t" \
    "v_mfma_f32_32x32x16_bf16 v[16:31], v[44:47], a[36:39], v[16:31]\n\t" \
    "ds_read_b128 v[44:47], v65 offset:13312\n\t" \
    "s_waitcnt vmcnt(15) lgkmcnt(7)\n\t" \
    "v_mfma_f32_32x32x16_bf16 v[0:15], v[48:51], a[40:43], v[0:15]\n\t" \
    "ds_read_b128 v[48:51], v64 offset:14336\n\t" \
    "global_load_dwordx4 a[104:107], v67, s[66:67] offset:2048 sc1\n\t" \
    "s_waitcnt lgkmcnt(7)\n\t" \
    "v_mfma_f32_32x32x16_bf16 v[16:31], v[52:55], a[40:43], v[16:31]\n\t" \
    "ds_read_b128 v[52:55], v65 offset:14336\n\t" \
    "s_waitcnt vmcnt(15) lgkmcnt(7)\n\t" \
    "v_mfma_f32_32x32x16_bf16 v[0:15], v[56:59], a[44:47], v[0:15]\n\t" \
    "ds_read_b128 v[56:59], v64 offset:15360\n\t" \
    "global_load_dwordx4 a[108:111], v67, s[66:67] offset:3072 sc1\n\t" \
    "global_load_dwordx4 v[68:71], v67, s[68:69] sc1\n\t" \
    "s_waitcnt lgkmcnt(7)\n\t" \
    "v_mfma_f32_32x32x16_bf16 v[16:31], v[60:63], a[44:47], v[16:31]\n\t" \
    "ds_read_b128 v[60:63], v65 offset:15360\n\t" \
    "s_waitcnt vmcnt(16) lgkmcnt(7)\n\t" \
    "v_mfma_f32_32x32x16_bf16 v[0:15], v[32:35], a[48:51], v[0:15]\n\t" \
    "s_barrier\n\t" \
    "v_mov_b32 v64, v66\n\t" \
    "v_xor_b32 v65, 0x1000, v66\n\t" \
    "ds_read_b128 v[32:35], v64 offset:0\n\t" \
    "s_waitcnt lgkmcnt(7)\n\t" \
    "v_mfma_f32_32x32x16_bf16 v[16:31], v[36:39], a[48:51], v[16:31]\n\t" \
    "ds_read_b128 v[36:39], v65 offset:0\n\t" \
    "s_waitcnt vmcnt(15) lgkmcnt(7)\n\t" \
    "v_mfma_f32_32x32x16_bf16 v[0:15], v[40:43], a[52:55], v[0:15]\n\t" \
    "ds_read_b128 v[40:43], v64 offset:1024\n\t" \
    "s_waitcnt lgkmcnt(7)\n\t" \
    "v_mfma_f32_32x32x16_bf16 v[16:31], v[44:47], a[52:55], v[16:31]\n\t" \
    "ds_read_b128 v[44:47], v65 offset:1024\n\t" \
    "s_waitcnt vmcnt(14) lgkmcnt(7)\n\t" \
    "v_mfma_f32_32x32x16_bf16 v[0:15], v[48:51], a[56:59], v[0:15]\n\t" \
    "ds_read_b128 v[48:51], v64 offset:2048\n\t" \
    "s_waitcnt lgkmcnt(7)\n\t" \
    "v_mfma_f32_32x32x16_bf16 v[16:31], v[52:55], a[56:59], v[16:31]\n\t" \
    "ds_read_b128 v[52:55], v65 offset:2048\n\t" \
    "s_waitcnt vmcnt(13) lgkmcnt(7)\n\t" \
    "v_mfma_f32_32x32x16_bf16 v[0:15], v[56:59], a[60:63], v[0:15]\n\t" \
    "ds_read_b128 v[56:59], v64 offset:3072\n\t" \
    "s_waitcnt lgkmcnt(7)\n\t" \
    "v_mfma_f32_32x32x16_bf16 v[16:31], v[60:63], a[60:63], v[16:31]\n\t" \
    "ds_read_b128 v[60:63], v65 offset:3072\n\t" \
    "s_waitcnt vmcnt(12) lgkmcnt(7)\n\t" \
    "v_mfma_f32_32x32x16_bf16 v[0:15], v[32:35], a[64:67], v[0:15]\n\t" \
    "s_waitcnt lgkmcnt(6)\n\t" \
    "v_mfma_f32_32x32x16_bf16 v[16:31], v[36:39], a[64:67], v[16:31]\n\t" \
    "s_waitcnt vmcnt(11) lgkmcnt(5)\n\t" \
    "v_mfma_f32_32x32x16_bf16 v[0:15], v[40:43], a[68:71], v[0:15]\n\t" \
    "s_waitcnt lgkmcnt(4)\n\t" \
    "v_mfma_f32_32x32x16_bf16 v[16:31], v[44:47], a[68:71], v[16:31]\n\t" \
    "s_waitcnt vmcnt(10) lgkmcnt(3)\n\t" \
    "v_mfma_f32_32x32x16_bf16 v[0:15], v[48:51], a[72:75], v[0:15]\n\t" \
    "s_waitcnt lgkmcnt(2)\n\t" \
    "v_mfma_f32_32x32x16_bf16 v[16:31], v[52:55], a[72:75], v[16:31]\n\t" \
    "s_waitcnt vmcnt(9) lgkmcnt(1)\n\t" \
    "v_mfma_f32_32x32x16_bf16 v[0:15], v[56:59], a[76:79], v[0:15]\n\t" \
    "s_waitcnt lgkmcnt(0)\n\t" \
    "v_mfma_f32_32x32x16_bf16 v[16:31], v[60:63], a[76:79], v[16:31]\n\t" \
    "s_waitcnt vmcnt(8)\n\t" \
    "v_mfma_f32_32x32x16_bf16 v[0:15], a[128:131], a[80:83], v[0:15]\n\t" \
    "v_mfma_f32_32x32x16_bf16 v[16:31], a[132:135], a[80:83], v[16:31]\n\t" \
    "s_xor_b32 s44, s43, 1\n\t" \
    "s_mul_i32 s44, s44, 6144\n\t" \
    "v_add_u32 v32, s44, v67\n\t" \
    "s_xor_b32 s44, s43, 2\n\t" \
    "s_mul_i32 s44, s44, 6144\n\t" \
    "s_add_u32 s44, s44, 2048\n\t" \
    "v_add_u32 v33, s44, v67\n\t" \
    "s_xor_b32 s44, s43, 3\n\t" \
    "s_mul_i32 s44, s44, 6144\n\t" \
    "s_add_u32 s44, s44, 4096\n\t" \
    "v_add_u32 v34, s44, v67\n\t" \
    "s_mul_i32 s44, s43, 6144\n\t" \
    "v_add_u32 v35, s44, v67\n\t" \
    "v_and_b32 v36, 0x1f0, v67\n\t" \
    "v_lshlrev_b32 v36, 6, v36\n\t" \
    "v_lshrrev_b32 v37, 9, v67\n\t" \
    "v_lshl_or_b32 v36, v37, 4, v36\n\t" \
    "s_waitcnt vmcnt(7)\n\t" \
    "v_mfma_f32_32x32x16_bf16 v[0:15], a[136:139], a[84:87], v[0:15]\n\t" \
    "v_mfma_f32_32x32x16_bf16 v[16:31], a[140:143], a[84:87], v[16:31]\n\t" \
    "s_waitcnt vmcnt(6)\n\t" \
    "v_mfma_f32_32x32x16_bf16 v[0:15], a[144:147], a[88:91], v[0:15]\n\t" \
    "v_mfma_f32_32x32x16_bf16 v[16:31], a[148:151], a[88:91], v[16:31]\n\t" \
    "s_waitcnt vmcnt(5)\n\t" \
    "v_mfma_f32_32x32x16_bf16 v[0:15], a[152:155], a[92:95], v[0:15]\n\t" \
    "v_mfma_f32_32x32x16_bf16 v[16:31], a[156:159], a[92:95], v[16:31]\n\t" \
    "s_waitcnt vmcnt(4)\n\t" \
    "v_mfma_f32_32x32x16_bf16 v[0:15], a[160:163], a[96:99], v[0:15]\n\t" \
    "v_mfma_f32_32x32x16_bf16 v[16:31], a[164:167], a[96:99], v[16:31]\n\t" \
    "s_waitcnt vmcnt(3)\n\t" \
    "v_mfma_f32_32x32x16_bf16 v[0:15], a[168:171], a[100:103], v[0:15]\n\t" \
    "v_mfma_f32_32x32x16_bf16 v[16:31], a[172:175], a[100:103], v[16:31]\n\t" \
    "s_waitcnt vmcnt(2)\n\t" \
    "v_mfma_f32_32x32x16_bf16 v[0:15], a[176:179], a[104:107], v[0:15]\n\t" \
    "v_mfma_f32_32x32x16_bf16 v[16:31], a[180:183], a[104:107], v[16:31]\n\t" \
    "s_waitcnt vmcnt(1)\n\t" \
    "v_mfma_f32_32x32x16_bf16 v[0:15], a[120:123], a[108:111], v[0:15]\n\t" \
    "v_mfma_f32_32x32x16_bf16 v[16:31], a[124:127], a[108:111], v[16:31]\n\t" \
    "s_waitcnt vmcnt(0)\n\t" \
    "v_lshlrev_b32 v40, 16, v68\n\t" \
    "v_and_b32 v41, 0xffff0000, v68\n\t" \
    "v_lshlrev_b32 v42, 16, v69\n\t" \
    "v_and_b32 v43, 0xffff0000, v69\n\t" \
    "v_lshlrev_b32 v44, 16, v70\n\t" \
    "v_and_b32 v45, 0xffff0000, v70\n\t" \
    "v_lshlrev_b32 v46, 16, v71\n\t" \
    "v_and_b32 v47, 0xffff0000, v71\n\t" \
    "s_nop 3\n\t" \
    "s_barrier\n\t" \
    "ds_write_b128 v32, v[8:11]\n\t" \
    "ds_write_b128 v32, v[12:15] offset:1024\n\t" \
    "ds_write_b128 v33, v[16:19]\n\t" \
    "ds_write_b128 v33, v[20:23] offset:1024\n\t" \
    "ds_write_b128 v34, v[24:27]\n\t" \
    "ds_write_b128 v34, v[28:31] offset:1024\n\t" \
    "v_add_f32 v0, v0, v40\n\t" \
    "v_add_f32 v1, v1, v41\n\t" \
    "v_add_f32 v2, v2, v42\n\t" \
    "v_add_f32 v3, v3, v43\n\t" \
    "v_add_f32 v4, v4, v44\n\t" \
    "v_add_f32 v5, v5, v45\n\t" \
    "v_add_f32 v6, v6, v46\n\t" \
    "v_add_f32 v7, v7, v47\n\t" \
    "s_waitcnt lgkmcnt(0)\n\t" \
    "s_barrier\n\t" \
    "ds_read_b128 v[40:43], v35 offset:0\n\t" \
    "ds_read_b128 v[44:47], v35 offset:1024\n\t" \
    "ds_read_b128 v[48:51], v35 offset:2048\n\t" \
    "ds_read_b128 v[52:55], v35 offset:3072\n\t" \
    "ds_read_b128 v[56:59], v35 offset:4096\n\t" \
    "ds_read_b128 v[60:63], v35 offset:5120\n\t" \
    "s_waitcnt lgkmcnt(4)\n\t" \
    "v_add_f32 v0, v0, v40\n\t" \
    "v_add_f32 v1, v1, v41\n\t" \
    "v_add_f32 v2, v2, v42\n\t" \
    "v_add_f32 v3, v3, v43\n\t" \
    "v_add_f32 v4, v4, v44\n\t" \
    "v_add_f32 v5, v5, v45\n\t" \
    "v_add_f32 v6, v6, v46\n\t" \
    "v_add_f32 v7, v7, v47\n\t" \
    "s_waitcnt lgkmcnt(2)\n\t" \
    "v_add_f32 v0, v0, v48\n\t" \
    "v_add_f32 v1, v1, v49\n\t" \
    "v_add_f32 v2, v2, v50\n\t" \
    "v_add_f32 v3, v3, v51\n\t" \
    "v_add_f32 v4, v4, v52\n\t" \
    "v_add_f32 v5, v5, v53\n\t" \
    "v_add_f32 v6, v6, v54\n\t" \
    "v_add_f32 v7, v7, v55\n\t" \
    "s_waitcnt lgkmcnt(0)\n\t" \
    "v_add_f32 v0, v0, v56\n\t" \
    "v_add_f32 v1, v1, v57\n\t" \
    "v_add_f32 v2, v2, v58\n\t" \
    "v_add_f32 v3, v3, v59\n\t" \
    "v_add_f32 v4, v4, v60\n\t" \
    "v_add_f32 v5, v5, v61\n\t" \
    "v_add_f32 v6, v6, v62\n\t" \
    "v_add_f32 v7, v7, v63\n\t" \
    "v_cvt_pk_bf16_f32 v16, v0, v1\n\t" \
    "v_cvt_pk_bf16_f32 v17, v2, v3\n\t" \
    "v_cvt_pk_bf16_f32 v18, v4, v5\n\t" \
    "v_cvt_pk_bf16_f32 v19, v6, v7\n\t" \
    "global_store_dwordx4 v36, v[16:19], s[70:71]\n\t" \
    "s_barrier\n\t" \
    "s_branch .Lend_%=\n\t" \
    ".Ldown_l_%=:\n\t" \
    "s_and_b32 s44, s43, 3\n\t" \
    "s_lshr_b32 s45, s44, 1\n\t" \
    "s_lshl_b32 s46, s44, 1\n\t" \
    "s_add_u32 s46, s46, s45\n\t" \
    "s_cmp_eq_u32 s44, 3\n\t" \
    "s_cselect_b32 s44, 32768, 0\n\t" \
    "s_lshl_b32 s45, s46, 14\n\t" \
    "s_add_u32 s45, s45, s44\n\t" \
    "v_add_u32 v64, s45, v62\n\t" \
    "v_xor_b32 v65, 0x4000, v64\n\t" \
    "s_lshl_b32 s45, s46, 12\n\t" \
    "s_add_u32 s45, s45, 98304\n\t" \
    "v_add_u32 v66, s45, v62\n\t" \
    "ds_read_b128 v[32:35], v64 offset:0\n\t" \
    "ds_read_b128 v[36:39], v65 offset:0\n\t" \
    "ds_read_b128 v[40:43], v64 offset:1024\n\t" \
    "ds_read_b128 v[44:47], v65 offset:1024\n\t" \
    "ds_read_b128 v[48:51], v64 offset:2048\n\t" \
    "ds_read_b128 v[52:55], v65 offset:2048\n\t" \
    "ds_read_b128 v[56:59], v64 offset:3072\n\t" \
    "ds_read_b128 v[60:63], v65 offset:3072\n\t" \
    "s_waitcnt vmcnt(13) lgkmcnt(7)\n\t" \
    "v_mfma_f32_32x32x16_bf16 v[0:15], v[32:35], a[0:3], 0\n\t" \
    "ds_read_b128 v[32:35], v64 offset:4096\n\t" \
    "global_load_dwordx4 a[56:59], v67, s[66:67] offset:-2048 sc1\n\t" \
    "s_waitcnt lgkmcnt(7)\n\t" \
    "v_mfma_f32_32x32x16_bf16 v[16:31], v[36:39], a[0:3], 0\n\t" \
    "ds_read_b128 v[36:39], v65 offset:4096\n\t" \
    "s_waitcnt vmcnt(13) lgkmcnt(7)\n\t" \
    "v_mfma_f32_32x32x16_bf16 v[0:15], v[40:43], a[4:7], v[0:15]\n\t" \
    "ds_read_b128 v[40:43], v64 offset:5120\n\t" \
    "global_load_dwordx4 a[60:63], v67, s[66:67] offset:-1024 sc1\n\t" \
    "s_waitcnt lgkmcnt(7)\n\t" \
    "v_mfma_f32_32x32x16_bf16 v[16:31], v[44:47], a[4:7], v[16:31]\n\t" \
    "ds_read_b128 v[44:47], v65 offset:5120\n\t" \
    "s_waitcnt vmcnt(13) lgkmcnt(7)\n\t" \
    "v_mfma_f32_32x32x16_bf16 v[0:15], v[48:51], a[8:11], v[0:15]\n\t" \
    "ds_read_b128 v[48:51], v64 offset:6144\n\t" \
    "global_load_dwordx4 a[64:67], v67, s[66:67] sc1\n\t" \
    "s_waitcnt lgkmcnt(7)\n\t" \
    "v_mfma_f32_32x32x16_bf16 v[16:31], v[52:55], a[8:11], v[16:31]\n\t" \
    "ds_read_b128 v[52:55], v65 offset:6144\n\t" \
    "s_waitcnt vmcnt(13) lgkmcnt(7)\n\t" \
    "v_mfma_f32_32x32x16_bf16 v[0:15], v[56:59], a[12:15], v[0:15]\n\t" \
    "ds_read_b128 v[56:59], v64 offset:7168\n\t" \
    "global_load_dwordx4 a[68:71], v67, s[66:67] offset:1024 sc1\n\t" \
    "s_waitcnt lgkmcnt(7)\n\t" \
    "v_mfma_f32_32x32x16_bf16 v[16:31], v[60:63], a[12:15], v[16:31]\n\t" \
    "ds_read_b128 v[60:63], v65 offset:7168\n\t" \
    "s_waitcnt vmcnt(13) lgkmcnt(7)\n\t" \
    "v_mfma_f32_32x32x16_bf16 v[0:15], v[32:35], a[16:19], v[0:15]\n\t" \
    "ds_read_b128 v[32:35], v64 offset:8192\n\t" \
    "global_load_dwordx4 a[72:75], v67, s[66:67] offset:2048 sc1\n\t" \
    "s_waitcnt lgkmcnt(7)\n\t" \
    "v_mfma_f32_32x32x16_bf16 v[16:31], v[36:39], a[16:19], v[16:31]\n\t" \
    "ds_read_b128 v[36:39], v65 offset:8192\n\t" \
    "s_waitcnt vmcnt(13) lgkmcnt(7)\n\t" \
    "v_mfma_f32_32x32x16_bf16 v[0:15], v[40:43], a[20:23], v[0:15]\n\t" \
    "ds_read_b128 v[40:43], v64 offset:9216\n\t" \
    "global_load_dwordx4 a[76:79], v67, s[66:67] offset:3072 sc1\n\t" \
    "s_waitcnt lgkmcnt(7)\n\t" \
    "v_mfma_f32_32x32x16_bf16 v[16:31], v[44:47], a[20:23], v[16:31]\n\t" \
    "ds_read_b128 v[44:47], v65 offset:9216\n\t" \
    "s_waitcnt vmcnt(13) lgkmcnt(7)\n\t" \
    "v_mfma_f32_32x32x16_bf16 v[0:15], v[48:51], a[24:27], v[0:15]\n\t" \
    "ds_read_b128 v[48:51], v64 offset:10240\n\t" \
    "s_add_u32 s66, s66, 8192\n\t" \
    "s_addc_u32 s67, s67, 0\n\t" \
    "global_load_dwordx4 a[80:83], v67, s[66:67] offset:-4096 sc1\n\t" \
    "s_waitcnt lgkmcnt(7)\n\t" \
    "v_mfma_f32_32x32x16_bf16 v[16:31], v[52:55], a[24:27], v[16:31]\n\t" \
    "ds_read_b128 v[52:55], v65 offset:10240\n\t" \
    "s_waitcnt vmcnt(13) lgkmcnt(7)\n\t" \
    "v_mfma_f32_32x32x16_bf16 v[0:15], v[56:59], a[28:31], v[0:15]\n\t" \
    "ds_read_b128 v[56:59], v64 offset:11264\n\t" \
    "global_load_dwordx4 a[84:87], v67, s[66:67] offset:-3072 sc1\n\t" \
    "s_waitcnt lgkmcnt(7)\n\t" \
    "v_mfma_f32_32x32x16_bf16 v[16:31], v[60:63], a[28:31], v[16:31]\n\t" \
    "ds_read_b128 v[60:63], v65 offset:11264\n\t" \
    "s_waitcnt vmcnt(13) lgkmcnt(7)\n\t" \
    "v_mfma_f32_32x32x16_bf16 v[0:15], v[32:35], a[32:35], v[0:15]\n\t" \
    "ds_read_b128 v[32:35], v64 offset:12288\n\t" \
    "global_load_dwordx4 a[88:91], v67, s[66:67] offset:-2048 sc1\n\t" \
    "s_waitcnt lgkmcnt(7)\n\t" \
    "v_mfma_f32_32x32x16_bf16 v[16:31], v[36:39], a[32:35], v[16:31]\n\t" \
    "ds_read_b128 v[36:39], v65 offset:12288\n\t" \
    "s_waitcnt vmcnt(13) lgkmcnt(7)\n\t" \
    "v_mfma_f32_32x32x16_bf16 v[0:15], v[40:43], a[36:39], v[0:15]\n\t" \
    "ds_read_b128 v[40:43], v64 offset:13312\n\t" \
    "global_load_dwordx4 a[92:95], v67, s[66:67] offset:-1024 sc1\n\t" \
    "s_waitcnt lgkmcnt(7)\n\t" \
    "v_mfma_f32_32x32x16_bf16 v[16:31], v[44:47], a[36:39], v[16:31]\n\t" \
    "ds_read_b128 v[44:47], v65 offset:13312\n\t" \
    "s_waitcnt vmcnt(13) lgkmcnt(7)\n\t" \
    "v_mfma_f32_32x32x16_bf16 v[0:15], v[48:51], a[40:43], v[0:15]\n\t" \
    "ds_read_b128 v[48:51], v64 offset:14336\n\t" \
    "global_load_dwordx4 a[96:99], v67, s[66:67] sc1\n\t" \
    "s_waitcnt lgkmcnt(7)\n\t" \
    "v_mfma_f32_32x32x16_bf16 v[16:31], v[52:55], a[40:43], v[16:31]\n\t" \
    "ds_read_b128 v[52:55], v65 offset:14336\n\t" \
    "s_waitcnt vmcnt(13) lgkmcnt(7)\n\t" \
    "v_mfma_f32_32x32x16_bf16 v[0:15], v[56:59], a[44:47], v[0:15]\n\t" \
    "ds_read_b128 v[56:59], v64 offset:15360\n\t" \
    "global_load_dwordx4 a[100:103], v67, s[66:67] offset:1024 sc1\n\t" \
    "s_waitcnt lgkmcnt(7)\n\t" \
    "v_mfma_f32_32x32x16_bf16 v[16:31], v[60:63], a[44:47], v[16:31]\n\t" \
    "ds_read_b128 v[60:63], v65 offset:15360\n\t" \
    "s_waitcnt vmcnt(13) lgkmcnt(7)\n\t" \
    "v_mfma_f32_32x32x16_bf16 v[0:15], v[32:35], a[48:51], v[0:15]\n\t" \
    "v_mov_b32 v64, v66\n\t" \
    "v_xor_b32 v65, 0x1000, v66\n\t" \
    "ds_read_b128 v[32:35], v64 offset:0\n\t" \
    "global_load_dwordx4 a[104:107], v67, s[66:67] offset:2048 sc1\n\t" \
    "s_waitcnt lgkmcnt(7)\n\t" \
    "v_mfma_f32_32x32x16_bf16 v[16:31], v[36:39], a[48:51], v[16:31]\n\t" \
    "ds_read_b128 v[36:39], v65 offset:0\n\t" \
    "s_waitcnt vmcnt(13) lgkmcnt(7)\n\t" \
    "v_mfma_f32_32x32x16_bf16 v[0:15], v[40:43], a[52:55], v[0:15]\n\t" \
    "ds_read_b128 v[40:43], v64 offset:1024\n\t" \
    "global_load_dwordx4 a[108:111], v67, s[66:67] offset:3072 sc1\n\t" \
    "global_load_dwordx4 v[68:71], v67, s[68:69] sc1\n\t" \
    "s_waitcnt lgkmcnt(7)\n\t" \
    "v_mfma_f32_32x32x16_bf16 v[16:31], v[44:47], a[52:55], v[16:31]\n\t" \
    "ds_read_b128 v[44:47], v65 offset:1024\n\t" \
    "s_waitcnt vmcnt(14) lgkmcnt(7)\n\t" \
    "v_mfma_f32_32x32x16_bf16 v[0:15], v[48:51], a[56:59], v[0:15]\n\t" \
    "ds_read_b128 v[48:51], v64 offset:2048\n\t" \
    "s_waitcnt lgkmcnt(7)\n\t" \
    "v_mfma_f32_32x32x16_bf16 v[16:31], v[52:55], a[56:59], v[16:31]\n\t" \
    "ds_read_b128 v[52:55], v65 offset:2048\n\t" \
    "s_waitcnt vmcnt(13) lgkmcnt(7)\n\t" \
    "v_mfma_f32_32x32x16_bf16 v[0:15], v[56:59], a[60:63], v[0:15]\n\t" \
    "ds_read_b128 v[56:59], v64 offset:3072\n\t" \
    "s_waitcnt lgkmcnt(7)\n\t" \
    "v_mfma_f32_32x32x16_bf16 v[16:31], v[60:63], a[60:63], v[16:31]\n\t" \
    "ds_read_b128 v[60:63], v65 offset:3072\n\t" \
    "s_barrier\n\t" \
    "s_waitcnt vmcnt(12) lgkmcnt(7)\n\t" \
    "v_mfma_f32_32x32x16_bf16 v[0:15], v[32:35], a[64:67], v[0:15]\n\t" \
    "s_waitcnt lgkmcnt(6)\n\t" \
    "v_mfma_f32_32x32x16_bf16 v[16:31], v[36:39], a[64:67], v[16:31]\n\t" \
    "s_waitcnt vmcnt(11) lgkmcnt(5)\n\t" \
    "v_mfma_f32_32x32x16_bf16 v[0:15], v[40:43], a[68:71], v[0:15]\n\t" \
    "s_waitcnt lgkmcnt(4)\n\t" \
    "v_mfma_f32_32x32x16_bf16 v[16:31], v[44:47], a[68:71], v[16:31]\n\t" \
    "s_waitcnt vmcnt(10) lgkmcnt(3)\n\t" \
    "v_mfma_f32_32x32x16_bf16 v[0:15], v[48:51], a[72:75], v[0:15]\n\t" \
    "s_waitcnt lgkmcnt(2)\n\t" \
    "v_mfma_f32_32x32x16_bf16 v[16:31], v[52:55], a[72:75], v[16:31]\n\t" \
    "s_waitcnt vmcnt(9) lgkmcnt(1)\n\t" \
    "v_mfma_f32_32x32x16_bf16 v[0:15], v[56:59], a[76:79], v[0:15]\n\t" \
    "s_waitcnt lgkmcnt(0)\n\t" \
    "v_mfma_f32_32x32x16_bf16 v[16:31], v[60:63], a[76:79], v[16:31]\n\t" \
    "s_waitcnt vmcnt(8)\n\t" \
    "v_mfma_f32_32x32x16_bf16 v[0:15], a[128:131], a[80:83], v[0:15]\n\t" \
    "v_mfma_f32_32x32x16_bf16 v[16:31], a[132:135], a[80:83], v[16:31]\n\t" \
    "s_xor_b32 s44, s43, 1\n\t" \
    "s_mul_i32 s44, s44, 6144\n\t" \
    "v_add_u32 v32, s44, v67\n\t" \
    "s_xor_b32 s44, s43, 2\n\t" \
    "s_mul_i32 s44, s44, 6144\n\t" \
    "s_add_u32 s44, s44, 2048\n\t" \
    "v_add_u32 v33, s44, v67\n\t" \
    "s_xor_b32 s44, s43, 3\n\t" \
    "s_mul_i32 s44, s44, 6144\n\t" \
    "s_add_u32 s44, s44, 4096\n\t" \
    "v_add_u32 v34, s44, v67\n\t" \
    "s_mul_i32 s44, s43, 6144\n\t" \
    "v_add_u32 v35, s44, v67\n\t" \
    "v_and_b32 v36, 0x1f0, v67\n\t" \
    "v_lshlrev_b32 v36, 6, v36\n\t" \
    "v_lshrrev_b32 v37, 9, v67\n\t" \
    "v_lshl_or_b32 v36, v37, 4, v36\n\t" \
    "s_waitcnt vmcnt(7)\n\t" \
    "v_mfma_f32_32x32x16_bf16 v[0:15], a[136:139], a[84:87], v[0:15]\n\t" \
    "v_mfma_f32_32x32x16_bf16 v[16:31], a[140:143], a[84:87], v[16:31]\n\t" \
    "s_waitcnt vmcnt(6)\n\t" \
    "v_mfma_f32_32x32x16_bf16 v[0:15], a[144:147], a[88:91], v[0:15]\n\t" \
    "v_mfma_f32_32x32x16_bf16 v[16:31], a[148:151], a[88:91], v[16:31]\n\t" \
    "s_waitcnt vmcnt(5)\n\t" \
    "v_mfma_f32_32x32x16_bf16 v[0:15], a[152:155], a[92:95], v[0:15]\n\t" \
    "v_mfma_f32_32x32x16_bf16 v[16:31], a[156:159], a[92:95], v[16:31]\n\t" \
    "s_barrier\n\t" \
    "s_waitcnt vmcnt(4)\n\t" \
    "v_mfma_f32_32x32x16_bf16 v[0:15], a[160:163], a[96:99], v[0:15]\n\t" \
    "v_mfma_f32_32x32x16_bf16 v[16:31], a[164:167], a[96:99], v[16:31]\n\t" \
    "s_waitcnt vmcnt(3)\n\t" \
    "v_mfma_f32_32x32x16_bf16 v[0:15], a[168:171], a[100:103], v[0:15]\n\t" \
    "v_mfma_f32_32x32x16_bf16 v[16:31], a[172:175], a[100:103], v[16:31]\n\t" \
    "s_waitcnt vmcnt(2)\n\t" \
    "v_mfma_f32_32x32x16_bf16 v[0:15], a[176:179], a[104:107], v[0:15]\n\t" \
    "v_mfma_f32_32x32x16_bf16 v[16:31], a[180:183], a[104:107], v[16:31]\n\t" \
    "s_waitcnt vmcnt(1)\n\t" \
    "v_mfma_f32_32x32x16_bf16 v[0:15], a[120:123], a[108:111], v[0:15]\n\t" \
    "v_mfma_f32_32x32x16_bf16 v[16:31], a[124:127], a[108:111], v[16:31]\n\t" \
    "s_waitcnt vmcnt(0)\n\t" \
    "v_lshlrev_b32 v40, 16, v68\n\t" \
    "v_and_b32 v41, 0xffff0000, v68\n\t" \
    "v_lshlrev_b32 v42, 16, v69\n\t" \
    "v_and_b32 v43, 0xffff0000, v69\n\t" \
    "v_lshlrev_b32 v44, 16, v70\n\t" \
    "v_and_b32 v45, 0xffff0000, v70\n\t" \
    "v_lshlrev_b32 v46, 16, v71\n\t" \
    "v_and_b32 v47, 0xffff0000, v71\n\t" \
    "s_nop 3\n\t" \
    "ds_write_b128 v32, v[8:11]\n\t" \
    "ds_write_b128 v32, v[12:15] offset:1024\n\t" \
    "ds_write_b128 v33, v[16:19]\n\t" \
    "ds_write_b128 v33, v[20:23] offset:1024\n\t" \
    "ds_write_b128 v34, v[24:27]\n\t" \
    "ds_write_b128 v34, v[28:31] offset:1024\n\t" \
    "v_add_f32 v0, v0, v40\n\t" \
    "v_add_f32 v1, v1, v41\n\t" \
    "v_add_f32 v2, v2, v42\n\t" \
    "v_add_f32 v3, v3, v43\n\t" \
    "v_add_f32 v4, v4, v44\n\t" \
    "v_add_f32 v5, v5, v45\n\t" \
    "v_add_f32 v6, v6, v46\n\t" \
    "v_add_f32 v7, v7, v47\n\t" \
    "s_waitcnt lgkmcnt(0)\n\t" \
    "s_barrier\n\t" \
    "ds_read_b128 v[40:43], v35 offset:0\n\t" \
    "ds_read_b128 v[44:47], v35 offset:1024\n\t" \
    "ds_read_b128 v[48:51], v35 offset:2048\n\t" \
    "ds_read_b128 v[52:55], v35 offset:3072\n\t" \
    "ds_read_b128 v[56:59], v35 offset:4096\n\t" \
    "ds_read_b128 v[60:63], v35 offset:5120\n\t" \
    "s_waitcnt lgkmcnt(4)\n\t" \
    "v_add_f32 v0, v0, v40\n\t" \
    "v_add_f32 v1, v1, v41\n\t" \
    "v_add_f32 v2, v2, v42\n\t" \
    "v_add_f32 v3, v3, v43\n\t" \
    "v_add_f32 v4, v4, v44\n\t" \
    "v_add_f32 v5, v5, v45\n\t" \
    "v_add_f32 v6, v6, v46\n\t" \
    "v_add_f32 v7, v7, v47\n\t" \
    "s_waitcnt lgkmcnt(2)\n\t" \
    "v_add_f32 v0, v0, v48\n\t" \
    "v_add_f32 v1, v1, v49\n\t" \
    "v_add_f32 v2, v2, v50\n\t" \
    "v_add_f32 v3, v3, v51\n\t" \
    "v_add_f32 v4, v4, v52\n\t" \
    "v_add_f32 v5, v5, v53\n\t" \
    "v_add_f32 v6, v6, v54\n\t" \
    "v_add_f32 v7, v7, v55\n\t" \
    "s_waitcnt lgkmcnt(0)\n\t" \
    "v_add_f32 v0, v0, v56\n\t" \
    "v_add_f32 v1, v1, v57\n\t" \
    "v_add_f32 v2, v2, v58\n\t" \
    "v_add_f32 v3, v3, v59\n\t" \
    "v_add_f32 v4, v4, v60\n\t" \
    "v_add_f32 v5, v5, v61\n\t" \
    "v_add_f32 v6, v6, v62\n\t" \
    "v_add_f32 v7, v7, v63\n\t" \
    "v_cvt_pk_bf16_f32 v16, v0, v1\n\t" \
    "v_cvt_pk_bf16_f32 v17, v2, v3\n\t" \
    "v_cvt_pk_bf16_f32 v18, v4, v5\n\t" \
    "v_cvt_pk_bf16_f32 v19, v6, v7\n\t" \
    "global_store_dwordx4 v36, v[16:19], s[70:71]\n\t" \
    ".Lend_%=:\n\t" \
    ""

#define K2_OUT_TAIL \
  { \
    const char* pw0 = ws + WS_WT + (size_t)(7 * sl + (w < 7 ? w : 0)) * 32768 + 4096; \
    asm volatile(K2_W0_ASM : "+{s[100:101]}"(pw0) : "{s43}"(w), "{v67}"(l16) \
                 : "memory", "scc", "a0", "a1", "a2", "a3", "a4", "a5", "a6", "a7", "a8", "a9", K23_A8(1), K23_A8(2), K23_A8(3), K23_A8(4), K23_A8(5), K23_A8(6)); \
  } \
  { \
    u32 ta, tb; \
    asm volatile("v_lshrrev_b32 %1, 2, %3\n\tv_bfe_i32 %2, %3, 8, 1\n\tv_and_b32 %1, 0xbc, %1\n\tv_and_b32 %2, 0x440, %2\n\tv_add3_u32 %1, %1, %2, %4\n\t" \
                 "v_permlane32_swap_b32 v0, v8\n\tv_permlane32_swap_b32 v1, v9\n\tv_permlane32_swap_b32 v2, v10\n\tv_permlane32_swap_b32 v3, v11\n\t" \
                 "v_add_f32 v0, v0, v8\n\tv_add_f32 v1, v1, v9\n\tv_add_f32 v2, v2, v10\n\tv_add_f32 v3, v3, v11\n\t" \
                 "ds_write2_b32 %1, v0, v1 offset1:68\n\tds_write2_b32 %1, v2, v3 offset0:136 offset1:204\n\t" \
                 "v_permlane32_swap_b32 v4, v12\n\tv_permlane32_swap_b32 v5, v13\n\tv_permlane32_swap_b32 v6, v14\n\tv_permlane32_swap_b32 v7, v15\n\t" \
                 "v_add_f32 v4, v4, v12\n\tv_add_f32 v5, v5, v13\n\tv_add_f32 v6, v6, v14\n\tv_add_f32 v7, v7, v15\n\t" \
                 "ds_write2_b32 %1, v4, v5 offset0:16 offset1:84\n\tds_write2_b32 %1, v6, v7 offset0:152 offset1:220" \
                 : "+{v[0:15]}"(acc), "=&v"(ta), "=&v"(tb) : "{v67}"(l16), "s"(w * 6144) : "memory"); \
  }
__global__ __launch_bounds__(512, 2) __attribute__((amdgpu_num_vgpr(72)))
void k2_rest(const u16* __restrict__ x, const u16* __restrict__ wg, const u16* __restrict__ wu, const u16* __restrict__ wd, char* __restrict__ ws,
             u16* __restrict__ out) {
  extern __shared__ __attribute__((aligned(16))) char smem[];
  AS3 char* lds = (AS3 char*)smem;
  PROF_BEGIN
  u32 junk;
  asm volatile("s_getpc_b64 s[4:5]\n"
               ".Lk2_here:\n\t"
               "s_bfe_u32 s6, %2, " CPFSEL2 "\n\tv_lshlrev_b32 %0, 7, %1\n\ts_mov_b32 s7, .Lk2_last-.Lk2_here\n\t"
               "s_cmp_eq_u32 s6, " CPF2A "\n\ts_cbranch_scc1 .Lk2_cpf\n\t"
               "s_branch .Lk2_nocpf\n"
               ".Lk2_cpf:\n\t"
               "v_cmp_gt_u32 vcc, s7, %0\n\ts_and_saveexec_b64 s[6:7], vcc\n\ts_cbranch_execz .Lk2_nocpf0\n\t"
               "global_load_dword %0, %0, s[4:5]\n"
               ".Lk2_nocpf0:\n\ts_mov_b64 exec, s[6:7]\n"
               ".Lk2_nocpf:"
               : "=&v"(junk) : "v"(threadIdx.x), "s"(blockIdx.x) : "memory", "scc", "vcc", "s4", "s5", "s6", "s7");
  const int l = threadIdx.x & 63, w = wave_id();
  const int b = blockIdx.x, tt = 4 * ((b & 7) >> 1) + ((b >> 3) & 3), sl = 8 * (b & 1) + (b >> 5), g = 16 * tt + sl;
  const int r = l & 31, h = l >> 5;
  u32 l16 = l * 16;
  u32x2 km2;
  u32 km, qm;
  u32x4 qf;
  asm volatile(K23_ASM1
               : "={v[22:23]}"(km2), "={v25}"(qm), "={v[48:51]}"(qf)
               : "{v26}"(l * 8), "{v27}"((l & 15) * 4), "{v67}"(l16), "{s[40:41]}"(ws + WS_K), "{s43}"(w), "{s[74:75]}"(ws + WS_KM),
                 "{s[76:77]}"(ws + WS_QM + g * 64), "{s[78:79]}"(ws + WS_Q + (size_t)(g * 8 + w) * 1024)
               : "memory", "scc", "s44", "s45", "s46");
  const int job = b < 224 ? b : b - 224, cgp = job % 28, kc = job / 28;
  const int blk = (l >> 4) & 1, q = (l & 15) >> 2, p = l & 3;
  const size_t ro = (size_t)(64 * kc + 8 * w) * 3584 + cgp * 128;
  const char* sg = (const char*)wg + ro;
  const char* su = (const char*)wu + ro;
  const char* sd = (const char*)wd + (size_t)(64 * cgp + 8 * w) * 1024 + kc * 128;
  const u32 vs = (l >> 3) * 3584 + (l & 7) * 16, vd = (l >> 3) * 1024 + (l & 7) * 16;
  const u32 at = K2_STG + (w >> 2) * 4096 + (l >> 4) * 1024 + q * 128 + (w & 3) * 32 + (p & 1) * 16 + (p >> 1) * 8;
  const u32 ad = K2_STG + 16384 + (16 * (w >> 1) + 4 * h + q) * 128 + (w & 1) * 64 + blk * 32 + (p & 1) * 16 + (p >> 1) * 8;
  char* dt = ws + WS_WT + ((size_t)(4 * cgp + (w & 3)) * 32 + 4 * kc + 2 * (w >> 2)) * 1024;
  char* dd = ws + WS_WD + ((size_t)(2 * kc + (w & 1)) * 112 + 4 * cgp + (w >> 1)) * 1024;
  const char* xs = (const char*)x + (size_t)(8 * g) * 1024 + w * 128;
  const char* wo = ws + WS_WO + (size_t)w * 65536;
  u32 va = K2_V + h * 288 + (r < 17 ? r : 17) * 16;
  const int jl = l < 24 ? min((7 * sl >> 2) + (l >> 3), (7 * sl + 6) >> 2) + 28 * (l & 7) : min(l - 24, 27) + 28 * (sl & 7);
  const u32 joff = jl * 128;
  char* const flg = ws + WS_FLG;
  asm volatile("s_waitcnt vmcnt(9)" : "+v"(km2), "+v"(qm)
               , "+v"(junk)
               : "v"(vs), "v"(vd), "v"(at), "v"(ad), "v"(va), "s"(sg), "s"(su), "s"(sd), "s"(dt), "s"(dd), "s"(xs), "s"(wo), "s"(out) : "memory");
  km = km2.x > km2.y ? km2.x : km2.y;
  km = u32_max<0x128, 0x124, 0x122, 0x121>(km);
  { const auto t = __builtin_amdgcn_permlane16_swap(km, km, false, false); km = t[0] > t[1] ? t[0] : t[1]; }
  { const auto t = __builtin_amdgcn_permlane32_swap(km, km, false, false); km = t[0] > t[1] ? t[0] : t[1]; }
  qm = u32_max<0x128, 0x124, 0x122, 0x121>(qm);
  const bool safe = __builtin_amdgcn_readfirstlane(asf(qm) * asf(km) * 1.05f <= SAFE_LOG2 * SAFE_LOG2);
  f32x16 acc;
  u32x4 xr;
  if (safe) {
    xr.x = vd; xr.y = at; xr.z = ad; xr.w = joff;
    u32 vs_ = vs;
    const char *wa = wo + 4096, *wl = wo + 6144;
    asm volatile(K23_ASM2
                 : "={v[0:15]}"(acc), "+{v[48:51]}"(qf), "+{v[68:71]}"(xr), "+{v65}"(vs_), "+{v66}"(va), "+{s[54:55]}"(wa), "+{s[62:63]}"(wl), "+{v67}"(l16)
                 : "{s[40:41]}"(ws + WS_K), "{s43}"(w), "{s[48:49]}"(sg), "{s[50:51]}"(su), "{s[52:53]}"(sd),
                   "{s[56:57]}"(dt), "{s[58:59]}"(dd), "{s[60:61]}"(xs), "{s[96:97]}"(flg), "{s[98:99]}"(flg + b * 128)
                 : "memory", "scc", "vcc", "s44", "s45", "s46", "s47", "v16", "v17", "v18",
                   "v19",
                   K23_V8(2), K23_V8(3), "v40", "v41", "v42", "v43",
                   "v44", "v45", "v46", "v47", "v52", "v53", "v54", "v55", "v56", "v57", "v58", "v59", "v60", "v61", "v62", "v63", "v64",
                   "a0", "a1", "a2", "a3", "a4", "a5", "a6", "a7", "a8", "a9", K23_A8(1), K23_A8(2), K23_A8(3), K23_A8(4), K23_A8(5), K23_A8(6),
                   K23_A8(7), K23_A8(8), K23_A8(9), K23_A8(10), K23_A8(11), K23_A8(12), K23_A8(13), K23_A8(14), K23_A8(15), K23_A8(16),
                   K23_A8(17), "a180", "a181", "a182", "a183");
    K2_OUT_TAIL
  } else {
    const char* kb = ws + WS_K;
    dma16x4((32 + 4 * w) * 1024, kb + (32 + 4 * w) * 1024, l16);
    dma16x4(K2_V + (32 + 5 * w) * 1024, kb + K2_V + (32 + 5 * w) * 1024, l16);
    dma16<0>(K2_V + (36 + 5 * w) * 1024, kb + K2_V + (36 + 5 * w) * 1024, l16);
    dma16<0>(K2_STG + w * 1024, sg, vs);
    dma16<0>(K2_STG + 8192 + w * 1024, su, vs);
    dma16<0>(K2_STG + 16384 + w * 1024, sd, vd);
    gld16<0>(xr, xs, vd);
    asm volatile("s_waitcnt vmcnt(0)\n\ts_barrier" : "+v"(qf), "+v"(xr)::"memory");
#pragma unroll
    for (int i = 0; i < 2; i++) st_wt(dt + i * 1024 + l16, cat(ldstr(lds + at + i * 8192), ldstr(lds + at + i * 8192 + 512)));
    st_wt(dd + l16, cat(ldstr(lds + ad), ldstr(lds + ad + 1024)));
    AS3 const char* kp = lds + l16;
    AS3 const char* vp = lds + va;
    float m = -3.0e38f;
#pragma unroll 1
    for (int t = 0; t < 64; t++) {
      const f32x16 z = {};
      const f32x16 s = mfma32(lds128(kp + t * 1024), qf, z);
#pragma unroll
      for (int j = 0; j < 16; j++) m = fmaxf(m, s[j]);
    }
    m = xor32_max(m);
    f32x16 o = {};
#pragma unroll 1
    for (int it = 0; it < 32; it++) {
      attn_tiles(kp, vp, qf, -m, o);
      kp += 2048; vp += 2304;
    }
    const float inv = __builtin_amdgcn_rcpf(lo32(o[8]));
    const u32x4 res = {pk(o[0] * inv, o[1] * inv), pk(o[2] * inv, o[3] * inv), pk(o[4] * inv, o[5] * inv), pk(o[6] * inv, o[7] * inv)};
    asm volatile("s_waitcnt vmcnt(0) lgkmcnt(0)\n\ts_barrier" ::: "memory");
    if (threadIdx.x == 0) __hip_atomic_store((u32*)(flg + b * 128), 1u, __ATOMIC_RELAXED, __HIP_MEMORY_SCOPE_AGENT);
    if (w == 7) {
      for (int spin = 0; spin < 200000; spin++) {
        const u32 f = __hip_atomic_load((u32*)(flg + joff), __ATOMIC_RELAXED, __HIP_MEMORY_SCOPE_AGENT);
        if (__all(f != 0)) break;
        __builtin_amdgcn_s_sleep(2);
      }
    }
    *(AS3 u32x4*)(lds + K2_OBUF + w * 1024 + ((r >> 3) * 2 + h) * 128 + (r & 7) * 16) = res;
    asm volatile("s_waitcnt lgkmcnt(0)\n\ts_barrier" ::: "memory");
    const int oidx = (l & 8) ? (int)0xEEEEEEEE : 0x44444444;
    f32x4 c[4] = {};
#pragma unroll 1
    for (int k8 = 0; k8 < 8; k8++) {
      const bf16x8 af = __builtin_bit_cast(bf16x8, lds128(lds + K2_OBUF + k8 * 1024 + l16));
#pragma unroll
      for (int c4 = 0; c4 < 4; c4++) {
        const char* src = wo + (k8 * 4 + c4) * 2048 + l16;
        const u32x4 b0 = *(const u32x4*)src, b1 = *(const u32x4*)(src + 1024);
        typedef u32 u32x8 __attribute__((ext_vector_type(8)));
        const u32x8 bb = __builtin_shufflevector(b0, b1, 0, 1, 2, 3, 4, 5, 6, 7);
        c[c4] = __builtin_amdgcn_smfmac_f32_16x16x64_bf16(af, __builtin_bit_cast(bf16x16, bb), c[c4], oidx, 0, 0);
      }
    }
#pragma unroll
    for (int j = 0; j < 16; j++) acc[j] = c[j >> 2][j & 3];
    K2_OUT_TAIL
  }
  const int lt = l16 >> 4;
  AS3 char* tr = lds + w * 6144;
  const u32 so = ((lt & 7) >> 1) * 1024 + (lt & 1) * 512 + (lt >> 3) * 16, son = ((lt & 7) >> 2) * 2048 + (lt & 3) * 256 + (lt >> 3) * 16;
  const char* const hb = ws + WS_H + (size_t)(g >> 2) * 32768 + w * 4096 + (g & 3) * 128;
  const char* const hnb = ws + WS_HN + (size_t)(g >> 2) * 32768 + w * 4096 + ((g & 3) >> 1) * 1024 + (g & 1) * 128;
  asm volatile("s_waitcnt lgkmcnt(0)" ::"v"(so), "v"(son), "s"(hb), "s"(hnb) : "memory");
  const f32x4 y0 = ldsf4(tr + (lt >> 3) * 272 + (lt & 7) * 32), y1 = ldsf4(tr + (lt >> 3) * 272 + (lt & 7) * 32 + 16);
  const u32x4 hv = {pk(blo(xr.x) + y0[0], bhi(xr.x) + y0[1]), pk(blo(xr.y) + y0[2], bhi(xr.y) + y0[3]),
                    pk(blo(xr.z) + y1[0], bhi(xr.z) + y1[1]), pk(blo(xr.w) + y1[2], bhi(xr.w) + y1[3])};
  float s1 = dot2(hv.x, ONES2, 0.f), s2 = dot2(hv.x, hv.x, 0.f);
  s1 = dot2(hv.y, ONES2, s1); s2 = dot2(hv.y, hv.y, s2);
  s1 = dot2(hv.z, ONES2, s1); s2 = dot2(hv.z, hv.z, s2);
  s1 = dot2(hv.w, ONES2, s1); s2 = dot2(hv.w, hv.w, s2);
  s1 = oct_add(s1); s2 = oct_add(s2);
  if ((lt & 7) == 0) { const f32x2 v = {s1, s2}; *(AS3 f32x2*)(lds + K2_STAT + ((lt >> 3) * 8 + w) * 8) = v; }
  asm volatile("s_waitcnt lgkmcnt(0)\n\ts_barrier" ::: "memory");
  PROF_AT(6)
  const f32x2 st = *(AS3 const f32x2*)(lds + K2_STAT + lt * 8);
  const float mu = oct_add(st.x) * (1.f / 512.f);
  const float rs = __builtin_amdgcn_rsqf(fmaxf(oct_add(st.y) * (1.f / 512.f) - mu * mu, 0.f) + 1e-5f);
  const u32x4 hn = {pk((blo(hv.x) - mu) * rs, (bhi(hv.x) - mu) * rs), pk((blo(hv.y) - mu) * rs, (bhi(hv.y) - mu) * rs),
                    pk((blo(hv.z) - mu) * rs, (bhi(hv.z) - mu) * rs), pk((blo(hv.w) - mu) * rs, (bhi(hv.w) - mu) * rs)};
  st_wt_s(hnb, son, hn);
  st_wt_s(hb, so, hv);
  PROF_WAVES
  {
    const int cg = sl & 7, q4 = w & 3, t32 = 4 * tt + (sl >> 3) + 2 * (w >> 2);
    const char* pw = ws + WS_WT + (size_t)(7 * sl + (w < 7 ? w : 0)) * 32768;
    const char* phn = ws + WS_HN + (size_t)tt * 131072;
    char* pfs = ws + WS_F + ((size_t)4 * tt * 112 + 7 * sl + w) * 1024;
    char* pgh = flg + 32768 + tt * 2048;
    char* pge = ws + WS_FLE + tt * 16384;
    char* poh = w == 7 ? pgh + sl * 128 : pge + sl * 1024 + w * 128;
    const char* pdl = ws + WS_WD + (size_t)2 * cg * 112 * 1024;
    const char* pd0 = pdl + ((q4 >> 1) * 112 + 28 * q4 + 20) * 1024;
    const char* pd1 = pdl + (((q4 >> 1) ^ 1) * 112 + 28 * q4 + 20) * 1024;
    const char* pfl = ws + WS_F + ((size_t)t32 * 112 + 28 * q4) * 1024;
    const char* ph = ws + WS_H + ((size_t)t32 * 32 + 4 * cg + q4) * 1024;
    char* po = (char*)out + (size_t)t32 * 32768 + cg * 128 + q4 * 32;
    asm volatile(KF_ASM
                 ".Lk2_last:"
                 : "+{s[40:41]}"(pw), "+{s[48:49]}"(phn), "+{s[50:51]}"(pfs), "+{s[60:61]}"(pdl), "+{s[62:63]}"(pd0), "+{s[64:65]}"(pd1),
                   "+{s[66:67]}"(pfl), "+{v67}"(l16)
                 : "{s42}"(tt & 3), "{s43}"(w), "{s[52:53]}"(pgh), "{s[54:55]}"(pge), "{s[56:57]}"(poh), "{s[68:69]}"(ph), "{s[70:71]}"(po)
                 : "memory", "scc", "vcc", "s44", "s45", "s46", "s47",
                   "v19",
                   "v0", "v1", "v2", "v3", "v4", "v5", "v6", "v7", "v8", "v9", "v10", "v11", "v12", "v13", "v14", "v15", "v16", "v17", "v18",
                   K23_V8(2), K23_V8(3), K23_V8(4), K23_V8(5), "v60", "v61", "v62", "v63", "v64", "v65", "v66", "v68", "v69", "v70", "v71",
                   "a0", "a1", "a2", "a3", "a4", "a5", "a6", "a7", "a8", "a9", K23_A8(1), K23_A8(2), K23_A8(3), K23_A8(4), K23_A8(5), K23_A8(6),
                   K23_A8(7), K23_A8(8), K23_A8(9), K23_A8(10), K23_A8(11), K23_A8(12), K23_A8(13), K23_A8(14), K23_A8(15), K23_A8(16),
                   K23_A8(17), "a180", "a181", "a182", "a183");
  }
  PROF_END(1)
}

extern "C" void kernel_launch(void* const* d_in, const int* in_sizes, int n_in, void* d_out, int out_size, void* d_ws, size_t ws_size,
                              hipStream_t stream) {
  const u16 *x = (const u16*)d_in[0], *wq = (const u16*)d_in[1], *wk = (const u16*)d_in[2], *wv = (const u16*)d_in[3],
            *wo = (const u16*)d_in[4], *wg = (const u16*)d_in[6], *wu = (const u16*)d_in[7], *wd = (const u16*)d_in[8];
  char* ws = (char*)d_ws;
  (void)hipFuncSetAttribute((const void*)k1_qkv, hipFuncAttributeMaxDynamicSharedMemorySize, K1_LDS);
  (void)hipFuncSetAttribute((const void*)k2_rest, hipFuncAttributeMaxDynamicSharedMemorySize, K2_LDS);
  k1_qkv<<<256, 512, K1_LDS, stream>>>(x, wq, wk, wv, wo, ws);
  k2_rest<<<256, 512, K2_LDS, stream>>>(x, wg, wu, wd, ws, (u16*)d_out);
}
```
